```python
import torch

def _softmax(x, axis=-1):
    return torch.softmax(x, dim=axis)
def _gelu(x):
    return torch.nn.functional.gelu(x, approximate="tanh")
def _silu(x):
    return torch.nn.functional.silu(x)
def _sigmoid(x):
    return torch.sigmoid(x)
def _layernorm(x, axis=-1, eps=1e-5):
    m = x.mean(dim=axis, keepdim=True)
    v = x.var(dim=axis, keepdim=True, unbiased=False)
    return (x - m) / torch.sqrt(v + eps)
def _scatter_add(tgt, idx, src, axis=0):
    return tgt.clone().index_add_(axis, idx, src)
def _segment_sum(src, seg, n):
    out = torch.zeros((n,) + src.shape[1:], dtype=src.dtype)
    return out.index_add_(0, seg, src)
def _pad(x, pw):
    flat = tuple(v for pair in reversed(pw) for v in pair)
    return torch.nn.functional.pad(x, flat)

kernel_name = 'gated_mix__spectral_fnet__swiglu_ffn__prompt'

def setup_inputs():

    _w = (lambda a: (lambda t: t.to(torch.bfloat16) if t.is_floating_point() else t)(torch.as_tensor(a)))
    def _A_setup():
        B, S, D = 1, 2048, 512
        ns = torch.arange(S)[:, None] * torch.arange(S)[None, :]
        nd = torch.arange(D)[:, None] * torch.arange(D)[None, :]
        Ws_re = torch.cos(-2.0 * torch.pi * ns / S).to(float)
        Ws_im = torch.sin(-2.0 * torch.pi * ns / S).to(float)
        Wd_re = torch.cos(-2.0 * torch.pi * nd / D).to(float)
        Wd_im = torch.sin(-2.0 * torch.pi * nd / D).to(float)
        return {
            "x": _w((lambda *s: torch.randn(*s, dtype=torch.bfloat16) * 0.05)(B, S, D)),
            "dft_seq_re": _w(Ws_re),
            "dft_seq_im": _w(Ws_im),
            "dft_hid_re": _w(Wd_re),
            "dft_hid_im": _w(Wd_im),
        }
    _A_in = _A_setup()

    def _B_setup():
        B, S, D, F = 1, 2048, 512, 1792
        return {
            "x": _w((lambda *s: torch.randn(*s, dtype=torch.bfloat16) * 0.05)(B, S, D)),
            "w_gate": _w((lambda *s: torch.randn(*s, dtype=torch.bfloat16) * 0.8)(D, F)),
            "w_up": _w((lambda *s: torch.randn(*s, dtype=torch.bfloat16) * 0.8)(D, F)),
            "w_down": _w((lambda *s: torch.randn(*s, dtype=torch.bfloat16) * 0.8)(F, D)),
        }
    _B_in = _B_setup()

    return {
        "A_x": _A_in["x"],
        "A_dft_seq_re": _A_in["dft_seq_re"],
        "A_dft_seq_im": _A_in["dft_seq_im"],
        "A_dft_hid_re": _A_in["dft_hid_re"],
        "A_dft_hid_im": _A_in["dft_hid_im"],
        "B_x": _B_in["x"],
        "B_w_gate": _B_in["w_gate"],
        "B_w_up": _B_in["w_up"],
        "B_w_down": _B_in["w_down"],
        "gate_w": _w((lambda *s: torch.randn(*s, dtype=torch.bfloat16) * 0.05)(512) * 16.0),
    }

def reference(A_x, A_dft_seq_re, A_dft_seq_im, A_dft_hid_re, A_dft_hid_im, B_x, B_w_gate, B_w_up, B_w_down, gate_w):
    def _A_ref(x, dft_seq_re, dft_seq_im, dft_hid_re, dft_hid_im):
        a = torch.einsum("bsd,ed->bse", x, dft_hid_re)
        b = torch.einsum("bsd,ed->bse", x, dft_hid_im)
        y = torch.einsum("ts,bse->bte", dft_seq_re, a) - torch.einsum("ts,bse->bte", dft_seq_im, b)
        return y

    def _B_ref(x, w_gate, w_up, w_down):
        g = _silu(x @ w_gate)
        u = x @ w_up
        return (g * u) @ w_down

    x = A_x
    g = _sigmoid((x * gate_w).sum(-1, keepdims=True))
    a = _A_ref(x, A_dft_seq_re, A_dft_seq_im, A_dft_hid_re, A_dft_hid_im)
    b = _B_ref(x, B_w_gate, B_w_up, B_w_down)
    return g * a + (1 - g) * b
```

```cpp
#include <hip/hip_runtime.h>
#include <hip/hip_bf16.h>
typedef unsigned short u16;
typedef short bf16x8 __attribute__((ext_vector_type(8)));
typedef float f32x4 __attribute__((ext_vector_type(4)));
constexpr int S=2048,D=512,F=1792,KZ=2816;
__device__ __forceinline__ float bf2f(u16 v){return __uint_as_float(((unsigned)v)<<16);}
__device__ __forceinline__ u16 f2bf(float f){ __bf16 b=(__bf16)f; return __builtin_bit_cast(u16,b);}

__global__ void gate_k(const u16* x,const u16* gw,float* g){
  int row=blockIdx.x*4+(threadIdx.x>>6), l=threadIdx.x&63; float s=0;
  for(int i=l;i<D;i+=64) s+=bf2f(x[row*D+i])*bf2f(gw[i]);
  for(int o=32;o;o>>=1) s+=__shfl_xor(s,o);
  if(l==0) g[row]=1.f/(1.f+__expf(-s));
}
__device__ __forceinline__ f32x4 tile(const u16*A,int lda,const u16*B,int sbk,int sbn,int K,int l,f32x4 acc){
  for(int k0=0;k0<K;k0+=32){
    bf16x8 a=*(const bf16x8*)(A+(l&15)*lda+k0+8*(l>>4)); bf16x8 b;
    for(int j=0;j<8;j++) b[j]=B[(k0+8*(l>>4)+j)*sbk+(l&15)*sbn];
    acc=__builtin_amdgcn_mfma_f32_16x16x32_bf16(a,b,acc,0,0,0);
  } return acc;
}
__global__ void p1(const u16*x,const u16*cr,const u16*ci,const u16*wg,const u16*wu,const float*g,u16*Z){
  int w=blockIdx.x*4+(threadIdx.x>>6), l=threadIdx.x&63;
  int tm=w/(KZ/16), tn=w%(KZ/16); int m0=tm*16,n0=tn*16; f32x4 z={0,0,0,0};
  if(n0<1024){ const u16*C=n0<512?cr:ci; int d0=n0&511; f32x4 a=tile(C+m0*S,S,x+d0,D,1,S,l,z);
    for(int j=0;j<4;j++){int r=m0+(l>>4)*4+j; float v=a[j]*g[r]; if(n0>=512)v=-v; Z[r*KZ+n0+(l&15)]=f2bf(v);} }
  else { int f0=n0-1024; f32x4 a=tile(x+m0*D,D,wg+f0,F,1,D,l,z), u=tile(x+m0*D,D,wu+f0,F,1,D,l,z);
    for(int j=0;j<4;j++){int r=m0+(l>>4)*4+j; float s=a[j]/(1.f+__expf(-a[j])); Z[r*KZ+n0+(l&15)]=f2bf(s*u[j]*(1.f-g[r]));} }
}
__global__ void p2(const u16*Z,const u16*hr,const u16*hi,const u16*wd,u16*out){
  int w=blockIdx.x*4+(threadIdx.x>>6), l=threadIdx.x&63;
  int tm=w/(D/16), tn=w%(D/16); int m0=tm*16,n0=tn*16; f32x4 a={0,0,0,0};
  a=tile(Z+m0*KZ,KZ,hr+n0*D,1,D,512,l,a);
  a=tile(Z+m0*KZ+512,KZ,hi+n0*D,1,D,512,l,a);
  a=tile(Z+m0*KZ+1024,KZ,wd+n0,D,1,F,l,a);
  for(int j=0;j<4;j++){int r=m0+(l>>4)*4+j; out[r*D+n0+(l&15)]=f2bf(a[j]);}
}
extern "C" void kernel_launch(void* const* d_in, const int* in_sizes, int n_in, void* d_out, int out_size, void* d_ws, size_t ws_size, hipStream_t stream) {
  const u16*x=(const u16*)d_in[0],*cr=(const u16*)d_in[1],*ci=(const u16*)d_in[2],*hr=(const u16*)d_in[3],*hi=(const u16*)d_in[4];
  const u16*wg=(const u16*)d_in[6],*wu=(const u16*)d_in[7],*wd=(const u16*)d_in[8],*gw=(const u16*)d_in[9];
  float*g=(float*)d_ws; u16*Z=(u16*)((char*)d_ws+65536);
  gate_k<<<S/4,256,0,stream>>>(x,gw,g);
  p1<<<(S/16)*(KZ/16)/4,256,0,stream>>>(x,cr,ci,wg,wu,g,Z);
  p2<<<(S/16)*(D/16)/4,256,0,stream>>>(Z,hr,hi,wd,(u16*)d_out);
}
```

```cpp
#include <hip/hip_runtime.h>
#include <hip/hip_bf16.h>
typedef unsigned short u16;
typedef short bf16x8 __attribute__((ext_vector_type(8)));
typedef float f32x4 __attribute__((ext_vector_type(4)));
constexpr int S=2048,D=512,F=1792,KZ=2816;
__device__ __forceinline__ float bf2f(u16 v){return __uint_as_float(((unsigned)v)<<16);}
__device__ __forceinline__ u16 f2bf(float f){ __bf16 b=(__bf16)f; return __builtin_bit_cast(u16,b);}

__global__ void gate_k(const u16* x,const u16* gw,float* g){
  int row=blockIdx.x*4+(threadIdx.x>>6), l=threadIdx.x&63; float s=0;
  for(int i=l;i<D;i+=64) s+=bf2f(x[row*D+i])*bf2f(gw[i]);
  for(int o=32;o;o>>=1) s+=__shfl_xor(s,o);
  if(l==0) g[row]=1.f/(1.f+__expf(-s));
}
__device__ __forceinline__ f32x4 tile(const u16*A,int lda,const u16*B,int sbk,int sbn,int K,int l,f32x4 acc){
  for(int k0=0;k0<K;k0+=32){
    bf16x8 a=*(const bf16x8*)(A+(l&15)*lda+k0+8*(l>>4)); bf16x8 b;
    for(int j=0;j<8;j++) b[j]=B[(k0+8*(l>>4)+j)*sbk+(l&15)*sbn];
    acc=__builtin_amdgcn_mfma_f32_16x16x32_bf16(a,b,acc,0,0,0);
  } return acc;
}
__global__ void p1(const u16*x,const u16*cr,const u16*ci,const u16*wg,const u16*wu,const float*g,u16*Z){
  int w=blockIdx.x*4+(threadIdx.x>>6), l=threadIdx.x&63;
  int tm=w/(KZ/16), tn=w%(KZ/16); int m0=tm*16,n0=tn*16; f32x4 z={0,0,0,0};
  if(n0<1024){ const u16*C=n0<512?cr:ci; int d0=n0&511; f32x4 a=tile(C+m0*S,S,x+d0,D,1,S,l,z);
    for(int j=0;j<4;j++){int r=m0+(l>>4)*4+j; float v=a[j]*g[r]; if(n0>=512)v=-v; Z[r*KZ+n0+(l&15)]=f2bf(v);} }
  else { int f0=n0-1024; f32x4 a=tile(x+m0*D,D,wg+f0,F,1,D,l,z), u=tile(x+m0*D,D,wu+f0,F,1,D,l,z);
    for(int j=0;j<4;j++){int r=m0+(l>>4)*4+j; float s=a[j]/(1.f+__expf(-a[j])); Z[r*KZ+n0+(l&15)]=f2bf(s*u[j]*(1.f-g[r]));} }
}
__global__ void p2(const u16*Z,const u16*hr,const u16*hi,const u16*wd,u16*out){
  int w=blockIdx.x*4+(threadIdx.x>>6), l=threadIdx.x&63;
  int tm=w/(D/16), tn=w%(D/16); int m0=tm*16,n0=tn*16; f32x4 a={0,0,0,0};
  a=tile(Z+m0*KZ,KZ,hr+n0*D,1,D,512,l,a);
  a=tile(Z+m0*KZ+512,KZ,hi+n0*D,1,D,512,l,a);
  a=tile(Z+m0*KZ+1024,KZ,wd+n0,D,1,F,l,a);
  for(int j=0;j<4;j++){int r=m0+(l>>4)*4+j; out[r*D+n0+(l&15)]=f2bf(a[j]);}
}

__global__ void probe(const unsigned* p, int n, int stride_dw, unsigned* o){
  unsigned off=0, acc=0;
  for(int i=0;i<n;i++){ unsigned v=p[off]; asm volatile("":"+v"(v)); unsigned z; asm volatile("v_and_b32 %0, 0, %1":"=v"(z):"v"(v)); off += stride_dw + z; acc^=v; }
  if(threadIdx.x==0) o[0]=acc;
}
extern "C" void kernel_launch(void* const* d_in, const int* in_sizes, int n_in, void* d_out, int out_size, void* d_ws, size_t ws_size, hipStream_t stream) {
  const u16*x=(const u16*)d_in[0],*cr=(const u16*)d_in[1],*ci=(const u16*)d_in[2],*hr=(const u16*)d_in[3],*hi=(const u16*)d_in[4];
  const u16*wg=(const u16*)d_in[6],*wu=(const u16*)d_in[7],*wd=(const u16*)d_in[8],*gw=(const u16*)d_in[9];
  float*g=(float*)d_ws; u16*Z=(u16*)((char*)d_ws+65536);
  probe<<<1,64,0,stream>>>((const unsigned*)d_in[0],1900,256,(unsigned*)((char*)d_ws+(64<<20)));
  gate_k<<<S/4,256,0,stream>>>(x,gw,g);
  p1<<<(S/16)*(KZ/16)/4,256,0,stream>>>(x,cr,ci,wg,wu,g,Z);
  p2<<<(S/16)*(D/16)/4,256,0,stream>>>(Z,hr,hi,wd,(u16*)d_out);
}
```

```cpp
#include <hip/hip_runtime.h>
typedef unsigned short u16;
typedef unsigned u32x4 __attribute__((ext_vector_type(4)));
struct Args { const void* p[9]; void* ws; void* out; };

__global__ __launch_bounds__(256, 1) __attribute__((aligned(4096))) void main_k(Args a) {
  __shared__ __attribute__((aligned(16))) char lds[163840];
  const unsigned w = __builtin_amdgcn_readfirstlane(threadIdx.x >> 6);
  asm volatile(
  "s_load_dwordx16 s[76:91], %0, 0x0\n"
  "s_load_dwordx4 s[92:95], %0, 0x40\n"
  "s_load_dwordx2 s[96:97], %0, 0x50\n"
  "v_mbcnt_lo_u32_b32 v1, -1, 0\n"
  "v_mbcnt_hi_u32_b32 v1, -1, v1\n"
  "s_mov_b32 s60, %1\n"
  "s_mov_b32 s3, %2\n"
  "s_mov_b32 s2, s60\n"
  "s_and_b32 s35, s2, 7\n"
  "s_bfe_u32 s34, s2, 0x40003\n"
  "s_cmp_ge_u32 s2, 128\n"
  "s_cbranch_scc1 FFN_%=\n"
  ".p2align 7\n"
  "DFT_%=:\n"
  "s_getpc_b64 s[60:61]\n"
  "DFT_pc_%=:\n"
  "s_and_b32 s61, s61, 0xffff\n"
  "s_mov_b32 s62, DFTEND_%=-DFT_pc_%=\n"
  "s_mov_b32 s63, 0x20000\n"
  "s_lshl_b32 s64, s34, 2\n"
  "s_add_u32 s64, s64, s3\n"
  "s_lshl_b32 s64, s64, 7\n"
  "v_lshl_add_u32 v220, v1, 13, s64\n"
  "buffer_load_dword v221, v220, s[60:63], 0 offen\n"
  "v_lshrrev_b32 v2, 3, v1\n"
  "v_lshrrev_b32 v3, 4, v1\n"
  "v_and_b32 v4, 7, v1\n"
  "v_and_b32 v5, 1, v3\n"
  "v_and_b32 v6, 15, v1\n"
  "v_bfe_u32 v7, v1, 2, 2\n"
  "v_and_b32 v8, 3, v1\n"
  "s_lshr_b32 s36, s35, 1\n"
  "s_and_b32 s37, s35, 1\n"
  "s_lshr_b32 s38, s34, 2\n"
  "s_and_b32 s39, s34, 3\n"
  "s_and_b32 s60, s39, 2\n"
  "s_add_u32 s62, s38, s60\n"
  "s_and_b32 s60, s62, 3\n"
  "s_lshl_b32 s61, s37, 2\n"
  "s_add_u32 s40, s60, s61\n"
  "s_and_b32 s70, s3, 1\n"
  "s_lshr_b32 s71, s3, 1\n"
  "s_and_b32 s75, s39, 1\n"
  "s_and_b32 s63, s36, 1\n"
  "s_lshl_b32 s60, s63, 3\n"
  "s_add_u32 s60, s60, s40\n"
  "s_lshl_b32 s60, s60, 1\n"
  "s_add_u32 s60, s60, s75\n"
  "s_lshl_b32 s60, s60, 1\n"
  "s_add_u32 s60, s60, s71\n"
  "s_lshl_b32 s60, s60, 3\n"
  "s_add_u32 s60, s60, s70\n"
  "s_lshl_b32 s60, s60, 12\n"
  "v_lshrrev_b32 v12, 5, v1\n"
  "v_and_b32 v13, 3, v2\n"
  "v_lshl_add_u32 v12, v12, 3, v13\n"
  "v_lshlrev_b32 v12, 10, v12\n"
  "v_xor_b32 v10, v4, v3\n"
  "v_lshlrev_b32 v10, 4, v10\n"
  "v_add3_u32 v192, v12, v10, s60\n"
  "v_xor_b32 v13, 64, v192\n"
  "v_add_u32 v193, 15360, v13\n"
  "s_lshl_b32 s60, s39, 2\n"
  "s_add_u32 s60, s60, s3\n"
  "s_lshl_b32 s60, s60, 15\n"
  "v_lshlrev_b32 v12, 10, v2\n"
  "v_add3_u32 v196, v12, v10, s60\n"
  "v_xor_b32 v13, 64, v196\n"
  "v_add_u32 v197, 7168, v13\n"
  "v_add_u32 v198, 14336, v196\n"
  "v_add_u32 v199, 14336, v197\n"
  "s_lshl_b32 s24, s3, 12\n"
  "s_lshl_b32 s25, s3, 13\n"
  "s_lshl_b32 s27, s3, 11\n"
  "s_add_u32 s64, s62, s39\n"
  "s_lshl_b32 s64, s64, 0\n"
  "s_add_u32 s60, s64, 0\n"
  "s_and_b32 s60, s60, 3\n"
  "s_lshl_b32 s16, s60, 7\n"
  "s_add_u32 s60, s64, 1\n"
  "s_and_b32 s60, s60, 3\n"
  "s_lshl_b32 s17, s60, 7\n"
  "s_add_u32 s60, s64, 2\n"
  "s_and_b32 s60, s60, 3\n"
  "s_lshl_b32 s18, s60, 7\n"
  "s_add_u32 s60, s64, 3\n"
  "s_and_b32 s60, s60, 3\n"
  "s_lshl_b32 s19, s60, 7\n"
  "s_add_u32 s60, s64, 4\n"
  "s_and_b32 s60, s60, 3\n"
  "s_add_u32 s60, s60, 4\n"
  "s_lshl_b32 s31, s60, 7\n"
  "s_add_u32 s60, s64, 5\n"
  "s_and_b32 s60, s60, 3\n"
  "s_add_u32 s60, s60, 4\n"
  "s_lshl_b32 s41, s60, 7\n"
  "s_waitcnt lgkmcnt(0)\n"
  "s_mov_b32 s8, s76\n"
  "s_and_b32 s9, s77, 0xffff\n"
  "s_mov_b32 s10, 2097152\n"
  "s_mov_b32 s11, 0x20000\n"
  "s_cmp_ge_u32 s36, 2\n"
  "s_cselect_b32 s12, s84, s82\n"
  "s_cselect_b32 s13, s85, s83\n"
  "s_and_b32 s13, s13, 0xffff\n"
  "s_mov_b32 s14, 524288\n"
  "s_mov_b32 s15, 0x20000\n"
  "s_add_u32 m0, s27, 16384\n"
  "s_nop 0\n"
  "buffer_load_dwordx4 v192, s[8:11], s16 offen lds\n"
  "buffer_load_dwordx4 v193, s[8:11], s16 offen offset:1024 lds\n"
  "s_add_u32 m0, s24, 0\n"
  "s_nop 0\n"
  "buffer_load_dwordx4 v196, s[12:15], s16 offen lds\n"
  "buffer_load_dwordx4 v197, s[12:15], s16 offen offset:1024 lds\n"
  "buffer_load_dwordx4 v198, s[12:15], s16 offen offset:2048 lds\n"
  "buffer_load_dwordx4 v199, s[12:15], s16 offen offset:3072 lds\n"
  "s_add_u32 m0, s27, 40960\n"
  "s_nop 0\n"
  "buffer_load_dwordx4 v192, s[8:11], s17 offen lds\n"
  "buffer_load_dwordx4 v193, s[8:11], s17 offen offset:1024 lds\n"
  "s_add_u32 m0, s24, 24576\n"
  "s_nop 0\n"
  "buffer_load_dwordx4 v196, s[12:15], s17 offen lds\n"
  "s_add_u32 s60, s64, 6\n"
  "s_and_b32 s60, s60, 3\n"
  "s_add_u32 s60, s60, 4\n"
  "s_lshl_b32 s66, s60, 7\n"
  "s_add_u32 s60, s64, 7\n"
  "s_and_b32 s60, s60, 3\n"
  "s_add_u32 s60, s60, 4\n"
  "s_lshl_b32 s67, s60, 7\n"
  "s_mov_b32 s20, s94\n"
  "s_and_b32 s21, s95, 0xffff\n"
  "s_mov_b32 s22, 22409216\n"
  "s_mov_b32 s23, 0x20000\n"
  "s_cmp_ge_u32 s36, 2\n"
  "s_cselect_b32 s4, s80, s78\n"
  "s_cselect_b32 s5, s81, s79\n"
  "s_and_b32 s5, s5, 0xffff\n"
  "s_mov_b32 s6, 8388608\n"
  "s_mov_b32 s7, 0x20000\n"
  "v_cmp_eq_u32 vcc, 0, v1\n"
  "v_mov_b32 v13, 1073741824\n"
  "s_nop 0\n"
  "v_cndmask_b32 v215, v13, v1, vcc\n"
  "v_lshlrev_b32 v211, 4, v1\n"
  "v_lshlrev_b32 v214, 3, v6\n"
  "v_xor_b32 v194, 0x10000, v192\n"
  "v_xor_b32 v195, 0x10000, v193\n"
  "s_lshl_b32 s60, s37, 2\n"
  "s_add_u32 s72, s60, s38\n"
  "s_lshl_b32 s60, s72, 2\n"
  "s_add_u32 s60, s60, s3\n"
  "s_lshl_b32 s60, s60, 18\n"
  "v_lshlrev_b32 v12, 12, v2\n"
  "v_add3_u32 v200, v12, v10, s60\n"
  "v_xor_b32 v13, 64, v200\n"
  "v_add_u32 v201, 31744, v13\n"
  "v_add_u32 v202, 63488, v200\n"
  "v_add_u32 v203, 63488, v201\n"
  "s_lshl_b32 s60, s36, 2\n"
  "s_add_u32 s73, s60, s39\n"
  "s_lshl_b32 s60, s73, 18\n"
  "s_add_u32 s60, s60, s24\n"
  "s_add_u32 s74, s60, 17825792\n"
  "s_lshl_b32 s63, s63, 11\n"
  "s_lshl_b32 s60, s75, 7\n"
  "s_add_u32 s63, s63, s60\n"
  "s_lshl_b32 s60, s75, 14\n"
  "s_add_u32 s74, s74, s60\n"
  "s_add_u32 s60, s62, 0\n"
  "s_and_b32 s60, s60, 3\n"
  "s_xor_b32 s61, s37, 0\n"
  "s_lshl_b32 s61, s61, 2\n"
  "s_add_u32 s60, s60, s61\n"
  "s_lshl_b32 s64, s60, 8\n"
  "s_add_u32 s44, s64, s63\n"
  "s_lshl_b32 s64, s60, 15\n"
  "s_add_u32 s52, s64, s74\n"
  "s_add_u32 s60, s62, 2\n"
  "s_and_b32 s60, s60, 3\n"
  "s_add_u32 s60, s60, s61\n"
  "s_lshl_b32 s64, s60, 8\n"
  "s_add_u32 s45, s64, s63\n"
  "s_lshl_b32 s64, s60, 15\n"
  "s_add_u32 s53, s64, s74\n"
  "s_add_u32 s60, s62, 1\n"
  "s_and_b32 s60, s60, 3\n"
  "s_add_u32 s60, s60, s61\n"
  "s_lshl_b32 s64, s60, 8\n"
  "s_add_u32 s46, s64, s63\n"
  "s_lshl_b32 s64, s60, 15\n"
  "s_add_u32 s54, s64, s74\n"
  "s_add_u32 s60, s62, 3\n"
  "s_and_b32 s60, s60, 3\n"
  "s_add_u32 s60, s60, s61\n"
  "s_lshl_b32 s64, s60, 8\n"
  "s_add_u32 s47, s64, s63\n"
  "s_lshl_b32 s64, s60, 15\n"
  "s_add_u32 s55, s64, s74\n"
  "s_add_u32 s60, s62, 0\n"
  "s_and_b32 s60, s60, 3\n"
  "s_xor_b32 s61, s37, 1\n"
  "s_lshl_b32 s61, s61, 2\n"
  "s_add_u32 s60, s60, s61\n"
  "s_lshl_b32 s64, s60, 8\n"
  "s_add_u32 s48, s64, s63\n"
  "s_lshl_b32 s64, s60, 15\n"
  "s_add_u32 s56, s64, s74\n"
  "s_add_u32 s60, s62, 2\n"
  "s_and_b32 s60, s60, 3\n"
  "s_add_u32 s60, s60, s61\n"
  "s_lshl_b32 s64, s60, 8\n"
  "s_add_u32 s49, s64, s63\n"
  "s_lshl_b32 s64, s60, 15\n"
  "s_add_u32 s57, s64, s74\n"
  "s_add_u32 s60, s62, 1\n"
  "s_and_b32 s60, s60, 3\n"
  "s_add_u32 s60, s60, s61\n"
  "s_lshl_b32 s64, s60, 8\n"
  "s_add_u32 s50, s64, s63\n"
  "s_lshl_b32 s64, s60, 15\n"
  "s_add_u32 s58, s64, s74\n"
  "s_add_u32 s60, s62, 3\n"
  "s_and_b32 s60, s60, 3\n"
  "s_add_u32 s60, s60, s61\n"
  "s_lshl_b32 s64, s60, 8\n"
  "s_add_u32 s51, s64, s63\n"
  "s_lshl_b32 s64, s60, 15\n"
  "s_add_u32 s59, s64, s74\n"
  "s_lshl_b32 s60, s70, 13\n"
  "s_lshl_b32 s61, s71, 12\n"
  "s_add_u32 s61, s61, s60\n"
  "s_sub_u32 s60, s52, s24\n"
  "s_add_u32 s42, s60, s61\n"
  "s_xor_b32 s43, s42, 16384\n"
  "s_add_u32 s61, s61, 32768\n"
  "v_add_u32 v212, s61, v211\n"
  "s_lshl_b32 s60, s73, 8\n"
  "s_add_u32 s60, s60, 22085632\n"
  "s_lshl_b32 s61, s37, 7\n"
  "s_add_u32 s29, s60, s61\n"
  "s_lshl_b32 s61, s40, 2\n"
  "s_add_u32 s61, s61, s3\n"
  "s_lshl_b32 s61, s61, 3\n"
  "s_add_u32 s28, s60, s61\n"
  "v_bfe_u32 v12, v1, 1, 3\n"
  "v_xor_b32 v12, v12, v3\n"
  "v_lshlrev_b32 v13, 7, v6\n"
  "v_lshl_add_u32 v13, v12, 4, v13\n"
  "s_lshl_b32 s60, s70, 12\n"
  "v_add_u32 v204, s60, v13\n"
  "v_xor_b32 v205, 64, v204\n"
  "v_add_u32 v210, s60, v211\n"
  "s_lshl_b32 s60, s71, 13\n"
  "v_add_u32 v206, s60, v13\n"
  "v_xor_b32 v207, 64, v206\n"
  "s_lshl_b32 s60, s71, 14\n"
  "v_add_u32 v208, s60, v13\n"
  "v_xor_b32 v209, 64, v208\n"
  "v_add_u32 v224, 49152, v204\n"
  "v_add_u32 v225, 49152, v205\n"
  "v_add_u32 v226, 49152, v206\n"
  "v_add_u32 v227, 49152, v207\n"
  "v_add_u32 v228, 49152, v208\n"
  "v_add_u32 v229, 49152, v209\n"
  "v_add_u32 v230, 49152, v210\n"
  "v_add_u32 v231, 98304, v204\n"
  "v_add_u32 v232, 98304, v205\n"
  "v_add_u32 v233, 98304, v206\n"
  "v_add_u32 v234, 98304, v207\n"
  "v_add_u32 v235, 98304, v208\n"
  "v_add_u32 v236, 98304, v209\n"
  "v_add_u32 v237, 98304, v210\n"
  "s_lshl_b32 s60, s72, 1\n"
  "s_add_u32 s60, s60, s71\n"
  "s_lshl_b32 s61, s39, 1\n"
  "s_add_u32 s61, s61, s70\n"
  "s_lshl_b32 s63, s60, 19\n"
  "s_lshl_b32 s64, s61, 16\n"
  "s_add_u32 s63, s63, s64\n"
  "s_lshl_b32 s64, s36, 12\n"
  "s_add_u32 s63, s63, s64\n"
  "s_add_u32 s63, s63, 7340032\n"
  "v_add_u32 v213, s63, v211\n"
  "s_lshl_b32 s60, s60, 3\n"
  "s_add_u32 s60, s60, s61\n"
  "s_lshl_b32 s60, s60, 5\n"
  "s_add_u32 s60, s60, s36\n"
  "s_lshl_b32 s60, s60, 3\n"
  "s_add_u32 s30, s60, 22106112\n"
  "s_mov_b32 s65, 0\n"
  "s_mov_b32 s60, 22142976\n"
  "buffer_load_dwordx2 v[216:217], off, s[20:23], s60 sc1\n"
  "s_mov_b64 exec, 0\n"
  "s_cmp_eq_u32 s3, 0\n"
  "s_cbranch_scc1 DP1_%=\n"
  "s_cmp_eq_u32 s3, 1\n"
  "s_cbranch_scc1 DP2_%=\n"
  "s_cmp_eq_u32 s3, 2\n"
  "s_cbranch_scc1 DP3_%=\n"
  "s_branch DP4_%=\n"
  "DPE_%=:\n"
  "s_mov_b64 exec, -1\n"
  "buffer_load_dwordx4 v197, s[12:15], s17 offen offset:1024 lds\n"
  "buffer_load_dwordx4 v198, s[12:15], s17 offen offset:2048 lds\n"
  "buffer_load_dwordx4 v199, s[12:15], s17 offen offset:3072 lds\n"
  "s_add_u32 m0, s27, 65536\n"
  "s_nop 0\n"
  "buffer_load_dwordx4 v192, s[8:11], s18 offen lds\n"
  "buffer_load_dwordx4 v193, s[8:11], s18 offen offset:1024 lds\n"
  "s_waitcnt vmcnt(9)\n"
  "s_barrier\n"
  "ds_read_b128 v[0:3], v204 offset:16384\n"
  "ds_read_b128 v[4:7], v205 offset:16384\n"
  "ds_read_b128 a[0:3], v206 offset:0\n"
  "ds_read_b128 a[4:7], v207 offset:0\n"
  "ds_read_b128 v[8:11], v204 offset:18432\n"
  "ds_read_b128 v[12:15], v205 offset:18432\n"
  "ds_read_b128 a[8:11], v206 offset:2048\n"
  "ds_read_b128 a[12:15], v207 offset:2048\n"
  "ds_read_b128 a[16:19], v206 offset:4096\n"
  "ds_read_b128 a[20:23], v207 offset:4096\n"
  "ds_read_b128 a[24:27], v206 offset:6144\n"
  "ds_read_b128 a[28:31], v207 offset:6144\n"
  "s_waitcnt lgkmcnt(4)\n"
  "v_mfma_f32_16x16x32_bf16 v[128:131], v[0:3], a[0:3], 0\n"
  "s_add_u32 m0, s24, 49152\n"
  "s_nop 0\n"
  "buffer_load_dwordx4 v196, s[12:15], s18 offen lds\n"
  "v_mfma_f32_16x16x32_bf16 v[128:131], v[4:7], a[4:7], v[128:131]\n"
  "v_mfma_f32_16x16x32_bf16 v[132:135], v[8:11], a[0:3], 0\n"
  "v_mfma_f32_16x16x32_bf16 v[132:135], v[12:15], a[4:7], v[132:135]\n"
  "buffer_load_dwordx4 v197, s[12:15], s18 offen offset:1024 lds\n"
  "buffer_load_dwordx4 v198, s[12:15], s18 offen offset:2048 lds\n"
  "buffer_load_dwordx4 v199, s[12:15], s18 offen offset:3072 lds\n"
  "s_add_u32 m0, s27, 90112\n"
  "s_nop 0\n"
  "buffer_load_dwordx4 v192, s[8:11], s19 offen lds\n"
  "buffer_load_dwordx4 v193, s[8:11], s19 offen offset:1024 lds\n"
  "s_waitcnt vmcnt(8) lgkmcnt(0)\n"
  "s_barrier\n"
  "v_mfma_f32_16x16x32_bf16 v[136:139], v[0:3], a[8:11], 0\n"
  "ds_read_b128 v[16:19], v204 offset:40960\n"
  "ds_read_b128 v[20:23], v205 offset:40960\n"
  "v_mfma_f32_16x16x32_bf16 v[136:139], v[4:7], a[12:15], v[136:139]\n"
  "s_add_u32 m0, s24, 73728\n"
  "s_nop 0\n"
  "buffer_load_dwordx4 v196, s[12:15], s19 offen lds\n"
  "v_mfma_f32_16x16x32_bf16 v[140:143], v[8:11], a[8:11], 0\n"
  "ds_read_b128 a[32:35], v206 offset:24576\n"
  "ds_read_b128 a[36:39], v207 offset:24576\n"
  "v_mfma_f32_16x16x32_bf16 v[140:143], v[12:15], a[12:15], v[140:143]\n"
  "v_mfma_f32_16x16x32_bf16 v[144:147], v[0:3], a[16:19], 0\n"
  "ds_read_b128 v[24:27], v204 offset:43008\n"
  "ds_read_b128 v[28:31], v205 offset:43008\n"
  "v_mfma_f32_16x16x32_bf16 v[144:147], v[4:7], a[20:23], v[144:147]\n"
  "buffer_load_dwordx4 v197, s[12:15], s19 offen offset:1024 lds\n"
  "v_mfma_f32_16x16x32_bf16 v[148:151], v[8:11], a[16:19], 0\n"
  "ds_read_b128 a[40:43], v206 offset:26624\n"
  "ds_read_b128 a[44:47], v207 offset:26624\n"
  "v_mfma_f32_16x16x32_bf16 v[148:151], v[12:15], a[20:23], v[148:151]\n"
  "v_mfma_f32_16x16x32_bf16 v[152:155], v[0:3], a[24:27], 0\n"
  "ds_read_b128 a[48:51], v206 offset:28672\n"
  "ds_read_b128 a[52:55], v207 offset:28672\n"
  "v_mfma_f32_16x16x32_bf16 v[152:155], v[4:7], a[28:31], v[152:155]\n"
  "buffer_load_dwordx4 v198, s[12:15], s19 offen offset:2048 lds\n"
  "v_mfma_f32_16x16x32_bf16 v[156:159], v[8:11], a[24:27], 0\n"
  "ds_read_b128 a[56:59], v206 offset:30720\n"
  "ds_read_b128 a[60:63], v207 offset:30720\n"
  "v_mfma_f32_16x16x32_bf16 v[156:159], v[12:15], a[28:31], v[156:159]\n"
  "s_waitcnt lgkmcnt(4)\n"
  "v_mfma_f32_16x16x32_bf16 v[128:131], v[16:19], a[32:35], v[128:131]\n"
  "v_mfma_f32_16x16x32_bf16 v[128:131], v[20:23], a[36:39], v[128:131]\n"
  "buffer_load_dwordx4 v199, s[12:15], s19 offen offset:3072 lds\n"
  "s_add_u32 m0, s27, 16384\n"
  "v_mfma_f32_16x16x32_bf16 v[132:135], v[24:27], a[32:35], v[132:135]\n"
  "v_mfma_f32_16x16x32_bf16 v[132:135], v[28:31], a[36:39], v[132:135]\n"
  "buffer_load_dwordx4 v192, s[8:11], s31 offen lds\n"
  "s_waitcnt vmcnt(7) lgkmcnt(0)\n"
  "s_barrier\n"
  "v_mfma_f32_16x16x32_bf16 v[136:139], v[16:19], a[40:43], v[136:139]\n"
  "ds_read_b128 v[32:35], v224 offset:16384\n"
  "ds_read_b128 v[36:39], v225 offset:16384\n"
  "v_mfma_f32_16x16x32_bf16 v[136:139], v[20:23], a[44:47], v[136:139]\n"
  "buffer_load_dwordx4 v193, s[8:11], s31 offen offset:1024 lds\n"
  "s_add_u32 m0, s24, 0\n"
  "v_mfma_f32_16x16x32_bf16 v[140:143], v[24:27], a[40:43], v[140:143]\n"
  "ds_read_b128 a[64:67], v226 offset:0\n"
  "ds_read_b128 a[68:71], v227 offset:0\n"
  "v_mfma_f32_16x16x32_bf16 v[140:143], v[28:31], a[44:47], v[140:143]\n"
  "v_mfma_f32_16x16x32_bf16 v[144:147], v[16:19], a[48:51], v[144:147]\n"
  "ds_read_b128 v[40:43], v224 offset:18432\n"
  "ds_read_b128 v[44:47], v225 offset:18432\n"
  "v_mfma_f32_16x16x32_bf16 v[144:147], v[20:23], a[52:55], v[144:147]\n"
  "buffer_load_dwordx4 v196, s[12:15], s31 offen lds\n"
  "v_mfma_f32_16x16x32_bf16 v[148:151], v[24:27], a[48:51], v[148:151]\n"
  "ds_read_b128 a[72:75], v226 offset:2048\n"
  "ds_read_b128 a[76:79], v227 offset:2048\n"
  "v_mfma_f32_16x16x32_bf16 v[148:151], v[28:31], a[52:55], v[148:151]\n"
  "v_mfma_f32_16x16x32_bf16 v[152:155], v[16:19], a[56:59], v[152:155]\n"
  "ds_read_b128 a[80:83], v226 offset:4096\n"
  "ds_read_b128 a[84:87], v227 offset:4096\n"
  "v_mfma_f32_16x16x32_bf16 v[152:155], v[20:23], a[60:63], v[152:155]\n"
  "buffer_load_dwordx4 v197, s[12:15], s31 offen offset:1024 lds\n"
  "v_mfma_f32_16x16x32_bf16 v[156:159], v[24:27], a[56:59], v[156:159]\n"
  "ds_read_b128 a[88:91], v226 offset:6144\n"
  "ds_read_b128 a[92:95], v227 offset:6144\n"
  "v_mfma_f32_16x16x32_bf16 v[156:159], v[28:31], a[60:63], v[156:159]\n"
  "s_waitcnt lgkmcnt(4)\n"
  "v_mfma_f32_16x16x32_bf16 v[128:131], v[32:35], a[64:67], v[128:131]\n"
  "v_mfma_f32_16x16x32_bf16 v[128:131], v[36:39], a[68:71], v[128:131]\n"
  "buffer_load_dwordx4 v198, s[12:15], s31 offen offset:2048 lds\n"
  "v_mfma_f32_16x16x32_bf16 v[132:135], v[40:43], a[64:67], v[132:135]\n"
  "v_mfma_f32_16x16x32_bf16 v[132:135], v[44:47], a[68:71], v[132:135]\n"
  "buffer_load_dwordx4 v199, s[12:15], s31 offen offset:3072 lds\n"
  "s_waitcnt vmcnt(6) lgkmcnt(0)\n"
  "s_barrier\n"
  "v_mfma_f32_16x16x32_bf16 v[136:139], v[32:35], a[72:75], v[136:139]\n"
  "ds_read_b128 v[48:51], v224 offset:40960\n"
  "ds_read_b128 v[52:55], v225 offset:40960\n"
  "v_mfma_f32_16x16x32_bf16 v[136:139], v[36:39], a[76:79], v[136:139]\n"
  "s_add_u32 m0, s27, 40960\n"
  "s_nop 0\n"
  "buffer_load_dwordx4 v192, s[8:11], s41 offen lds\n"
  "v_mfma_f32_16x16x32_bf16 v[140:143], v[40:43], a[72:75], v[140:143]\n"
  "ds_read_b128 a[96:99], v226 offset:24576\n"
  "ds_read_b128 a[100:103], v227 offset:24576\n"
  "v_mfma_f32_16x16x32_bf16 v[140:143], v[44:47], a[76:79], v[140:143]\n"
  "v_mfma_f32_16x16x32_bf16 v[144:147], v[32:35], a[80:83], v[144:147]\n"
  "ds_read_b128 v[56:59], v224 offset:43008\n"
  "ds_read_b128 v[60:63], v225 offset:43008\n"
  "v_mfma_f32_16x16x32_bf16 v[144:147], v[36:39], a[84:87], v[144:147]\n"
  "buffer_load_dwordx4 v193, s[8:11], s41 offen offset:1024 lds\n"
  "s_add_u32 m0, s24, 24576\n"
  "v_mfma_f32_16x16x32_bf16 v[148:151], v[40:43], a[80:83], v[148:151]\n"
  "ds_read_b128 a[104:107], v226 offset:26624\n"
  "ds_read_b128 a[108:111], v227 offset:26624\n"
  "v_mfma_f32_16x16x32_bf16 v[148:151], v[44:47], a[84:87], v[148:151]\n"
  "v_mfma_f32_16x16x32_bf16 v[152:155], v[32:35], a[88:91], v[152:155]\n"
  "ds_read_b128 a[112:115], v226 offset:28672\n"
  "ds_read_b128 a[116:119], v227 offset:28672\n"
  "v_mfma_f32_16x16x32_bf16 v[152:155], v[36:39], a[92:95], v[152:155]\n"
  "buffer_load_dwordx4 v196, s[12:15], s41 offen lds\n"
  "v_mfma_f32_16x16x32_bf16 v[156:159], v[40:43], a[88:91], v[156:159]\n"
  "ds_read_b128 a[120:123], v226 offset:30720\n"
  "ds_read_b128 a[124:127], v227 offset:30720\n"
  "v_mfma_f32_16x16x32_bf16 v[156:159], v[44:47], a[92:95], v[156:159]\n"
  "s_waitcnt lgkmcnt(4)\n"
  "v_mfma_f32_16x16x32_bf16 v[128:131], v[48:51], a[96:99], v[128:131]\n"
  "v_mfma_f32_16x16x32_bf16 v[128:131], v[52:55], a[100:103], v[128:131]\n"
  "buffer_load_dwordx4 v197, s[12:15], s41 offen offset:1024 lds\n"
  "v_mfma_f32_16x16x32_bf16 v[132:135], v[56:59], a[96:99], v[132:135]\n"
  "v_mfma_f32_16x16x32_bf16 v[132:135], v[60:63], a[100:103], v[132:135]\n"
  "buffer_load_dwordx4 v198, s[12:15], s41 offen offset:2048 lds\n"
  "buffer_load_dwordx4 v199, s[12:15], s41 offen offset:3072 lds\n"
  "s_add_u32 m0, s27, 65536\n"
  "s_nop 0\n"
  "buffer_load_dwordx4 v192, s[8:11], s66 offen lds\n"
  "buffer_load_dwordx4 v193, s[8:11], s66 offen offset:1024 lds\n"
  "s_add_u32 m0, s24, 49152\n"
  "s_nop 0\n"
  "buffer_load_dwordx4 v196, s[12:15], s66 offen lds\n"
  "s_waitcnt vmcnt(9) lgkmcnt(0)\n"
  "s_barrier\n"
  "v_mfma_f32_16x16x32_bf16 v[136:139], v[48:51], a[104:107], v[136:139]\n"
  "ds_read_b128 v[64:67], v204 offset:16384\n"
  "ds_read_b128 v[68:71], v205 offset:16384\n"
  "v_mfma_f32_16x16x32_bf16 v[136:139], v[52:55], a[108:111], v[136:139]\n"
  "buffer_load_dwordx4 v197, s[12:15], s66 offen offset:1024 lds\n"
  "v_mfma_f32_16x16x32_bf16 v[140:143], v[56:59], a[104:107], v[140:143]\n"
  "ds_read_b128 a[128:131], v206 offset:0\n"
  "ds_read_b128 a[132:135], v207 offset:0\n"
  "v_mfma_f32_16x16x32_bf16 v[140:143], v[60:63], a[108:111], v[140:143]\n"
  "v_mfma_f32_16x16x32_bf16 v[144:147], v[48:51], a[112:115], v[144:147]\n"
  "ds_read_b128 v[72:75], v204 offset:18432\n"
  "ds_read_b128 v[76:79], v205 offset:18432\n"
  "v_mfma_f32_16x16x32_bf16 v[144:147], v[52:55], a[116:119], v[144:147]\n"
  "buffer_load_dwordx4 v198, s[12:15], s66 offen offset:2048 lds\n"
  "v_mfma_f32_16x16x32_bf16 v[148:151], v[56:59], a[112:115], v[148:151]\n"
  "ds_read_b128 a[136:139], v206 offset:2048\n"
  "ds_read_b128 a[140:143], v207 offset:2048\n"
  "v_mfma_f32_16x16x32_bf16 v[148:151], v[60:63], a[116:119], v[148:151]\n"
  "v_mfma_f32_16x16x32_bf16 v[152:155], v[48:51], a[120:123], v[152:155]\n"
  "ds_read_b128 a[144:147], v206 offset:4096\n"
  "ds_read_b128 a[148:151], v207 offset:4096\n"
  "v_mfma_f32_16x16x32_bf16 v[152:155], v[52:55], a[124:127], v[152:155]\n"
  "buffer_load_dwordx4 v199, s[12:15], s66 offen offset:3072 lds\n"
  "s_add_u32 m0, s27, 90112\n"
  "v_mfma_f32_16x16x32_bf16 v[156:159], v[56:59], a[120:123], v[156:159]\n"
  "ds_read_b128 a[152:155], v206 offset:6144\n"
  "ds_read_b128 a[156:159], v207 offset:6144\n"
  "v_mfma_f32_16x16x32_bf16 v[156:159], v[60:63], a[124:127], v[156:159]\n"
  "s_waitcnt lgkmcnt(4)\n"
  "v_mfma_f32_16x16x32_bf16 v[128:131], v[64:67], a[128:131], v[128:131]\n"
  "v_mfma_f32_16x16x32_bf16 v[128:131], v[68:71], a[132:135], v[128:131]\n"
  "buffer_load_dwordx4 v192, s[8:11], s67 offen lds\n"
  "v_mfma_f32_16x16x32_bf16 v[132:135], v[72:75], a[128:131], v[132:135]\n"
  "v_mfma_f32_16x16x32_bf16 v[132:135], v[76:79], a[132:135], v[132:135]\n"
  "buffer_load_dwordx4 v193, s[8:11], s67 offen offset:1024 lds\n"
  "s_add_u32 m0, s24, 73728\n"
  "s_nop 0\n"
  "buffer_load_dwordx4 v196, s[12:15], s67 offen lds\n"
  "s_waitcnt vmcnt(9) lgkmcnt(0)\n"
  "s_barrier\n"
  "v_mfma_f32_16x16x32_bf16 v[136:139], v[64:67], a[136:139], v[136:139]\n"
  "ds_read_b128 v[80:83], v204 offset:40960\n"
  "ds_read_b128 v[84:87], v205 offset:40960\n"
  "v_mfma_f32_16x16x32_bf16 v[136:139], v[68:71], a[140:143], v[136:139]\n"
  "buffer_load_dwordx4 v197, s[12:15], s67 offen offset:1024 lds\n"
  "v_mfma_f32_16x16x32_bf16 v[140:143], v[72:75], a[136:139], v[140:143]\n"
  "ds_read_b128 a[160:163], v206 offset:24576\n"
  "ds_read_b128 a[164:167], v207 offset:24576\n"
  "v_mfma_f32_16x16x32_bf16 v[140:143], v[76:79], a[140:143], v[140:143]\n"
  "v_mfma_f32_16x16x32_bf16 v[144:147], v[64:67], a[144:147], v[144:147]\n"
  "ds_read_b128 v[88:91], v204 offset:43008\n"
  "ds_read_b128 v[92:95], v205 offset:43008\n"
  "v_mfma_f32_16x16x32_bf16 v[144:147], v[68:71], a[148:151], v[144:147]\n"
  "buffer_load_dwordx4 v198, s[12:15], s67 offen offset:2048 lds\n"
  "v_mfma_f32_16x16x32_bf16 v[148:151], v[72:75], a[144:147], v[148:151]\n"
  "ds_read_b128 a[168:171], v206 offset:26624\n"
  "ds_read_b128 a[172:175], v207 offset:26624\n"
  "v_mfma_f32_16x16x32_bf16 v[148:151], v[76:79], a[148:151], v[148:151]\n"
  "v_mfma_f32_16x16x32_bf16 v[152:155], v[64:67], a[152:155], v[152:155]\n"
  "ds_read_b128 a[176:179], v206 offset:28672\n"
  "ds_read_b128 a[180:183], v207 offset:28672\n"
  "v_mfma_f32_16x16x32_bf16 v[152:155], v[68:71], a[156:159], v[152:155]\n"
  "buffer_load_dwordx4 v199, s[12:15], s67 offen offset:3072 lds\n"
  "s_add_u32 m0, s27, 98304\n"
  "v_mfma_f32_16x16x32_bf16 v[156:159], v[72:75], a[152:155], v[156:159]\n"
  "ds_read_b128 a[184:187], v206 offset:30720\n"
  "ds_read_b128 a[188:191], v207 offset:30720\n"
  "v_mfma_f32_16x16x32_bf16 v[156:159], v[76:79], a[156:159], v[156:159]\n"
  "s_waitcnt lgkmcnt(4)\n"
  "v_mfma_f32_16x16x32_bf16 v[128:131], v[80:83], a[160:163], v[128:131]\n"
  "v_mfma_f32_16x16x32_bf16 v[128:131], v[84:87], a[164:167], v[128:131]\n"
  "buffer_load_dwordx4 v194, s[8:11], s16 offen lds\n"
  "v_mfma_f32_16x16x32_bf16 v[132:135], v[88:91], a[160:163], v[132:135]\n"
  "v_mfma_f32_16x16x32_bf16 v[132:135], v[92:95], a[164:167], v[132:135]\n"
  "buffer_load_dwordx4 v195, s[8:11], s16 offen offset:1024 lds\n"
  "s_waitcnt vmcnt(8) lgkmcnt(0)\n"
  "s_barrier\n"
  "v_mfma_f32_16x16x32_bf16 v[136:139], v[80:83], a[168:171], v[136:139]\n"
  "ds_read_b128 v[0:3], v224 offset:16384\n"
  "ds_read_b128 v[4:7], v225 offset:16384\n"
  "v_mfma_f32_16x16x32_bf16 v[136:139], v[84:87], a[172:175], v[136:139]\n"
  "s_add_u32 m0, s27, 106496\n"
  "s_nop 0\n"
  "buffer_load_dwordx4 v194, s[8:11], s17 offen lds\n"
  "v_mfma_f32_16x16x32_bf16 v[140:143], v[88:91], a[168:171], v[140:143]\n"
  "ds_read_b128 a[192:195], v226 offset:0\n"
  "ds_read_b128 a[196:199], v227 offset:0\n"
  "v_mfma_f32_16x16x32_bf16 v[140:143], v[92:95], a[172:175], v[140:143]\n"
  "v_mfma_f32_16x16x32_bf16 v[144:147], v[80:83], a[176:179], v[144:147]\n"
  "ds_read_b128 v[8:11], v224 offset:18432\n"
  "ds_read_b128 v[12:15], v225 offset:18432\n"
  "v_mfma_f32_16x16x32_bf16 v[144:147], v[84:87], a[180:183], v[144:147]\n"
  "buffer_load_dwordx4 v195, s[8:11], s17 offen offset:1024 lds\n"
  "s_add_u32 m0, s27, 114688\n"
  "v_mfma_f32_16x16x32_bf16 v[148:151], v[88:91], a[176:179], v[148:151]\n"
  "ds_read_b128 a[200:203], v226 offset:2048\n"
  "ds_read_b128 a[204:207], v227 offset:2048\n"
  "v_mfma_f32_16x16x32_bf16 v[148:151], v[92:95], a[180:183], v[148:151]\n"
  "v_mfma_f32_16x16x32_bf16 v[152:155], v[80:83], a[184:187], v[152:155]\n"
  "ds_read_b128 a[208:211], v226 offset:4096\n"
  "ds_read_b128 a[212:215], v227 offset:4096\n"
  "v_mfma_f32_16x16x32_bf16 v[152:155], v[84:87], a[188:191], v[152:155]\n"
  "buffer_load_dwordx4 v194, s[8:11], s18 offen lds\n"
  "v_mfma_f32_16x16x32_bf16 v[156:159], v[88:91], a[184:187], v[156:159]\n"
  "ds_read_b128 a[216:219], v226 offset:6144\n"
  "ds_read_b128 a[220:223], v227 offset:6144\n"
  "v_mfma_f32_16x16x32_bf16 v[156:159], v[92:95], a[188:191], v[156:159]\n"
  "s_waitcnt lgkmcnt(4)\n"
  "v_mfma_f32_16x16x32_bf16 v[128:131], v[0:3], a[192:195], v[128:131]\n"
  "v_mfma_f32_16x16x32_bf16 v[128:131], v[4:7], a[196:199], v[128:131]\n"
  "buffer_load_dwordx4 v195, s[8:11], s18 offen offset:1024 lds\n"
  "s_add_u32 m0, s27, 122880\n"
  "v_mfma_f32_16x16x32_bf16 v[132:135], v[8:11], a[192:195], v[132:135]\n"
  "v_mfma_f32_16x16x32_bf16 v[132:135], v[12:15], a[196:199], v[132:135]\n"
  "buffer_load_dwordx4 v194, s[8:11], s19 offen lds\n"
  "s_waitcnt vmcnt(7) lgkmcnt(0)\n"
  "s_barrier\n"
  "v_mfma_f32_16x16x32_bf16 v[136:139], v[0:3], a[200:203], v[136:139]\n"
  "ds_read_b128 v[16:19], v224 offset:40960\n"
  "ds_read_b128 v[20:23], v225 offset:40960\n"
  "v_mfma_f32_16x16x32_bf16 v[136:139], v[4:7], a[204:207], v[136:139]\n"
  "buffer_load_dwordx4 v195, s[8:11], s19 offen offset:1024 lds\n"
  "s_add_u32 m0, s27, 131072\n"
  "v_mfma_f32_16x16x32_bf16 v[140:143], v[8:11], a[200:203], v[140:143]\n"
  "ds_read_b128 a[224:227], v226 offset:24576\n"
  "ds_read_b128 a[228:231], v227 offset:24576\n"
  "v_mfma_f32_16x16x32_bf16 v[140:143], v[12:15], a[204:207], v[140:143]\n"
  "v_mfma_f32_16x16x32_bf16 v[144:147], v[0:3], a[208:211], v[144:147]\n"
  "ds_read_b128 v[24:27], v224 offset:43008\n"
  "ds_read_b128 v[28:31], v225 offset:43008\n"
  "v_mfma_f32_16x16x32_bf16 v[144:147], v[4:7], a[212:215], v[144:147]\n"
  "buffer_load_dwordx4 v194, s[8:11], s31 offen lds\n"
  "v_mfma_f32_16x16x32_bf16 v[148:151], v[8:11], a[208:211], v[148:151]\n"
  "ds_read_b128 a[232:235], v226 offset:26624\n"
  "ds_read_b128 a[236:239], v227 offset:26624\n"
  "v_mfma_f32_16x16x32_bf16 v[148:151], v[12:15], a[212:215], v[148:151]\n"
  "v_mfma_f32_16x16x32_bf16 v[152:155], v[0:3], a[216:219], v[152:155]\n"
  "ds_read_b128 a[240:243], v226 offset:28672\n"
  "ds_read_b128 a[244:247], v227 offset:28672\n"
  "v_mfma_f32_16x16x32_bf16 v[152:155], v[4:7], a[220:223], v[152:155]\n"
  "buffer_load_dwordx4 v195, s[8:11], s31 offen offset:1024 lds\n"
  "s_add_u32 m0, s27, 139264\n"
  "v_mfma_f32_16x16x32_bf16 v[156:159], v[8:11], a[216:219], v[156:159]\n"
  "ds_read_b128 a[248:251], v226 offset:30720\n"
  "ds_read_b128 a[252:255], v227 offset:30720\n"
  "v_mfma_f32_16x16x32_bf16 v[156:159], v[12:15], a[220:223], v[156:159]\n"
  "s_waitcnt lgkmcnt(4)\n"
  "v_mfma_f32_16x16x32_bf16 v[128:131], v[16:19], a[224:227], v[128:131]\n"
  "v_mfma_f32_16x16x32_bf16 v[128:131], v[20:23], a[228:231], v[128:131]\n"
  "buffer_load_dwordx4 v194, s[8:11], s41 offen lds\n"
  "v_mfma_f32_16x16x32_bf16 v[132:135], v[24:27], a[224:227], v[132:135]\n"
  "v_mfma_f32_16x16x32_bf16 v[132:135], v[28:31], a[228:231], v[132:135]\n"
  "s_waitcnt vmcnt(7) lgkmcnt(0)\n"
  "s_barrier\n"
  "v_mfma_f32_16x16x32_bf16 v[136:139], v[16:19], a[232:235], v[136:139]\n"
  "ds_read_b128 v[32:35], v231 offset:0\n"
  "ds_read_b128 v[36:39], v232 offset:0\n"
  "v_mfma_f32_16x16x32_bf16 v[136:139], v[20:23], a[236:239], v[136:139]\n"
  "buffer_load_dwordx4 v195, s[8:11], s41 offen offset:1024 lds\n"
  "s_add_u32 m0, s27, 147456\n"
  "v_mfma_f32_16x16x32_bf16 v[140:143], v[24:27], a[232:235], v[140:143]\n"
  "ds_read_b128 v[40:43], v231 offset:2048\n"
  "ds_read_b128 v[44:47], v232 offset:2048\n"
  "v_mfma_f32_16x16x32_bf16 v[140:143], v[28:31], a[236:239], v[140:143]\n"
  "v_cvt_pk_bf16_f32 v128, v128, v129\n"
  "v_cvt_pk_bf16_f32 v129, v130, v131\n"
  "v_mfma_f32_16x16x32_bf16 v[144:147], v[16:19], a[240:243], v[144:147]\n"
  "ds_read_b128 v[48:51], v231 offset:8192\n"
  "ds_read_b128 v[52:55], v232 offset:8192\n"
  "v_mfma_f32_16x16x32_bf16 v[144:147], v[20:23], a[244:247], v[144:147]\n"
  "buffer_load_dwordx4 v194, s[8:11], s66 offen lds\n"
  "v_cvt_pk_bf16_f32 v130, v132, v133\n"
  "v_mfma_f32_16x16x32_bf16 v[148:151], v[24:27], a[240:243], v[148:151]\n"
  "ds_read_b128 v[56:59], v231 offset:10240\n"
  "ds_read_b128 v[60:63], v232 offset:10240\n"
  "DP1_%=:\n"
  "s_cbranch_execz DP5_%=\n"
  "v_mfma_f32_16x16x32_bf16 v[148:151], v[28:31], a[244:247], v[148:151]\n"
  "v_cvt_pk_bf16_f32 v131, v134, v135\n"
  "ds_write_b128 v212, v[128:131] offset:0\n"
  "v_mfma_f32_16x16x32_bf16 v[152:155], v[16:19], a[248:251], v[152:155]\n"
  "v_cvt_pk_bf16_f32 v136, v136, v137\n"
  "v_cvt_pk_bf16_f32 v137, v138, v139\n"
  "v_mfma_f32_16x16x32_bf16 v[152:155], v[20:23], a[252:255], v[152:155]\n"
  "buffer_store_dwordx4 v[128:131], v211, s[20:23], s42 offen offset:0 sc1\n"
  "v_cvt_pk_bf16_f32 v138, v140, v141\n"
  "v_mfma_f32_16x16x32_bf16 v[156:159], v[24:27], a[248:251], v[156:159]\n"
  "v_cvt_pk_bf16_f32 v139, v142, v143\n"
  "ds_write_b128 v212, v[136:139] offset:1024\n"
  "v_mfma_f32_16x16x32_bf16 v[156:159], v[28:31], a[252:255], v[156:159]\n"
  "v_cvt_pk_bf16_f32 v144, v144, v145\n"
  "s_waitcnt lgkmcnt(6)\n"
  "v_mfma_f32_16x16x32_bf16 v[160:163], v[32:35], a[0:3], 0\n"
  "v_cvt_pk_bf16_f32 v145, v146, v147\n"
  "v_cvt_pk_bf16_f32 v146, v148, v149\n"
  "v_mfma_f32_16x16x32_bf16 v[160:163], v[36:39], a[4:7], v[160:163]\n"
  "s_waitcnt vmcnt(6)\n"
  "s_barrier\n"
  "v_mfma_f32_16x16x32_bf16 v[164:167], v[40:43], a[0:3], 0\n"
  "ds_read_b128 v[64:67], v231 offset:16384\n"
  "ds_read_b128 v[68:71], v232 offset:16384\n"
  "v_mfma_f32_16x16x32_bf16 v[164:167], v[44:47], a[4:7], v[164:167]\n"
  "v_add_u32 v216, 0x9e3779b9, v216\n"
  "ds_read_b128 v[72:75], v231 offset:18432\n"
  "v_mfma_f32_16x16x32_bf16 v[168:171], v[32:35], a[8:11], 0\n"
  "ds_read_b128 v[76:79], v232 offset:18432\n"
  "v_cvt_pk_bf16_f32 v147, v150, v151\n"
  "v_mfma_f32_16x16x32_bf16 v[168:171], v[36:39], a[12:15], v[168:171]\n"
  "buffer_store_dwordx4 v[136:139], v211, s[20:23], s42 offen offset:1024 sc1\n"
  "ds_write_b128 v212, v[144:147] offset:2048\n"
  "v_mfma_f32_16x16x32_bf16 v[172:175], v[40:43], a[8:11], 0\n"
  "ds_read_b128 v[80:83], v231 offset:24576\n"
  "v_cvt_pk_bf16_f32 v152, v152, v153\n"
  "v_mfma_f32_16x16x32_bf16 v[172:175], v[44:47], a[12:15], v[172:175]\n"
  "v_cvt_pk_bf16_f32 v153, v154, v155\n"
  "v_cvt_pk_bf16_f32 v154, v156, v157\n"
  "v_mfma_f32_16x16x32_bf16 v[176:179], v[32:35], a[16:19], 0\n"
  "ds_read_b128 v[84:87], v232 offset:24576\n"
  "v_cvt_pk_bf16_f32 v155, v158, v159\n"
  "v_mfma_f32_16x16x32_bf16 v[176:179], v[36:39], a[20:23], v[176:179]\n"
  "buffer_load_dwordx4 v195, s[8:11], s66 offen offset:1024 lds\n"
  "s_add_u32 m0, s27, 155648\n"
  "v_mfma_f32_16x16x32_bf16 v[180:183], v[40:43], a[16:19], 0\n"
  "ds_read_b128 v[88:91], v231 offset:26624\n"
  "ds_read_b128 v[92:95], v232 offset:26624\n"
  "v_mfma_f32_16x16x32_bf16 v[180:183], v[44:47], a[20:23], v[180:183]\n"
  "ds_write_b128 v212, v[152:155] offset:3072\n"
  "v_mfma_f32_16x16x32_bf16 v[184:187], v[32:35], a[24:27], 0\n"
  "v_mfma_f32_16x16x32_bf16 v[184:187], v[36:39], a[28:31], v[184:187]\n"
  "buffer_store_dwordx4 v[144:147], v211, s[20:23], s42 offen offset:2048 sc1\n"
  "v_mfma_f32_16x16x32_bf16 v[188:191], v[40:43], a[24:27], 0\n"
  "v_mfma_f32_16x16x32_bf16 v[188:191], v[44:47], a[28:31], v[188:191]\n"
  "s_waitcnt lgkmcnt(12)\n"
  "v_mfma_f32_16x16x32_bf16 v[160:163], v[48:51], a[32:35], v[160:163]\n"
  "v_mfma_f32_16x16x32_bf16 v[160:163], v[52:55], a[36:39], v[160:163]\n"
  "s_waitcnt vmcnt(5)\n"
  "s_barrier\n"
  "v_mfma_f32_16x16x32_bf16 v[164:167], v[56:59], a[32:35], v[164:167]\n"
  "ds_read_b128 v[0:3], v231 offset:32768\n"
  "ds_read_b128 v[4:7], v232 offset:32768\n"
  "v_mfma_f32_16x16x32_bf16 v[164:167], v[60:63], a[36:39], v[164:167]\n"
  "ds_read_b128 v[8:11], v231 offset:34816\n"
  "v_mfma_f32_16x16x32_bf16 v[168:171], v[48:51], a[40:43], v[168:171]\n"
  "ds_read_b128 v[12:15], v232 offset:34816\n"
  "v_mfma_f32_16x16x32_bf16 v[168:171], v[52:55], a[44:47], v[168:171]\n"
  "buffer_store_dwordx4 v[152:155], v211, s[20:23], s42 offen offset:3072 sc1\n"
  "v_mfma_f32_16x16x32_bf16 v[172:175], v[56:59], a[40:43], v[172:175]\n"
  "ds_read_b128 v[16:19], v231 offset:40960\n"
  "v_mfma_f32_16x16x32_bf16 v[172:175], v[60:63], a[44:47], v[172:175]\n"
  "v_mfma_f32_16x16x32_bf16 v[176:179], v[48:51], a[48:51], v[176:179]\n"
  "ds_read_b128 v[20:23], v232 offset:40960\n"
  "v_mfma_f32_16x16x32_bf16 v[176:179], v[52:55], a[52:55], v[176:179]\n"
  "buffer_load_dwordx4 v194, s[8:11], s67 offen lds\n"
  "v_mfma_f32_16x16x32_bf16 v[180:183], v[56:59], a[48:51], v[180:183]\n"
  "ds_read_b128 v[24:27], v231 offset:43008\n"
  "ds_read_b128 v[28:31], v232 offset:43008\n"
  "v_mfma_f32_16x16x32_bf16 v[180:183], v[60:63], a[52:55], v[180:183]\n"
  "v_mfma_f32_16x16x32_bf16 v[184:187], v[48:51], a[56:59], v[184:187]\n"
  "v_mfma_f32_16x16x32_bf16 v[184:187], v[52:55], a[60:63], v[184:187]\n"
  "buffer_load_dwordx4 v195, s[8:11], s67 offen offset:1024 lds\n"
  "s_add_u32 m0, s25, 0\n"
  "v_mfma_f32_16x16x32_bf16 v[188:191], v[56:59], a[56:59], v[188:191]\n"
  "s_xor_b32 s26, s44, 0\n"
  "v_mfma_f32_16x16x32_bf16 v[188:191], v[60:63], a[60:63], v[188:191]\n"
  "s_waitcnt lgkmcnt(14)\n"
  "v_mfma_f32_16x16x32_bf16 v[160:163], v[64:67], a[64:67], v[160:163]\n"
  "v_mfma_f32_16x16x32_bf16 v[160:163], v[68:71], a[68:71], v[160:163]\n"
  "buffer_load_dwordx4 v200, s[4:7], s26 offen lds\n"
  "buffer_load_dwordx4 v201, s[4:7], s26 offen offset:1024 lds\n"
  "buffer_load_dwordx4 v202, s[4:7], s26 offen offset:2048 lds\n"
  "buffer_load_dwordx4 v203, s[4:7], s26 offen offset:3072 lds\n"
  "s_add_u32 m0, s25, 4096\n"
  "s_xor_b32 s26, s44, 131072\n"
  "buffer_load_dwordx4 v200, s[4:7], s26 offen lds\n"
  "buffer_load_dwordx4 v201, s[4:7], s26 offen offset:1024 lds\n"
  "buffer_load_dwordx4 v202, s[4:7], s26 offen offset:2048 lds\n"
  "buffer_load_dwordx4 v203, s[4:7], s26 offen offset:3072 lds\n"
  "s_add_u32 m0, s25, 49152\n"
  "s_xor_b32 s26, s44, 128\n"
  "buffer_load_dwordx4 v200, s[4:7], s26 offen lds\n"
  "s_waitcnt vmcnt(9)\n"
  "s_barrier\n"
  "v_mfma_f32_16x16x32_bf16 v[164:167], v[72:75], a[64:67], v[164:167]\n"
  "ds_read_b128 v[32:35], v231 offset:49152\n"
  "v_mfma_f32_16x16x32_bf16 v[164:167], v[76:79], a[68:71], v[164:167]\n"
  "v_mfma_f32_16x16x32_bf16 v[168:171], v[64:67], a[72:75], v[168:171]\n"
  "ds_read_b128 v[36:39], v232 offset:49152\n"
  "v_mfma_f32_16x16x32_bf16 v[168:171], v[68:71], a[76:79], v[168:171]\n"
  "v_mfma_f32_16x16x32_bf16 v[172:175], v[72:75], a[72:75], v[172:175]\n"
  "ds_read_b128 v[40:43], v231 offset:51200\n"
  "v_mfma_f32_16x16x32_bf16 v[172:175], v[76:79], a[76:79], v[172:175]\n"
  "v_mfma_f32_16x16x32_bf16 v[176:179], v[64:67], a[80:83], v[176:179]\n"
  "ds_read_b128 v[44:47], v232 offset:51200\n"
  "v_mfma_f32_16x16x32_bf16 v[176:179], v[68:71], a[84:87], v[176:179]\n"
  "v_mfma_f32_16x16x32_bf16 v[180:183], v[72:75], a[80:83], v[180:183]\n"
  "v_mfma_f32_16x16x32_bf16 v[180:183], v[76:79], a[84:87], v[180:183]\n"
  "v_mfma_f32_16x16x32_bf16 v[184:187], v[64:67], a[88:91], v[184:187]\n"
  "v_mfma_f32_16x16x32_bf16 v[184:187], v[68:71], a[92:95], v[184:187]\n"
  "v_mfma_f32_16x16x32_bf16 v[188:191], v[72:75], a[88:91], v[188:191]\n"
  "v_mfma_f32_16x16x32_bf16 v[188:191], v[76:79], a[92:95], v[188:191]\n"
  "s_waitcnt lgkmcnt(13)\n"
  "v_mfma_f32_16x16x32_bf16 v[160:163], v[80:83], a[96:99], v[160:163]\n"
  "v_mfma_f32_16x16x32_bf16 v[160:163], v[84:87], a[100:103], v[160:163]\n"
  "v_mfma_f32_16x16x32_bf16 v[164:167], v[88:91], a[96:99], v[164:167]\n"
  "ds_read_b128 v[48:51], v231 offset:57344\n"
  "v_mfma_f32_16x16x32_bf16 v[164:167], v[92:95], a[100:103], v[164:167]\n"
  "v_mfma_f32_16x16x32_bf16 v[168:171], v[80:83], a[104:107], v[168:171]\n"
  "ds_read_b128 v[52:55], v232 offset:57344\n"
  "v_mfma_f32_16x16x32_bf16 v[168:171], v[84:87], a[108:111], v[168:171]\n"
  "v_mfma_f32_16x16x32_bf16 v[172:175], v[88:91], a[104:107], v[172:175]\n"
  "ds_read_b128 v[56:59], v231 offset:59392\n"
  "v_mfma_f32_16x16x32_bf16 v[172:175], v[92:95], a[108:111], v[172:175]\n"
  "v_mfma_f32_16x16x32_bf16 v[176:179], v[80:83], a[112:115], v[176:179]\n"
  "ds_read_b128 v[60:63], v232 offset:59392\n"
  "v_mfma_f32_16x16x32_bf16 v[176:179], v[84:87], a[116:119], v[176:179]\n"
  "v_mfma_f32_16x16x32_bf16 v[180:183], v[88:91], a[112:115], v[180:183]\n"
  "v_mfma_f32_16x16x32_bf16 v[180:183], v[92:95], a[116:119], v[180:183]\n"
  "v_mfma_f32_16x16x32_bf16 v[184:187], v[80:83], a[120:123], v[184:187]\n"
  "v_mfma_f32_16x16x32_bf16 v[184:187], v[84:87], a[124:127], v[184:187]\n"
  "v_mfma_f32_16x16x32_bf16 v[188:191], v[88:91], a[120:123], v[188:191]\n"
  "v_mfma_f32_16x16x32_bf16 v[188:191], v[92:95], a[124:127], v[188:191]\n"
  "s_waitcnt lgkmcnt(12)\n"
  "v_mfma_f32_16x16x32_bf16 v[160:163], v[0:3], a[128:131], v[160:163]\n"
  "v_mfma_f32_16x16x32_bf16 v[160:163], v[4:7], a[132:135], v[160:163]\n"
  "v_mfma_f32_16x16x32_bf16 v[164:167], v[8:11], a[128:131], v[164:167]\n"
  "v_mfma_f32_16x16x32_bf16 v[164:167], v[12:15], a[132:135], v[164:167]\n"
  "s_waitcnt lgkmcnt(8)\n"
  "v_mfma_f32_16x16x32_bf16 v[160:163], v[16:19], a[160:163], v[160:163]\n"
  "v_mfma_f32_16x16x32_bf16 v[160:163], v[20:23], a[164:167], v[160:163]\n"
  "v_mfma_f32_16x16x32_bf16 v[164:167], v[24:27], a[160:163], v[164:167]\n"
  "v_mfma_f32_16x16x32_bf16 v[164:167], v[28:31], a[164:167], v[164:167]\n"
  "buffer_store_dwordx2 v[216:217], v215, s[20:23], s28 offen offset:0 sc1\n"
  "s_add_u32 s28, s28, 53248\n"
  "s_waitcnt lgkmcnt(4)\n"
  "v_mfma_f32_16x16x32_bf16 v[160:163], v[32:35], a[192:195], v[160:163]\n"
  "v_mfma_f32_16x16x32_bf16 v[160:163], v[36:39], a[196:199], v[160:163]\n"
  "v_mfma_f32_16x16x32_bf16 v[164:167], v[40:43], a[192:195], v[164:167]\n"
  "v_mfma_f32_16x16x32_bf16 v[164:167], v[44:47], a[196:199], v[164:167]\n"
  "buffer_load_dwordx4 v201, s[4:7], s26 offen offset:1024 lds\n"
  "s_waitcnt lgkmcnt(0)\n"
  "v_mfma_f32_16x16x32_bf16 v[160:163], v[48:51], a[224:227], v[160:163]\n"
  "v_mfma_f32_16x16x32_bf16 v[160:163], v[52:55], a[228:231], v[160:163]\n"
  "buffer_load_dwordx4 v202, s[4:7], s26 offen offset:2048 lds\n"
  "buffer_load_dwordx4 v203, s[4:7], s26 offen offset:3072 lds\n"
  "s_add_u32 m0, s25, 53248\n"
  "s_xor_b32 s26, s44, 131200\n"
  "buffer_load_dwordx4 v200, s[4:7], s26 offen lds\n"
  "buffer_load_dwordx4 v201, s[4:7], s26 offen offset:1024 lds\n"
  "buffer_load_dwordx4 v202, s[4:7], s26 offen offset:2048 lds\n"
  "buffer_load_dwordx4 v203, s[4:7], s26 offen offset:3072 lds\n"
  "s_waitcnt vmcnt(9)\n"
  "s_barrier\n"
  "v_mfma_f32_16x16x32_bf16 v[164:167], v[56:59], a[224:227], v[164:167]\n"
  "ds_read_b128 a[0:3], v210 offset:32768\n"
  "s_add_u32 m0, s25, 98304\n"
  "v_mfma_f32_16x16x32_bf16 v[164:167], v[60:63], a[228:231], v[164:167]\n"
  "ds_read_b128 a[4:7], v210 offset:40960\n"
  "s_xor_b32 s26, s45, 0\n"
  "v_mfma_f32_16x16x32_bf16 v[168:171], v[0:3], a[136:139], v[168:171]\n"
  "ds_read_b128 a[8:11], v210 offset:33792\n"
  "v_mfma_f32_16x16x32_bf16 v[168:171], v[4:7], a[140:143], v[168:171]\n"
  "v_mfma_f32_16x16x32_bf16 v[172:175], v[8:11], a[136:139], v[172:175]\n"
  "ds_read_b128 a[12:15], v210 offset:41984\n"
  "v_mfma_f32_16x16x32_bf16 v[172:175], v[12:15], a[140:143], v[172:175]\n"
  "buffer_load_dwordx4 v200, s[4:7], s26 offen lds\n"
  "v_cvt_pk_bf16_f32 v160, v160, v161\n"
  "v_mfma_f32_16x16x32_bf16 v[168:171], v[16:19], a[168:171], v[168:171]\n"
  "ds_read_b128 a[16:19], v210 offset:34816\n"
  "v_cvt_pk_bf16_f32 v161, v162, v163\n"
  "v_mfma_f32_16x16x32_bf16 v[168:171], v[20:23], a[172:175], v[168:171]\n"
  "v_cvt_pk_bf16_f32 v162, v164, v165\n"
  "v_cvt_pk_bf16_f32 v163, v166, v167\n"
  "v_mfma_f32_16x16x32_bf16 v[172:175], v[24:27], a[168:171], v[172:175]\n"
  "ds_read_b128 a[20:23], v210 offset:43008\n"
  "ds_write_b128 v212, v[160:163] offset:49152\n"
  "v_mfma_f32_16x16x32_bf16 v[172:175], v[28:31], a[172:175], v[172:175]\n"
  "buffer_store_dwordx4 v[160:163], v211, s[20:23], s43 offen offset:0 sc1\n"
  "v_mfma_f32_16x16x32_bf16 v[168:171], v[32:35], a[200:203], v[168:171]\n"
  "ds_read_b128 a[24:27], v210 offset:35840\n"
  "v_mfma_f32_16x16x32_bf16 v[168:171], v[36:39], a[204:207], v[168:171]\n"
  "v_mfma_f32_16x16x32_bf16 v[172:175], v[40:43], a[200:203], v[172:175]\n"
  "ds_read_b128 a[28:31], v210 offset:44032\n"
  "v_mfma_f32_16x16x32_bf16 v[172:175], v[44:47], a[204:207], v[172:175]\n"
  "v_mfma_f32_16x16x32_bf16 v[168:171], v[48:51], a[232:235], v[168:171]\n"
  "ds_read_b128 a[32:35], v208 offset:0\n"
  "v_mfma_f32_16x16x32_bf16 v[168:171], v[52:55], a[236:239], v[168:171]\n"
  "v_mfma_f32_16x16x32_bf16 v[172:175], v[56:59], a[232:235], v[172:175]\n"
  "ds_read_b128 a[36:39], v209 offset:0\n"
  "v_mfma_f32_16x16x32_bf16 v[172:175], v[60:63], a[236:239], v[172:175]\n"
  "v_mfma_f32_16x16x32_bf16 v[176:179], v[0:3], a[144:147], v[176:179]\n"
  "ds_read_b128 a[40:43], v208 offset:2048\n"
  "v_mfma_f32_16x16x32_bf16 v[176:179], v[4:7], a[148:151], v[176:179]\n"
  "v_mfma_f32_16x16x32_bf16 v[180:183], v[8:11], a[144:147], v[180:183]\n"
  "ds_read_b128 a[44:47], v209 offset:2048\n"
  "v_mfma_f32_16x16x32_bf16 v[180:183], v[12:15], a[148:151], v[180:183]\n"
  "buffer_load_dwordx4 v201, s[4:7], s26 offen offset:1024 lds\n"
  "v_cvt_pk_bf16_f32 v168, v168, v169\n"
  "v_mfma_f32_16x16x32_bf16 v[176:179], v[16:19], a[176:179], v[176:179]\n"
  "ds_read_b128 a[48:51], v208 offset:4096\n"
  "v_cvt_pk_bf16_f32 v169, v170, v171\n"
  "v_mfma_f32_16x16x32_bf16 v[176:179], v[20:23], a[180:183], v[176:179]\n"
  "v_cvt_pk_bf16_f32 v170, v172, v173\n"
  "v_cvt_pk_bf16_f32 v171, v174, v175\n"
  "v_mfma_f32_16x16x32_bf16 v[180:183], v[24:27], a[176:179], v[180:183]\n"
  "ds_read_b128 a[52:55], v209 offset:4096\n"
  "ds_write_b128 v212, v[168:171] offset:50176\n"
  "v_mfma_f32_16x16x32_bf16 v[180:183], v[28:31], a[180:183], v[180:183]\n"
  "buffer_store_dwordx4 v[168:171], v211, s[20:23], s43 offen offset:1024 sc1\n"
  "v_mfma_f32_16x16x32_bf16 v[176:179], v[32:35], a[208:211], v[176:179]\n"
  "ds_read_b128 a[56:59], v208 offset:6144\n"
  "v_mfma_f32_16x16x32_bf16 v[176:179], v[36:39], a[212:215], v[176:179]\n"
  "v_mfma_f32_16x16x32_bf16 v[180:183], v[40:43], a[208:211], v[180:183]\n"
  "ds_read_b128 a[60:63], v209 offset:6144\n"
  "v_mfma_f32_16x16x32_bf16 v[180:183], v[44:47], a[212:215], v[180:183]\n"
  "v_mfma_f32_16x16x32_bf16 v[176:179], v[48:51], a[240:243], v[176:179]\n"
  "ds_read_b128 a[64:67], v208 offset:8192\n"
  "v_mfma_f32_16x16x32_bf16 v[176:179], v[52:55], a[244:247], v[176:179]\n"
  "buffer_load_dwordx4 v202, s[4:7], s26 offen offset:2048 lds\n"
  "v_mfma_f32_16x16x32_bf16 v[180:183], v[56:59], a[240:243], v[180:183]\n"
  "ds_read_b128 a[68:71], v209 offset:8192\n"
  "v_mfma_f32_16x16x32_bf16 v[180:183], v[60:63], a[244:247], v[180:183]\n"
  "v_mfma_f32_16x16x32_bf16 v[184:187], v[0:3], a[152:155], v[184:187]\n"
  "ds_read_b128 a[72:75], v208 offset:10240\n"
  "v_mfma_f32_16x16x32_bf16 v[184:187], v[4:7], a[156:159], v[184:187]\n"
  "buffer_load_dwordx4 v203, s[4:7], s26 offen offset:3072 lds\n"
  "s_add_u32 m0, s25, 102400\n"
  "v_mfma_f32_16x16x32_bf16 v[188:191], v[8:11], a[152:155], v[188:191]\n"
  "ds_read_b128 a[76:79], v209 offset:10240\n"
  "s_xor_b32 s26, s45, 131072\n"
  "v_mfma_f32_16x16x32_bf16 v[188:191], v[12:15], a[156:159], v[188:191]\n"
  "v_cvt_pk_bf16_f32 v176, v176, v177\n"
  "v_cvt_pk_bf16_f32 v177, v178, v179\n"
  "v_mfma_f32_16x16x32_bf16 v[184:187], v[16:19], a[184:187], v[184:187]\n"
  "ds_read_b128 a[80:83], v208 offset:12288\n"
  "v_cvt_pk_bf16_f32 v178, v180, v181\n"
  "v_mfma_f32_16x16x32_bf16 v[184:187], v[20:23], a[188:191], v[184:187]\n"
  "buffer_load_dwordx4 v200, s[4:7], s26 offen lds\n"
  "v_cvt_pk_bf16_f32 v179, v182, v183\n"
  "v_mfma_f32_16x16x32_bf16 v[188:191], v[24:27], a[184:187], v[188:191]\n"
  "ds_read_b128 a[84:87], v209 offset:12288\n"
  "ds_write_b128 v212, v[176:179] offset:51200\n"
  "v_mfma_f32_16x16x32_bf16 v[188:191], v[28:31], a[188:191], v[188:191]\n"
  "buffer_load_dwordx2 v[218:219], v214, s[20:23], s29 offen sc1\n"
  "v_mfma_f32_16x16x32_bf16 v[184:187], v[32:35], a[216:219], v[184:187]\n"
  "ds_read_b128 a[88:91], v208 offset:14336\n"
  "ds_read_b128 a[92:95], v209 offset:14336\n"
  "v_mfma_f32_16x16x32_bf16 v[184:187], v[36:39], a[220:223], v[184:187]\n"
  "v_mfma_f32_16x16x32_bf16 v[188:191], v[40:43], a[216:219], v[188:191]\n"
  "v_mfma_f32_16x16x32_bf16 v[188:191], v[44:47], a[220:223], v[188:191]\n"
  "buffer_store_dwordx4 v[176:179], v211, s[20:23], s43 offen offset:2048 sc1\n"
  "v_mfma_f32_16x16x32_bf16 v[184:187], v[48:51], a[248:251], v[184:187]\n"
  "v_mfma_f32_16x16x32_bf16 v[184:187], v[52:55], a[252:255], v[184:187]\n"
  "v_mfma_f32_16x16x32_bf16 v[188:191], v[56:59], a[248:251], v[188:191]\n"
  "v_mfma_f32_16x16x32_bf16 v[188:191], v[60:63], a[252:255], v[188:191]\n"
  "s_waitcnt lgkmcnt(0)\n"
  "s_barrier\n"
  "v_mfma_f32_16x16x32_bf16 v[0:3], a[0:3], a[32:35], 0\n"
  "buffer_load_dwordx4 v201, s[4:7], s26 offen offset:1024 lds\n"
  "v_mfma_f32_16x16x32_bf16 v[0:3], a[4:7], a[36:39], v[0:3]\n"
  "v_mfma_f32_16x16x32_bf16 v[4:7], a[8:11], a[32:35], 0\n"
  "v_mfma_f32_16x16x32_bf16 v[4:7], a[12:15], a[36:39], v[4:7]\n"
  "v_cvt_pk_bf16_f32 v184, v184, v185\n"
  "v_cvt_pk_bf16_f32 v185, v186, v187\n"
  "v_mfma_f32_16x16x32_bf16 v[8:11], a[16:19], a[32:35], 0\n"
  "buffer_load_dwordx4 v202, s[4:7], s26 offen offset:2048 lds\n"
  "v_cvt_pk_bf16_f32 v186, v188, v189\n"
  "v_mfma_f32_16x16x32_bf16 v[8:11], a[20:23], a[36:39], v[8:11]\n"
  "v_cvt_pk_bf16_f32 v187, v190, v191\n"
  "ds_write_b128 v212, v[184:187] offset:52224\n"
  "v_mfma_f32_16x16x32_bf16 v[12:15], a[24:27], a[32:35], 0\n"
  "v_mfma_f32_16x16x32_bf16 v[12:15], a[28:31], a[36:39], v[12:15]\n"
  "v_mfma_f32_16x16x32_bf16 v[16:19], a[0:3], a[40:43], 0\n"
  "buffer_store_dwordx4 v[184:187], v211, s[20:23], s43 offen offset:3072 sc1\n"
  "v_mfma_f32_16x16x32_bf16 v[16:19], a[4:7], a[44:47], v[16:19]\n"
  "v_mfma_f32_16x16x32_bf16 v[20:23], a[8:11], a[40:43], 0\n"
  "v_mfma_f32_16x16x32_bf16 v[20:23], a[12:15], a[44:47], v[20:23]\n"
  "v_mfma_f32_16x16x32_bf16 v[24:27], a[16:19], a[40:43], 0\n"
  "buffer_load_dwordx4 v203, s[4:7], s26 offen offset:3072 lds\n"
  "s_add_u32 m0, s25, 0\n"
  "v_mfma_f32_16x16x32_bf16 v[24:27], a[20:23], a[44:47], v[24:27]\n"
  "s_xor_b32 s26, s46, 0\n"
  "v_mfma_f32_16x16x32_bf16 v[28:31], a[24:27], a[40:43], 0\n"
  "v_mfma_f32_16x16x32_bf16 v[28:31], a[28:31], a[44:47], v[28:31]\n"
  "v_mfma_f32_16x16x32_bf16 v[32:35], a[0:3], a[48:51], 0\n"
  "buffer_load_dwordx4 v200, s[4:7], s26 offen lds\n"
  "v_mfma_f32_16x16x32_bf16 v[32:35], a[4:7], a[52:55], v[32:35]\n"
  "v_mfma_f32_16x16x32_bf16 v[36:39], a[8:11], a[48:51], 0\n"
  "v_mfma_f32_16x16x32_bf16 v[36:39], a[12:15], a[52:55], v[36:39]\n"
  "v_mfma_f32_16x16x32_bf16 v[40:43], a[16:19], a[48:51], 0\n"
  "buffer_load_dwordx4 v201, s[4:7], s26 offen offset:1024 lds\n"
  "v_mfma_f32_16x16x32_bf16 v[40:43], a[20:23], a[52:55], v[40:43]\n"
  "v_mfma_f32_16x16x32_bf16 v[44:47], a[24:27], a[48:51], 0\n"
  "v_mfma_f32_16x16x32_bf16 v[44:47], a[28:31], a[52:55], v[44:47]\n"
  "v_mfma_f32_16x16x32_bf16 v[48:51], a[0:3], a[56:59], 0\n"
  "buffer_load_dwordx4 v202, s[4:7], s26 offen offset:2048 lds\n"
  "v_mfma_f32_16x16x32_bf16 v[48:51], a[4:7], a[60:63], v[48:51]\n"
  "v_mfma_f32_16x16x32_bf16 v[52:55], a[8:11], a[56:59], 0\n"
  "v_mfma_f32_16x16x32_bf16 v[52:55], a[12:15], a[60:63], v[52:55]\n"
  "v_mfma_f32_16x16x32_bf16 v[56:59], a[16:19], a[56:59], 0\n"
  "buffer_load_dwordx4 v203, s[4:7], s26 offen offset:3072 lds\n"
  "s_add_u32 m0, s25, 4096\n"
  "v_mfma_f32_16x16x32_bf16 v[56:59], a[20:23], a[60:63], v[56:59]\n"
  "s_waitcnt vmcnt(17) lgkmcnt(0)\n"
  "s_barrier\n"
  "v_mfma_f32_16x16x32_bf16 v[60:63], a[24:27], a[56:59], 0\n"
  "ds_read_b128 a[96:99], v230 offset:32768\n"
  "ds_read_b128 a[100:103], v230 offset:40960\n"
  "v_mfma_f32_16x16x32_bf16 v[60:63], a[28:31], a[60:63], v[60:63]\n"
  "s_xor_b32 s26, s46, 131072\n"
  "v_mfma_f32_16x16x32_bf16 v[64:67], a[0:3], a[64:67], 0\n"
  "ds_read_b128 a[104:107], v230 offset:33792\n"
  "ds_read_b128 a[108:111], v230 offset:41984\n"
  "v_mfma_f32_16x16x32_bf16 v[64:67], a[4:7], a[68:71], v[64:67]\n"
  "v_mfma_f32_16x16x32_bf16 v[68:71], a[8:11], a[64:67], 0\n"
  "ds_read_b128 a[112:115], v230 offset:34816\n"
  "ds_read_b128 a[116:119], v230 offset:43008\n"
  "v_mfma_f32_16x16x32_bf16 v[68:71], a[12:15], a[68:71], v[68:71]\n"
  "v_mfma_f32_16x16x32_bf16 v[72:75], a[16:19], a[64:67], 0\n"
  "ds_read_b128 a[120:123], v230 offset:35840\n"
  "ds_read_b128 a[124:127], v230 offset:44032\n"
  "v_mfma_f32_16x16x32_bf16 v[72:75], a[20:23], a[68:71], v[72:75]\n"
  "v_mfma_f32_16x16x32_bf16 v[76:79], a[24:27], a[64:67], 0\n"
  "ds_read_b128 a[128:131], v228 offset:0\n"
  "ds_read_b128 a[132:135], v229 offset:0\n"
  "v_mfma_f32_16x16x32_bf16 v[76:79], a[28:31], a[68:71], v[76:79]\n"
  "ds_read_b128 a[136:139], v228 offset:2048\n"
  "v_mfma_f32_16x16x32_bf16 v[80:83], a[0:3], a[72:75], 0\n"
  "ds_read_b128 a[140:143], v229 offset:2048\n"
  "v_mfma_f32_16x16x32_bf16 v[80:83], a[4:7], a[76:79], v[80:83]\n"
  "buffer_load_dwordx4 v200, s[4:7], s26 offen lds\n"
  "v_mfma_f32_16x16x32_bf16 v[84:87], a[8:11], a[72:75], 0\n"
  "ds_read_b128 a[144:147], v228 offset:4096\n"
  "v_mfma_f32_16x16x32_bf16 v[84:87], a[12:15], a[76:79], v[84:87]\n"
  "s_waitcnt vmcnt(6)\n"
  "buffer_store_dwordx2 v[216:217], v215, s[20:23], s28 offen offset:0 sc1\n"
  "v_mfma_f32_16x16x32_bf16 v[88:91], a[16:19], a[72:75], 0\n"
  "ds_read_b128 a[148:151], v229 offset:4096\n"
  "ds_read_b128 a[152:155], v228 offset:6144\n"
  "v_mfma_f32_16x16x32_bf16 v[88:91], a[20:23], a[76:79], v[88:91]\n"
  "v_mfma_f32_16x16x32_bf16 v[92:95], a[24:27], a[72:75], 0\n"
  "ds_read_b128 a[156:159], v229 offset:6144\n"
  "v_mfma_f32_16x16x32_bf16 v[92:95], a[28:31], a[76:79], v[92:95]\n"
  "v_cmp_ne_u64 vcc, v[218:219], v[216:217]\n"
  "s_cbranch_vccnz SLA_%=\n"
  "BKA_%=:\n"
  "s_add_u32 s29, s29, 53248\n"
  "v_mfma_f32_16x16x32_bf16 v[96:99], a[0:3], a[80:83], 0\n"
  "ds_read_b128 a[160:163], v228 offset:8192\n"
  "ds_read_b128 a[164:167], v229 offset:8192\n"
  "v_mfma_f32_16x16x32_bf16 v[96:99], a[4:7], a[84:87], v[96:99]\n"
  "buffer_load_dwordx4 v201, s[4:7], s26 offen offset:1024 lds\n"
  "v_mfma_f32_16x16x32_bf16 v[100:103], a[8:11], a[80:83], 0\n"
  "ds_read_b128 a[168:171], v228 offset:10240\n"
  "v_mfma_f32_16x16x32_bf16 v[100:103], a[12:15], a[84:87], v[100:103]\n"
  "ds_read_b128 a[172:175], v229 offset:10240\n"
  "v_mfma_f32_16x16x32_bf16 v[104:107], a[16:19], a[80:83], 0\n"
  "ds_read_b128 a[176:179], v228 offset:12288\n"
  "v_mfma_f32_16x16x32_bf16 v[104:107], a[20:23], a[84:87], v[104:107]\n"
  "buffer_load_dwordx4 v202, s[4:7], s26 offen offset:2048 lds\n"
  "v_mfma_f32_16x16x32_bf16 v[108:111], a[24:27], a[80:83], 0\n"
  "ds_read_b128 a[180:183], v229 offset:12288\n"
  "v_mfma_f32_16x16x32_bf16 v[108:111], a[28:31], a[84:87], v[108:111]\n"
  "v_mfma_f32_16x16x32_bf16 v[112:115], a[0:3], a[88:91], 0\n"
  "ds_read_b128 a[184:187], v228 offset:14336\n"
  "ds_read_b128 a[188:191], v229 offset:14336\n"
  "v_mfma_f32_16x16x32_bf16 v[112:115], a[4:7], a[92:95], v[112:115]\n"
  "buffer_load_dwordx4 v203, s[4:7], s26 offen offset:3072 lds\n"
  "s_add_u32 m0, s24, 131072\n"
  "v_mfma_f32_16x16x32_bf16 v[116:119], a[8:11], a[88:91], 0\n"
  "s_xor_b32 s26, s53, 0\n"
  "v_mfma_f32_16x16x32_bf16 v[116:119], a[12:15], a[92:95], v[116:119]\n"
  "v_mfma_f32_16x16x32_bf16 v[120:123], a[16:19], a[88:91], 0\n"
  "v_mfma_f32_16x16x32_bf16 v[120:123], a[20:23], a[92:95], v[120:123]\n"
  "buffer_load_dwordx4 v211, s[20:23], s26 offen sc1 lds\n"
  "v_mfma_f32_16x16x32_bf16 v[124:127], a[24:27], a[88:91], 0\n"
  "v_mfma_f32_16x16x32_bf16 v[124:127], a[28:31], a[92:95], v[124:127]\n"
  "s_waitcnt vmcnt(10) lgkmcnt(0)\n"
  "s_barrier\n"
  "v_mfma_f32_16x16x32_bf16 v[0:3], a[96:99], a[128:131], v[0:3]\n"
  "ds_read_b128 a[32:35], v235 offset:0\n"
  "v_mfma_f32_16x16x32_bf16 v[0:3], a[100:103], a[132:135], v[0:3]\n"
  "buffer_load_dwordx4 v211, s[20:23], s26 offen offset:1024 sc1 lds\n"
  "v_mfma_f32_16x16x32_bf16 v[4:7], a[104:107], a[128:131], v[4:7]\n"
  "ds_read_b128 a[36:39], v236 offset:0\n"
  "v_mfma_f32_16x16x32_bf16 v[4:7], a[108:111], a[132:135], v[4:7]\n"
  "v_mfma_f32_16x16x32_bf16 v[8:11], a[112:115], a[128:131], v[8:11]\n"
  "ds_read_b128 a[40:43], v235 offset:2048\n"
  "v_mfma_f32_16x16x32_bf16 v[8:11], a[116:119], a[132:135], v[8:11]\n"
  "buffer_load_dwordx4 v211, s[20:23], s26 offen offset:2048 sc1 lds\n"
  "v_mfma_f32_16x16x32_bf16 v[12:15], a[120:123], a[128:131], v[12:15]\n"
  "ds_read_b128 a[44:47], v236 offset:2048\n"
  "v_mfma_f32_16x16x32_bf16 v[12:15], a[124:127], a[132:135], v[12:15]\n"
  "v_mfma_f32_16x16x32_bf16 v[16:19], a[96:99], a[136:139], v[16:19]\n"
  "ds_read_b128 a[48:51], v235 offset:4096\n"
  "v_mfma_f32_16x16x32_bf16 v[16:19], a[100:103], a[140:143], v[16:19]\n"
  "buffer_load_dwordx4 v211, s[20:23], s26 offen offset:3072 sc1 lds\n"
  "s_add_u32 m0, s24, 32768\n"
  "v_mfma_f32_16x16x32_bf16 v[20:23], a[104:107], a[136:139], v[20:23]\n"
  "ds_read_b128 a[52:55], v236 offset:4096\n"
  "s_xor_b32 s26, s54, 0\n"
  "v_mfma_f32_16x16x32_bf16 v[20:23], a[108:111], a[140:143], v[20:23]\n"
  "v_mfma_f32_16x16x32_bf16 v[24:27], a[112:115], a[136:139], v[24:27]\n"
  "ds_read_b128 a[56:59], v235 offset:6144\n"
  "v_mfma_f32_16x16x32_bf16 v[24:27], a[116:119], a[140:143], v[24:27]\n"
  "buffer_load_dwordx4 v211, s[20:23], s26 offen sc1 lds\n"
  "v_mfma_f32_16x16x32_bf16 v[28:31], a[120:123], a[136:139], v[28:31]\n"
  "ds_read_b128 a[60:63], v236 offset:6144\n"
  "v_mfma_f32_16x16x32_bf16 v[28:31], a[124:127], a[140:143], v[28:31]\n"
  "v_mfma_f32_16x16x32_bf16 v[32:35], a[96:99], a[144:147], v[32:35]\n"
  "ds_read_b128 a[64:67], v235 offset:8192\n"
  "v_mfma_f32_16x16x32_bf16 v[32:35], a[100:103], a[148:151], v[32:35]\n"
  "buffer_load_dwordx4 v211, s[20:23], s26 offen offset:1024 sc1 lds\n"
  "v_mfma_f32_16x16x32_bf16 v[36:39], a[104:107], a[144:147], v[36:39]\n"
  "ds_read_b128 a[68:71], v236 offset:8192\n"
  "v_mfma_f32_16x16x32_bf16 v[36:39], a[108:111], a[148:151], v[36:39]\n"
  "v_mfma_f32_16x16x32_bf16 v[40:43], a[112:115], a[144:147], v[40:43]\n"
  "ds_read_b128 a[72:75], v235 offset:10240\n"
  "v_mfma_f32_16x16x32_bf16 v[40:43], a[116:119], a[148:151], v[40:43]\n"
  "buffer_load_dwordx4 v211, s[20:23], s26 offen offset:2048 sc1 lds\n"
  "v_mfma_f32_16x16x32_bf16 v[44:47], a[120:123], a[144:147], v[44:47]\n"
  "ds_read_b128 a[76:79], v236 offset:10240\n"
  "v_mfma_f32_16x16x32_bf16 v[44:47], a[124:127], a[148:151], v[44:47]\n"
  "v_mfma_f32_16x16x32_bf16 v[48:51], a[96:99], a[152:155], v[48:51]\n"
  "ds_read_b128 a[80:83], v235 offset:12288\n"
  "v_mfma_f32_16x16x32_bf16 v[48:51], a[100:103], a[156:159], v[48:51]\n"
  "buffer_load_dwordx4 v211, s[20:23], s26 offen offset:3072 sc1 lds\n"
  "s_add_u32 m0, s25, 49152\n"
  "v_mfma_f32_16x16x32_bf16 v[52:55], a[104:107], a[152:155], v[52:55]\n"
  "ds_read_b128 a[84:87], v236 offset:12288\n"
  "s_xor_b32 s26, s45, 128\n"
  "v_mfma_f32_16x16x32_bf16 v[52:55], a[108:111], a[156:159], v[52:55]\n"
  "v_mfma_f32_16x16x32_bf16 v[56:59], a[112:115], a[152:155], v[56:59]\n"
  "ds_read_b128 a[88:91], v235 offset:14336\n"
  "v_mfma_f32_16x16x32_bf16 v[56:59], a[116:119], a[156:159], v[56:59]\n"
  "buffer_load_dwordx2 v[218:219], v214, s[20:23], s29 offen sc1\n"
  "v_mfma_f32_16x16x32_bf16 v[60:63], a[120:123], a[152:155], v[60:63]\n"
  "ds_read_b128 a[92:95], v236 offset:14336\n"
  "v_mfma_f32_16x16x32_bf16 v[60:63], a[124:127], a[156:159], v[60:63]\n"
  "v_mfma_f32_16x16x32_bf16 v[64:67], a[96:99], a[160:163], v[64:67]\n"
  "v_mfma_f32_16x16x32_bf16 v[64:67], a[100:103], a[164:167], v[64:67]\n"
  "v_mfma_f32_16x16x32_bf16 v[68:71], a[104:107], a[160:163], v[68:71]\n"
  "v_mfma_f32_16x16x32_bf16 v[68:71], a[108:111], a[164:167], v[68:71]\n"
  "v_mfma_f32_16x16x32_bf16 v[72:75], a[112:115], a[160:163], v[72:75]\n"
  "v_mfma_f32_16x16x32_bf16 v[72:75], a[116:119], a[164:167], v[72:75]\n"
  "buffer_load_dwordx4 v200, s[4:7], s26 offen lds\n"
  "v_mfma_f32_16x16x32_bf16 v[76:79], a[120:123], a[160:163], v[76:79]\n"
  "v_mfma_f32_16x16x32_bf16 v[76:79], a[124:127], a[164:167], v[76:79]\n"
  "v_mfma_f32_16x16x32_bf16 v[80:83], a[96:99], a[168:171], v[80:83]\n"
  "v_mfma_f32_16x16x32_bf16 v[80:83], a[100:103], a[172:175], v[80:83]\n"
  "buffer_load_dwordx4 v201, s[4:7], s26 offen offset:1024 lds\n"
  "v_mfma_f32_16x16x32_bf16 v[84:87], a[104:107], a[168:171], v[84:87]\n"
  "v_mfma_f32_16x16x32_bf16 v[84:87], a[108:111], a[172:175], v[84:87]\n"
  "v_mfma_f32_16x16x32_bf16 v[88:91], a[112:115], a[168:171], v[88:91]\n"
  "v_mfma_f32_16x16x32_bf16 v[88:91], a[116:119], a[172:175], v[88:91]\n"
  "buffer_load_dwordx4 v202, s[4:7], s26 offen offset:2048 lds\n"
  "buffer_load_dwordx4 v203, s[4:7], s26 offen offset:3072 lds\n"
  "s_waitcnt vmcnt(9)\n"
  "s_barrier\n"
  "v_mfma_f32_16x16x32_bf16 v[92:95], a[120:123], a[168:171], v[92:95]\n"
  "ds_read_b128 a[0:3], v237 offset:32768\n"
  "s_add_u32 m0, s25, 53248\n"
  "v_mfma_f32_16x16x32_bf16 v[92:95], a[124:127], a[172:175], v[92:95]\n"
  "ds_read_b128 a[4:7], v237 offset:40960\n"
  "s_xor_b32 s26, s45, 131200\n"
  "v_mfma_f32_16x16x32_bf16 v[96:99], a[96:99], a[176:179], v[96:99]\n"
  "ds_read_b128 a[8:11], v237 offset:33792\n"
  "v_mfma_f32_16x16x32_bf16 v[96:99], a[100:103], a[180:183], v[96:99]\n"
  "v_mfma_f32_16x16x32_bf16 v[100:103], a[104:107], a[176:179], v[100:103]\n"
  "ds_read_b128 a[12:15], v237 offset:41984\n"
  "v_mfma_f32_16x16x32_bf16 v[100:103], a[108:111], a[180:183], v[100:103]\n"
  "v_mfma_f32_16x16x32_bf16 v[104:107], a[112:115], a[176:179], v[104:107]\n"
  "ds_read_b128 a[16:19], v237 offset:34816\n"
  "v_mfma_f32_16x16x32_bf16 v[104:107], a[116:119], a[180:183], v[104:107]\n"
  "buffer_load_dwordx4 v200, s[4:7], s26 offen lds\n"
  "v_mfma_f32_16x16x32_bf16 v[108:111], a[120:123], a[176:179], v[108:111]\n"
  "ds_read_b128 a[20:23], v237 offset:43008\n"
  "v_mfma_f32_16x16x32_bf16 v[108:111], a[124:127], a[180:183], v[108:111]\n"
  "v_mfma_f32_16x16x32_bf16 v[112:115], a[96:99], a[184:187], v[112:115]\n"
  "ds_read_b128 a[24:27], v237 offset:35840\n"
  "ds_read_b128 a[28:31], v237 offset:44032\n"
  "v_mfma_f32_16x16x32_bf16 v[112:115], a[100:103], a[188:191], v[112:115]\n"
  "buffer_load_dwordx4 v201, s[4:7], s26 offen offset:1024 lds\n"
  "v_mfma_f32_16x16x32_bf16 v[116:119], a[104:107], a[184:187], v[116:119]\n"
  "v_mfma_f32_16x16x32_bf16 v[116:119], a[108:111], a[188:191], v[116:119]\n"
  "v_mfma_f32_16x16x32_bf16 v[120:123], a[112:115], a[184:187], v[120:123]\n"
  "v_mfma_f32_16x16x32_bf16 v[120:123], a[116:119], a[188:191], v[120:123]\n"
  "buffer_load_dwordx4 v202, s[4:7], s26 offen offset:2048 lds\n"
  "v_mfma_f32_16x16x32_bf16 v[124:127], a[120:123], a[184:187], v[124:127]\n"
  "v_mfma_f32_16x16x32_bf16 v[124:127], a[124:127], a[188:191], v[124:127]\n"
  "s_waitcnt vmcnt(8) lgkmcnt(0)\n"
  "s_barrier\n"
  "v_mfma_f32_16x16x32_bf16 v[0:3], a[0:3], a[32:35], v[0:3]\n"
  "ds_read_b128 a[96:99], v210 offset:32768\n"
  "v_mfma_f32_16x16x32_bf16 v[0:3], a[4:7], a[36:39], v[0:3]\n"
  "buffer_load_dwordx4 v203, s[4:7], s26 offen offset:3072 lds\n"
  "s_add_u32 m0, s25, 98304\n"
  "v_mfma_f32_16x16x32_bf16 v[4:7], a[8:11], a[32:35], v[4:7]\n"
  "ds_read_b128 a[100:103], v210 offset:40960\n"
  "s_xor_b32 s26, s46, 128\n"
  "v_mfma_f32_16x16x32_bf16 v[4:7], a[12:15], a[36:39], v[4:7]\n"
  "v_mfma_f32_16x16x32_bf16 v[8:11], a[16:19], a[32:35], v[8:11]\n"
  "ds_read_b128 a[104:107], v210 offset:33792\n"
  "v_mfma_f32_16x16x32_bf16 v[8:11], a[20:23], a[36:39], v[8:11]\n"
  "v_mfma_f32_16x16x32_bf16 v[12:15], a[24:27], a[32:35], v[12:15]\n"
  "ds_read_b128 a[108:111], v210 offset:41984\n"
  "v_mfma_f32_16x16x32_bf16 v[12:15], a[28:31], a[36:39], v[12:15]\n"
  "v_mfma_f32_16x16x32_bf16 v[16:19], a[0:3], a[40:43], v[16:19]\n"
  "ds_read_b128 a[112:115], v210 offset:34816\n"
  "DP2_%=:\n"
  "s_cbranch_execz DP6_%=\n"
  "v_mfma_f32_16x16x32_bf16 v[16:19], a[4:7], a[44:47], v[16:19]\n"
  "v_mfma_f32_16x16x32_bf16 v[20:23], a[8:11], a[40:43], v[20:23]\n"
  "ds_read_b128 a[116:119], v210 offset:43008\n"
  "v_mfma_f32_16x16x32_bf16 v[20:23], a[12:15], a[44:47], v[20:23]\n"
  "v_mfma_f32_16x16x32_bf16 v[24:27], a[16:19], a[40:43], v[24:27]\n"
  "ds_read_b128 a[120:123], v210 offset:35840\n"
  "v_mfma_f32_16x16x32_bf16 v[24:27], a[20:23], a[44:47], v[24:27]\n"
  "v_mfma_f32_16x16x32_bf16 v[28:31], a[24:27], a[40:43], v[28:31]\n"
  "ds_read_b128 a[124:127], v210 offset:44032\n"
  "v_mfma_f32_16x16x32_bf16 v[28:31], a[28:31], a[44:47], v[28:31]\n"
  "v_mfma_f32_16x16x32_bf16 v[32:35], a[0:3], a[48:51], v[32:35]\n"
  "ds_read_b128 a[128:131], v208 offset:0\n"
  "v_mfma_f32_16x16x32_bf16 v[32:35], a[4:7], a[52:55], v[32:35]\n"
  "v_mfma_f32_16x16x32_bf16 v[36:39], a[8:11], a[48:51], v[36:39]\n"
  "ds_read_b128 a[132:135], v209 offset:0\n"
  "v_mfma_f32_16x16x32_bf16 v[36:39], a[12:15], a[52:55], v[36:39]\n"
  "s_waitcnt vmcnt(8)\n"
  "v_cmp_ne_u64 vcc, v[218:219], v[216:217]\n"
  "s_cbranch_vccnz SLB_%=\n"
  "BKB_%=:\n"
  "s_xor_b32 s29, s29, 128\n"
  "v_mfma_f32_16x16x32_bf16 v[40:43], a[16:19], a[48:51], v[40:43]\n"
  "ds_read_b128 a[136:139], v208 offset:2048\n"
  "v_mfma_f32_16x16x32_bf16 v[40:43], a[20:23], a[52:55], v[40:43]\n"
  "buffer_load_dwordx4 v200, s[4:7], s26 offen lds\n"
  "v_mfma_f32_16x16x32_bf16 v[44:47], a[24:27], a[48:51], v[44:47]\n"
  "ds_read_b128 a[140:143], v209 offset:2048\n"
  "v_mfma_f32_16x16x32_bf16 v[44:47], a[28:31], a[52:55], v[44:47]\n"
  "v_mfma_f32_16x16x32_bf16 v[48:51], a[0:3], a[56:59], v[48:51]\n"
  "ds_read_b128 a[144:147], v208 offset:4096\n"
  "v_mfma_f32_16x16x32_bf16 v[48:51], a[4:7], a[60:63], v[48:51]\n"
  "buffer_load_dwordx4 v201, s[4:7], s26 offen offset:1024 lds\n"
  "v_mfma_f32_16x16x32_bf16 v[52:55], a[8:11], a[56:59], v[52:55]\n"
  "ds_read_b128 a[148:151], v209 offset:4096\n"
  "v_mfma_f32_16x16x32_bf16 v[52:55], a[12:15], a[60:63], v[52:55]\n"
  "v_mfma_f32_16x16x32_bf16 v[56:59], a[16:19], a[56:59], v[56:59]\n"
  "ds_read_b128 a[152:155], v208 offset:6144\n"
  "v_mfma_f32_16x16x32_bf16 v[56:59], a[20:23], a[60:63], v[56:59]\n"
  "buffer_load_dwordx4 v202, s[4:7], s26 offen offset:2048 lds\n"
  "v_mfma_f32_16x16x32_bf16 v[60:63], a[24:27], a[56:59], v[60:63]\n"
  "ds_read_b128 a[156:159], v209 offset:6144\n"
  "v_mfma_f32_16x16x32_bf16 v[60:63], a[28:31], a[60:63], v[60:63]\n"
  "v_mfma_f32_16x16x32_bf16 v[64:67], a[0:3], a[64:67], v[64:67]\n"
  "ds_read_b128 a[160:163], v208 offset:8192\n"
  "v_mfma_f32_16x16x32_bf16 v[64:67], a[4:7], a[68:71], v[64:67]\n"
  "buffer_load_dwordx4 v203, s[4:7], s26 offen offset:3072 lds\n"
  "s_add_u32 m0, s24, 81920\n"
  "v_mfma_f32_16x16x32_bf16 v[68:71], a[8:11], a[64:67], v[68:71]\n"
  "ds_read_b128 a[164:167], v209 offset:8192\n"
  "s_xor_b32 s26, s53, 16384\n"
  "v_mfma_f32_16x16x32_bf16 v[68:71], a[12:15], a[68:71], v[68:71]\n"
  "v_mfma_f32_16x16x32_bf16 v[72:75], a[16:19], a[64:67], v[72:75]\n"
  "ds_read_b128 a[168:171], v208 offset:10240\n"
  "v_mfma_f32_16x16x32_bf16 v[72:75], a[20:23], a[68:71], v[72:75]\n"
  "buffer_load_dwordx4 v211, s[20:23], s26 offen sc1 lds\n"
  "v_mfma_f32_16x16x32_bf16 v[76:79], a[24:27], a[64:67], v[76:79]\n"
  "ds_read_b128 a[172:175], v209 offset:10240\n"
  "v_mfma_f32_16x16x32_bf16 v[76:79], a[28:31], a[68:71], v[76:79]\n"
  "v_mfma_f32_16x16x32_bf16 v[80:83], a[0:3], a[72:75], v[80:83]\n"
  "ds_read_b128 a[176:179], v208 offset:12288\n"
  "v_mfma_f32_16x16x32_bf16 v[80:83], a[4:7], a[76:79], v[80:83]\n"
  "buffer_load_dwordx4 v211, s[20:23], s26 offen offset:1024 sc1 lds\n"
  "v_mfma_f32_16x16x32_bf16 v[84:87], a[8:11], a[72:75], v[84:87]\n"
  "ds_read_b128 a[180:183], v209 offset:12288\n"
  "v_mfma_f32_16x16x32_bf16 v[84:87], a[12:15], a[76:79], v[84:87]\n"
  "v_mfma_f32_16x16x32_bf16 v[88:91], a[16:19], a[72:75], v[88:91]\n"
  "ds_read_b128 a[184:187], v208 offset:14336\n"
  "v_mfma_f32_16x16x32_bf16 v[88:91], a[20:23], a[76:79], v[88:91]\n"
  "buffer_load_dwordx4 v211, s[20:23], s26 offen offset:2048 sc1 lds\n"
  "v_mfma_f32_16x16x32_bf16 v[92:95], a[24:27], a[72:75], v[92:95]\n"
  "ds_read_b128 a[188:191], v209 offset:14336\n"
  "v_mfma_f32_16x16x32_bf16 v[92:95], a[28:31], a[76:79], v[92:95]\n"
  "v_mfma_f32_16x16x32_bf16 v[96:99], a[0:3], a[80:83], v[96:99]\n"
  "v_mfma_f32_16x16x32_bf16 v[96:99], a[4:7], a[84:87], v[96:99]\n"
  "buffer_load_dwordx4 v211, s[20:23], s26 offen offset:3072 sc1 lds\n"
  "s_add_u32 m0, s25, 102400\n"
  "v_mfma_f32_16x16x32_bf16 v[100:103], a[8:11], a[80:83], v[100:103]\n"
  "s_xor_b32 s26, s46, 131200\n"
  "v_mfma_f32_16x16x32_bf16 v[100:103], a[12:15], a[84:87], v[100:103]\n"
  "v_mfma_f32_16x16x32_bf16 v[104:107], a[16:19], a[80:83], v[104:107]\n"
  "v_mfma_f32_16x16x32_bf16 v[104:107], a[20:23], a[84:87], v[104:107]\n"
  "v_mfma_f32_16x16x32_bf16 v[108:111], a[24:27], a[80:83], v[108:111]\n"
  "v_mfma_f32_16x16x32_bf16 v[108:111], a[28:31], a[84:87], v[108:111]\n"
  "v_mfma_f32_16x16x32_bf16 v[112:115], a[0:3], a[88:91], v[112:115]\n"
  "v_mfma_f32_16x16x32_bf16 v[112:115], a[4:7], a[92:95], v[112:115]\n"
  "v_mfma_f32_16x16x32_bf16 v[116:119], a[8:11], a[88:91], v[116:119]\n"
  "v_mfma_f32_16x16x32_bf16 v[116:119], a[12:15], a[92:95], v[116:119]\n"
  "buffer_load_dwordx4 v200, s[4:7], s26 offen lds\n"
  "v_mfma_f32_16x16x32_bf16 v[120:123], a[16:19], a[88:91], v[120:123]\n"
  "v_mfma_f32_16x16x32_bf16 v[120:123], a[20:23], a[92:95], v[120:123]\n"
  "v_mfma_f32_16x16x32_bf16 v[124:127], a[24:27], a[88:91], v[124:127]\n"
  "v_mfma_f32_16x16x32_bf16 v[124:127], a[28:31], a[92:95], v[124:127]\n"
  "s_waitcnt vmcnt(9) lgkmcnt(0)\n"
  "s_barrier\n"
  "v_mfma_f32_16x16x32_bf16 v[0:3], a[96:99], a[128:131], v[0:3]\n"
  "ds_read_b128 a[32:35], v228 offset:0\n"
  "v_mfma_f32_16x16x32_bf16 v[0:3], a[100:103], a[132:135], v[0:3]\n"
  "buffer_load_dwordx4 v201, s[4:7], s26 offen offset:1024 lds\n"
  "v_mfma_f32_16x16x32_bf16 v[4:7], a[104:107], a[128:131], v[4:7]\n"
  "ds_read_b128 a[36:39], v229 offset:0\n"
  "v_mfma_f32_16x16x32_bf16 v[4:7], a[108:111], a[132:135], v[4:7]\n"
  "v_mfma_f32_16x16x32_bf16 v[8:11], a[112:115], a[128:131], v[8:11]\n"
  "ds_read_b128 a[40:43], v228 offset:2048\n"
  "v_mfma_f32_16x16x32_bf16 v[8:11], a[116:119], a[132:135], v[8:11]\n"
  "buffer_load_dwordx4 v202, s[4:7], s26 offen offset:2048 lds\n"
  "v_mfma_f32_16x16x32_bf16 v[12:15], a[120:123], a[128:131], v[12:15]\n"
  "ds_read_b128 a[44:47], v229 offset:2048\n"
  "v_mfma_f32_16x16x32_bf16 v[12:15], a[124:127], a[132:135], v[12:15]\n"
  "v_mfma_f32_16x16x32_bf16 v[16:19], a[96:99], a[136:139], v[16:19]\n"
  "ds_read_b128 a[48:51], v228 offset:4096\n"
  "v_mfma_f32_16x16x32_bf16 v[16:19], a[100:103], a[140:143], v[16:19]\n"
  "buffer_load_dwordx4 v203, s[4:7], s26 offen offset:3072 lds\n"
  "s_add_u32 m0, s24, 131072\n"
  "v_mfma_f32_16x16x32_bf16 v[20:23], a[104:107], a[136:139], v[20:23]\n"
  "ds_read_b128 a[52:55], v229 offset:4096\n"
  "s_xor_b32 s26, s54, 16384\n"
  "v_mfma_f32_16x16x32_bf16 v[20:23], a[108:111], a[140:143], v[20:23]\n"
  "v_mfma_f32_16x16x32_bf16 v[24:27], a[112:115], a[136:139], v[24:27]\n"
  "ds_read_b128 a[56:59], v228 offset:6144\n"
  "v_mfma_f32_16x16x32_bf16 v[24:27], a[116:119], a[140:143], v[24:27]\n"
  "buffer_load_dwordx4 v211, s[20:23], s26 offen sc1 lds\n"
  "v_mfma_f32_16x16x32_bf16 v[28:31], a[120:123], a[136:139], v[28:31]\n"
  "ds_read_b128 a[60:63], v229 offset:6144\n"
  "v_mfma_f32_16x16x32_bf16 v[28:31], a[124:127], a[140:143], v[28:31]\n"
  "v_mfma_f32_16x16x32_bf16 v[32:35], a[96:99], a[144:147], v[32:35]\n"
  "ds_read_b128 a[64:67], v228 offset:8192\n"
  "v_mfma_f32_16x16x32_bf16 v[32:35], a[100:103], a[148:151], v[32:35]\n"
  "buffer_load_dwordx4 v211, s[20:23], s26 offen offset:1024 sc1 lds\n"
  "v_mfma_f32_16x16x32_bf16 v[36:39], a[104:107], a[144:147], v[36:39]\n"
  "ds_read_b128 a[68:71], v229 offset:8192\n"
  "v_mfma_f32_16x16x32_bf16 v[36:39], a[108:111], a[148:151], v[36:39]\n"
  "v_mfma_f32_16x16x32_bf16 v[40:43], a[112:115], a[144:147], v[40:43]\n"
  "ds_read_b128 a[72:75], v228 offset:10240\n"
  "v_mfma_f32_16x16x32_bf16 v[40:43], a[116:119], a[148:151], v[40:43]\n"
  "buffer_load_dwordx4 v211, s[20:23], s26 offen offset:2048 sc1 lds\n"
  "v_mfma_f32_16x16x32_bf16 v[44:47], a[120:123], a[144:147], v[44:47]\n"
  "ds_read_b128 a[76:79], v229 offset:10240\n"
  "v_mfma_f32_16x16x32_bf16 v[44:47], a[124:127], a[148:151], v[44:47]\n"
  "v_mfma_f32_16x16x32_bf16 v[48:51], a[96:99], a[152:155], v[48:51]\n"
  "ds_read_b128 a[80:83], v228 offset:12288\n"
  "v_mfma_f32_16x16x32_bf16 v[48:51], a[100:103], a[156:159], v[48:51]\n"
  "buffer_load_dwordx4 v211, s[20:23], s26 offen offset:3072 sc1 lds\n"
  "s_add_u32 m0, s25, 0\n"
  "v_mfma_f32_16x16x32_bf16 v[52:55], a[104:107], a[152:155], v[52:55]\n"
  "ds_read_b128 a[84:87], v229 offset:12288\n"
  "s_xor_b32 s26, s47, 0\n"
  "v_mfma_f32_16x16x32_bf16 v[52:55], a[108:111], a[156:159], v[52:55]\n"
  "v_mfma_f32_16x16x32_bf16 v[56:59], a[112:115], a[152:155], v[56:59]\n"
  "ds_read_b128 a[88:91], v228 offset:14336\n"
  "v_mfma_f32_16x16x32_bf16 v[56:59], a[116:119], a[156:159], v[56:59]\n"
  "buffer_load_dwordx4 v200, s[4:7], s26 offen lds\n"
  "v_mfma_f32_16x16x32_bf16 v[60:63], a[120:123], a[152:155], v[60:63]\n"
  "ds_read_b128 a[92:95], v229 offset:14336\n"
  "v_mfma_f32_16x16x32_bf16 v[60:63], a[124:127], a[156:159], v[60:63]\n"
  "v_mfma_f32_16x16x32_bf16 v[64:67], a[96:99], a[160:163], v[64:67]\n"
  "v_mfma_f32_16x16x32_bf16 v[64:67], a[100:103], a[164:167], v[64:67]\n"
  "buffer_load_dwordx4 v201, s[4:7], s26 offen offset:1024 lds\n"
  "v_mfma_f32_16x16x32_bf16 v[68:71], a[104:107], a[160:163], v[68:71]\n"
  "v_mfma_f32_16x16x32_bf16 v[68:71], a[108:111], a[164:167], v[68:71]\n"
  "v_mfma_f32_16x16x32_bf16 v[72:75], a[112:115], a[160:163], v[72:75]\n"
  "v_mfma_f32_16x16x32_bf16 v[72:75], a[116:119], a[164:167], v[72:75]\n"
  "s_waitcnt vmcnt(10)\n"
  "s_barrier\n"
  "v_mfma_f32_16x16x32_bf16 v[76:79], a[120:123], a[160:163], v[76:79]\n"
  "ds_read_b128 a[0:3], v230 offset:32768\n"
  "v_mfma_f32_16x16x32_bf16 v[76:79], a[124:127], a[164:167], v[76:79]\n"
  "buffer_load_dwordx4 v202, s[4:7], s26 offen offset:2048 lds\n"
  "v_mfma_f32_16x16x32_bf16 v[80:83], a[96:99], a[168:171], v[80:83]\n"
  "ds_read_b128 a[4:7], v230 offset:40960\n"
  "v_mfma_f32_16x16x32_bf16 v[80:83], a[100:103], a[172:175], v[80:83]\n"
  "v_mfma_f32_16x16x32_bf16 v[84:87], a[104:107], a[168:171], v[84:87]\n"
  "ds_read_b128 a[8:11], v230 offset:33792\n"
  "v_mfma_f32_16x16x32_bf16 v[84:87], a[108:111], a[172:175], v[84:87]\n"
  "buffer_load_dwordx4 v203, s[4:7], s26 offen offset:3072 lds\n"
  "s_add_u32 m0, s25, 4096\n"
  "v_mfma_f32_16x16x32_bf16 v[88:91], a[112:115], a[168:171], v[88:91]\n"
  "ds_read_b128 a[12:15], v230 offset:41984\n"
  "s_xor_b32 s26, s47, 131072\n"
  "v_mfma_f32_16x16x32_bf16 v[88:91], a[116:119], a[172:175], v[88:91]\n"
  "v_mfma_f32_16x16x32_bf16 v[92:95], a[120:123], a[168:171], v[92:95]\n"
  "ds_read_b128 a[16:19], v230 offset:34816\n"
  "v_mfma_f32_16x16x32_bf16 v[92:95], a[124:127], a[172:175], v[92:95]\n"
  "buffer_load_dwordx4 v200, s[4:7], s26 offen lds\n"
  "v_mfma_f32_16x16x32_bf16 v[96:99], a[96:99], a[176:179], v[96:99]\n"
  "ds_read_b128 a[20:23], v230 offset:43008\n"
  "v_mfma_f32_16x16x32_bf16 v[96:99], a[100:103], a[180:183], v[96:99]\n"
  "v_mfma_f32_16x16x32_bf16 v[100:103], a[104:107], a[176:179], v[100:103]\n"
  "ds_read_b128 a[24:27], v230 offset:35840\n"
  "v_mfma_f32_16x16x32_bf16 v[100:103], a[108:111], a[180:183], v[100:103]\n"
  "buffer_load_dwordx4 v201, s[4:7], s26 offen offset:1024 lds\n"
  "v_mfma_f32_16x16x32_bf16 v[104:107], a[112:115], a[176:179], v[104:107]\n"
  "ds_read_b128 a[28:31], v230 offset:44032\n"
  "v_mfma_f32_16x16x32_bf16 v[104:107], a[116:119], a[180:183], v[104:107]\n"
  "v_mfma_f32_16x16x32_bf16 v[108:111], a[120:123], a[176:179], v[108:111]\n"
  "v_mfma_f32_16x16x32_bf16 v[108:111], a[124:127], a[180:183], v[108:111]\n"
  "buffer_load_dwordx4 v202, s[4:7], s26 offen offset:2048 lds\n"
  "v_mfma_f32_16x16x32_bf16 v[112:115], a[96:99], a[184:187], v[112:115]\n"
  "v_mfma_f32_16x16x32_bf16 v[112:115], a[100:103], a[188:191], v[112:115]\n"
  "v_mfma_f32_16x16x32_bf16 v[116:119], a[104:107], a[184:187], v[116:119]\n"
  "v_mfma_f32_16x16x32_bf16 v[116:119], a[108:111], a[188:191], v[116:119]\n"
  "buffer_load_dwordx4 v203, s[4:7], s26 offen offset:3072 lds\n"
  "s_add_u32 m0, s24, 32768\n"
  "v_mfma_f32_16x16x32_bf16 v[120:123], a[112:115], a[184:187], v[120:123]\n"
  "s_xor_b32 s26, s55, 0\n"
  "v_mfma_f32_16x16x32_bf16 v[120:123], a[116:119], a[188:191], v[120:123]\n"
  "v_mfma_f32_16x16x32_bf16 v[124:127], a[120:123], a[184:187], v[124:127]\n"
  "v_mfma_f32_16x16x32_bf16 v[124:127], a[124:127], a[188:191], v[124:127]\n"
  "s_waitcnt lgkmcnt(0)\n"
  "s_barrier\n"
  "v_mfma_f32_16x16x32_bf16 v[0:3], a[0:3], a[32:35], v[0:3]\n"
  "buffer_load_dwordx4 v211, s[20:23], s26 offen sc1 lds\n"
  "v_mfma_f32_16x16x32_bf16 v[0:3], a[4:7], a[36:39], v[0:3]\n"
  "v_mfma_f32_16x16x32_bf16 v[4:7], a[8:11], a[32:35], v[4:7]\n"
  "v_mfma_f32_16x16x32_bf16 v[4:7], a[12:15], a[36:39], v[4:7]\n"
  "v_mfma_f32_16x16x32_bf16 v[8:11], a[16:19], a[32:35], v[8:11]\n"
  "buffer_load_dwordx4 v211, s[20:23], s26 offen offset:1024 sc1 lds\n"
  "v_mfma_f32_16x16x32_bf16 v[8:11], a[20:23], a[36:39], v[8:11]\n"
  "v_mfma_f32_16x16x32_bf16 v[12:15], a[24:27], a[32:35], v[12:15]\n"
  "v_mfma_f32_16x16x32_bf16 v[12:15], a[28:31], a[36:39], v[12:15]\n"
  "v_mfma_f32_16x16x32_bf16 v[16:19], a[0:3], a[40:43], v[16:19]\n"
  "buffer_load_dwordx4 v211, s[20:23], s26 offen offset:2048 sc1 lds\n"
  "v_mfma_f32_16x16x32_bf16 v[16:19], a[4:7], a[44:47], v[16:19]\n"
  "v_mfma_f32_16x16x32_bf16 v[20:23], a[8:11], a[40:43], v[20:23]\n"
  "v_mfma_f32_16x16x32_bf16 v[20:23], a[12:15], a[44:47], v[20:23]\n"
  "v_mfma_f32_16x16x32_bf16 v[24:27], a[16:19], a[40:43], v[24:27]\n"
  "buffer_load_dwordx4 v211, s[20:23], s26 offen offset:3072 sc1 lds\n"
  "s_add_u32 m0, s25, 49152\n"
  "v_mfma_f32_16x16x32_bf16 v[24:27], a[20:23], a[44:47], v[24:27]\n"
  "s_xor_b32 s26, s47, 128\n"
  "v_mfma_f32_16x16x32_bf16 v[28:31], a[24:27], a[40:43], v[28:31]\n"
  "v_mfma_f32_16x16x32_bf16 v[28:31], a[28:31], a[44:47], v[28:31]\n"
  "v_mfma_f32_16x16x32_bf16 v[32:35], a[0:3], a[48:51], v[32:35]\n"
  "v_mfma_f32_16x16x32_bf16 v[32:35], a[4:7], a[52:55], v[32:35]\n"
  "v_mfma_f32_16x16x32_bf16 v[36:39], a[8:11], a[48:51], v[36:39]\n"
  "v_mfma_f32_16x16x32_bf16 v[36:39], a[12:15], a[52:55], v[36:39]\n"
  "v_mfma_f32_16x16x32_bf16 v[40:43], a[16:19], a[48:51], v[40:43]\n"
  "v_mfma_f32_16x16x32_bf16 v[40:43], a[20:23], a[52:55], v[40:43]\n"
  "s_waitcnt vmcnt(12)\n"
  "s_barrier\n"
  "v_mfma_f32_16x16x32_bf16 v[44:47], a[24:27], a[48:51], v[44:47]\n"
  "ds_read_b128 a[96:99], v237 offset:32768\n"
  "v_mfma_f32_16x16x32_bf16 v[44:47], a[28:31], a[52:55], v[44:47]\n"
  "ds_read_b128 a[100:103], v237 offset:40960\n"
  "v_mfma_f32_16x16x32_bf16 v[48:51], a[0:3], a[56:59], v[48:51]\n"
  "ds_read_b128 a[104:107], v237 offset:33792\n"
  "v_mfma_f32_16x16x32_bf16 v[48:51], a[4:7], a[60:63], v[48:51]\n"
  "buffer_load_dwordx2 v[218:219], v214, s[20:23], s29 offen sc1\n"
  "v_mfma_f32_16x16x32_bf16 v[52:55], a[8:11], a[56:59], v[52:55]\n"
  "ds_read_b128 a[108:111], v237 offset:41984\n"
  "v_mfma_f32_16x16x32_bf16 v[52:55], a[12:15], a[60:63], v[52:55]\n"
  "ds_read_b128 a[112:115], v237 offset:34816\n"
  "v_mfma_f32_16x16x32_bf16 v[56:59], a[16:19], a[56:59], v[56:59]\n"
  "ds_read_b128 a[116:119], v237 offset:43008\n"
  "v_mfma_f32_16x16x32_bf16 v[56:59], a[20:23], a[60:63], v[56:59]\n"
  "buffer_load_dwordx4 v200, s[4:7], s26 offen lds\n"
  "v_mfma_f32_16x16x32_bf16 v[60:63], a[24:27], a[56:59], v[60:63]\n"
  "ds_read_b128 a[120:123], v237 offset:35840\n"
  "v_mfma_f32_16x16x32_bf16 v[60:63], a[28:31], a[60:63], v[60:63]\n"
  "ds_read_b128 a[124:127], v237 offset:44032\n"
  "v_mfma_f32_16x16x32_bf16 v[64:67], a[0:3], a[64:67], v[64:67]\n"
  "ds_read_b128 a[128:131], v235 offset:0\n"
  "v_mfma_f32_16x16x32_bf16 v[64:67], a[4:7], a[68:71], v[64:67]\n"
  "buffer_load_dwordx4 v201, s[4:7], s26 offen offset:1024 lds\n"
  "v_mfma_f32_16x16x32_bf16 v[68:71], a[8:11], a[64:67], v[68:71]\n"
  "ds_read_b128 a[132:135], v236 offset:0\n"
  "v_mfma_f32_16x16x32_bf16 v[68:71], a[12:15], a[68:71], v[68:71]\n"
  "ds_read_b128 a[136:139], v235 offset:2048\n"
  "v_mfma_f32_16x16x32_bf16 v[72:75], a[16:19], a[64:67], v[72:75]\n"
  "ds_read_b128 a[140:143], v236 offset:2048\n"
  "v_mfma_f32_16x16x32_bf16 v[72:75], a[20:23], a[68:71], v[72:75]\n"
  "buffer_load_dwordx4 v202, s[4:7], s26 offen offset:2048 lds\n"
  "v_mfma_f32_16x16x32_bf16 v[76:79], a[24:27], a[64:67], v[76:79]\n"
  "ds_read_b128 a[144:147], v235 offset:4096\n"
  "v_mfma_f32_16x16x32_bf16 v[76:79], a[28:31], a[68:71], v[76:79]\n"
  "ds_read_b128 a[148:151], v236 offset:4096\n"
  "v_mfma_f32_16x16x32_bf16 v[80:83], a[0:3], a[72:75], v[80:83]\n"
  "ds_read_b128 a[152:155], v235 offset:6144\n"
  "v_mfma_f32_16x16x32_bf16 v[80:83], a[4:7], a[76:79], v[80:83]\n"
  "buffer_load_dwordx4 v203, s[4:7], s26 offen offset:3072 lds\n"
  "s_add_u32 m0, s25, 53248\n"
  "v_mfma_f32_16x16x32_bf16 v[84:87], a[8:11], a[72:75], v[84:87]\n"
  "ds_read_b128 a[156:159], v236 offset:6144\n"
  "s_xor_b32 s26, s47, 131200\n"
  "v_mfma_f32_16x16x32_bf16 v[84:87], a[12:15], a[76:79], v[84:87]\n"
  "v_mfma_f32_16x16x32_bf16 v[88:91], a[16:19], a[72:75], v[88:91]\n"
  "ds_read_b128 a[160:163], v235 offset:8192\n"
  "v_mfma_f32_16x16x32_bf16 v[88:91], a[20:23], a[76:79], v[88:91]\n"
  "buffer_load_dwordx4 v200, s[4:7], s26 offen lds\n"
  "v_mfma_f32_16x16x32_bf16 v[92:95], a[24:27], a[72:75], v[92:95]\n"
  "ds_read_b128 a[164:167], v236 offset:8192\n"
  "v_mfma_f32_16x16x32_bf16 v[92:95], a[28:31], a[76:79], v[92:95]\n"
  "v_mfma_f32_16x16x32_bf16 v[96:99], a[0:3], a[80:83], v[96:99]\n"
  "ds_read_b128 a[168:171], v235 offset:10240\n"
  "v_mfma_f32_16x16x32_bf16 v[96:99], a[4:7], a[84:87], v[96:99]\n"
  "buffer_load_dwordx4 v201, s[4:7], s26 offen offset:1024 lds\n"
  "v_mfma_f32_16x16x32_bf16 v[100:103], a[8:11], a[80:83], v[100:103]\n"
  "ds_read_b128 a[172:175], v236 offset:10240\n"
  "v_mfma_f32_16x16x32_bf16 v[100:103], a[12:15], a[84:87], v[100:103]\n"
  "v_mfma_f32_16x16x32_bf16 v[104:107], a[16:19], a[80:83], v[104:107]\n"
  "ds_read_b128 a[176:179], v235 offset:12288\n"
  "v_mfma_f32_16x16x32_bf16 v[104:107], a[20:23], a[84:87], v[104:107]\n"
  "buffer_load_dwordx4 v202, s[4:7], s26 offen offset:2048 lds\n"
  "v_mfma_f32_16x16x32_bf16 v[108:111], a[24:27], a[80:83], v[108:111]\n"
  "ds_read_b128 a[180:183], v236 offset:12288\n"
  "v_mfma_f32_16x16x32_bf16 v[108:111], a[28:31], a[84:87], v[108:111]\n"
  "v_mfma_f32_16x16x32_bf16 v[112:115], a[0:3], a[88:91], v[112:115]\n"
  "ds_read_b128 a[184:187], v235 offset:14336\n"
  "ds_read_b128 a[188:191], v236 offset:14336\n"
  "v_mfma_f32_16x16x32_bf16 v[112:115], a[4:7], a[92:95], v[112:115]\n"
  "buffer_load_dwordx4 v203, s[4:7], s26 offen offset:3072 lds\n"
  "s_add_u32 m0, s24, 81920\n"
  "v_mfma_f32_16x16x32_bf16 v[116:119], a[8:11], a[88:91], v[116:119]\n"
  "s_xor_b32 s26, s55, 16384\n"
  "v_mfma_f32_16x16x32_bf16 v[116:119], a[12:15], a[92:95], v[116:119]\n"
  "v_mfma_f32_16x16x32_bf16 v[120:123], a[16:19], a[88:91], v[120:123]\n"
  "v_mfma_f32_16x16x32_bf16 v[120:123], a[20:23], a[92:95], v[120:123]\n"
  "buffer_load_dwordx4 v211, s[20:23], s26 offen sc1 lds\n"
  "v_mfma_f32_16x16x32_bf16 v[124:127], a[24:27], a[88:91], v[124:127]\n"
  "v_mfma_f32_16x16x32_bf16 v[124:127], a[28:31], a[92:95], v[124:127]\n"
  "s_waitcnt vmcnt(10) lgkmcnt(0)\n"
  "s_barrier\n"
  "v_mfma_f32_16x16x32_bf16 v[0:3], a[96:99], a[128:131], v[0:3]\n"
  "ds_read_b128 a[0:3], v210 offset:32768\n"
  "v_mfma_f32_16x16x32_bf16 v[0:3], a[100:103], a[132:135], v[0:3]\n"
  "buffer_load_dwordx4 v211, s[20:23], s26 offen offset:1024 sc1 lds\n"
  "v_mfma_f32_16x16x32_bf16 v[4:7], a[104:107], a[128:131], v[4:7]\n"
  "ds_read_b128 a[4:7], v210 offset:40960\n"
  "v_mfma_f32_16x16x32_bf16 v[4:7], a[108:111], a[132:135], v[4:7]\n"
  "v_mfma_f32_16x16x32_bf16 v[8:11], a[112:115], a[128:131], v[8:11]\n"
  "ds_read_b128 a[8:11], v210 offset:33792\n"
  "v_mfma_f32_16x16x32_bf16 v[8:11], a[116:119], a[132:135], v[8:11]\n"
  "buffer_load_dwordx4 v211, s[20:23], s26 offen offset:2048 sc1 lds\n"
  "v_mfma_f32_16x16x32_bf16 v[12:15], a[120:123], a[128:131], v[12:15]\n"
  "ds_read_b128 a[12:15], v210 offset:41984\n"
  "v_mfma_f32_16x16x32_bf16 v[12:15], a[124:127], a[132:135], v[12:15]\n"
  "v_mfma_f32_16x16x32_bf16 v[16:19], a[96:99], a[136:139], v[16:19]\n"
  "ds_read_b128 a[16:19], v210 offset:34816\n"
  "v_mfma_f32_16x16x32_bf16 v[16:19], a[100:103], a[140:143], v[16:19]\n"
  "buffer_load_dwordx4 v211, s[20:23], s26 offen offset:3072 sc1 lds\n"
  "s_add_u32 m0, s25, 98304\n"
  "v_mfma_f32_16x16x32_bf16 v[20:23], a[104:107], a[136:139], v[20:23]\n"
  "ds_read_b128 a[20:23], v210 offset:43008\n"
  "s_xor_b32 s26, s48, 0\n"
  "v_mfma_f32_16x16x32_bf16 v[20:23], a[108:111], a[140:143], v[20:23]\n"
  "s_waitcnt vmcnt(12)\n"
  "v_cmp_ne_u64 vcc, v[218:219], v[216:217]\n"
  "s_cbranch_vccnz SLC_%=\n"
  "BKC_%=:\n"
  "v_mfma_f32_16x16x32_bf16 v[24:27], a[112:115], a[136:139], v[24:27]\n"
  "ds_read_b128 a[24:27], v210 offset:35840\n"
  "v_mfma_f32_16x16x32_bf16 v[24:27], a[116:119], a[140:143], v[24:27]\n"
  "v_mfma_f32_16x16x32_bf16 v[28:31], a[120:123], a[136:139], v[28:31]\n"
  "ds_read_b128 a[28:31], v210 offset:44032\n"
  "v_mfma_f32_16x16x32_bf16 v[28:31], a[124:127], a[140:143], v[28:31]\n"
  "v_mfma_f32_16x16x32_bf16 v[32:35], a[96:99], a[144:147], v[32:35]\n"
  "ds_read_b128 a[32:35], v208 offset:0\n"
  "v_mfma_f32_16x16x32_bf16 v[32:35], a[100:103], a[148:151], v[32:35]\n"
  "v_mfma_f32_16x16x32_bf16 v[36:39], a[104:107], a[144:147], v[36:39]\n"
  "ds_read_b128 a[36:39], v209 offset:0\n"
  "v_mfma_f32_16x16x32_bf16 v[36:39], a[108:111], a[148:151], v[36:39]\n"
  "v_mfma_f32_16x16x32_bf16 v[40:43], a[112:115], a[144:147], v[40:43]\n"
  "ds_read_b128 a[40:43], v208 offset:2048\n"
  "v_mfma_f32_16x16x32_bf16 v[40:43], a[116:119], a[148:151], v[40:43]\n"
  "buffer_load_dwordx4 v200, s[4:7], s26 offen lds\n"
  "v_mfma_f32_16x16x32_bf16 v[44:47], a[120:123], a[144:147], v[44:47]\n"
  "ds_read_b128 a[44:47], v209 offset:2048\n"
  "v_mfma_f32_16x16x32_bf16 v[44:47], a[124:127], a[148:151], v[44:47]\n"
  "v_mfma_f32_16x16x32_bf16 v[48:51], a[96:99], a[152:155], v[48:51]\n"
  "ds_read_b128 a[48:51], v208 offset:4096\n"
  "v_mfma_f32_16x16x32_bf16 v[48:51], a[100:103], a[156:159], v[48:51]\n"
  "buffer_load_dwordx4 v201, s[4:7], s26 offen offset:1024 lds\n"
  "v_mfma_f32_16x16x32_bf16 v[52:55], a[104:107], a[152:155], v[52:55]\n"
  "ds_read_b128 a[52:55], v209 offset:4096\n"
  "v_mfma_f32_16x16x32_bf16 v[52:55], a[108:111], a[156:159], v[52:55]\n"
  "v_mfma_f32_16x16x32_bf16 v[56:59], a[112:115], a[152:155], v[56:59]\n"
  "ds_read_b128 a[56:59], v208 offset:6144\n"
  "v_mfma_f32_16x16x32_bf16 v[56:59], a[116:119], a[156:159], v[56:59]\n"
  "buffer_load_dwordx4 v202, s[4:7], s26 offen offset:2048 lds\n"
  "v_mfma_f32_16x16x32_bf16 v[60:63], a[120:123], a[152:155], v[60:63]\n"
  "ds_read_b128 a[60:63], v209 offset:6144\n"
  "v_mfma_f32_16x16x32_bf16 v[60:63], a[124:127], a[156:159], v[60:63]\n"
  "v_mfma_f32_16x16x32_bf16 v[64:67], a[96:99], a[160:163], v[64:67]\n"
  "ds_read_b128 a[64:67], v208 offset:8192\n"
  "v_mfma_f32_16x16x32_bf16 v[64:67], a[100:103], a[164:167], v[64:67]\n"
  "buffer_load_dwordx4 v203, s[4:7], s26 offen offset:3072 lds\n"
  "s_add_u32 m0, s25, 102400\n"
  "v_mfma_f32_16x16x32_bf16 v[68:71], a[104:107], a[160:163], v[68:71]\n"
  "ds_read_b128 a[68:71], v209 offset:8192\n"
  "s_xor_b32 s26, s48, 131072\n"
  "v_mfma_f32_16x16x32_bf16 v[68:71], a[108:111], a[164:167], v[68:71]\n"
  "v_mfma_f32_16x16x32_bf16 v[72:75], a[112:115], a[160:163], v[72:75]\n"
  "ds_read_b128 a[72:75], v208 offset:10240\n"
  "v_mfma_f32_16x16x32_bf16 v[72:75], a[116:119], a[164:167], v[72:75]\n"
  "buffer_load_dwordx4 v200, s[4:7], s26 offen lds\n"
  "v_mfma_f32_16x16x32_bf16 v[76:79], a[120:123], a[160:163], v[76:79]\n"
  "ds_read_b128 a[76:79], v209 offset:10240\n"
  "v_mfma_f32_16x16x32_bf16 v[76:79], a[124:127], a[164:167], v[76:79]\n"
  "v_mfma_f32_16x16x32_bf16 v[80:83], a[96:99], a[168:171], v[80:83]\n"
  "ds_read_b128 a[80:83], v208 offset:12288\n"
  "v_mfma_f32_16x16x32_bf16 v[80:83], a[100:103], a[172:175], v[80:83]\n"
  "buffer_load_dwordx4 v201, s[4:7], s26 offen offset:1024 lds\n"
  "v_mfma_f32_16x16x32_bf16 v[84:87], a[104:107], a[168:171], v[84:87]\n"
  "ds_read_b128 a[84:87], v209 offset:12288\n"
  "v_mfma_f32_16x16x32_bf16 v[84:87], a[108:111], a[172:175], v[84:87]\n"
  "v_mfma_f32_16x16x32_bf16 v[88:91], a[112:115], a[168:171], v[88:91]\n"
  "ds_read_b128 a[88:91], v208 offset:14336\n"
  "v_mfma_f32_16x16x32_bf16 v[88:91], a[116:119], a[172:175], v[88:91]\n"
  "buffer_load_dwordx4 v202, s[4:7], s26 offen offset:2048 lds\n"
  "v_mfma_f32_16x16x32_bf16 v[92:95], a[120:123], a[168:171], v[92:95]\n"
  "ds_read_b128 a[92:95], v209 offset:14336\n"
  "v_mfma_f32_16x16x32_bf16 v[92:95], a[124:127], a[172:175], v[92:95]\n"
  "v_mfma_f32_16x16x32_bf16 v[96:99], a[96:99], a[176:179], v[96:99]\n"
  "v_mfma_f32_16x16x32_bf16 v[96:99], a[100:103], a[180:183], v[96:99]\n"
  "buffer_load_dwordx4 v203, s[4:7], s26 offen offset:3072 lds\n"
  "s_add_u32 m0, s24, 131072\n"
  "v_mfma_f32_16x16x32_bf16 v[100:103], a[104:107], a[176:179], v[100:103]\n"
  "s_xor_b32 s26, s56, 0\n"
  "v_mfma_f32_16x16x32_bf16 v[100:103], a[108:111], a[180:183], v[100:103]\n"
  "v_mfma_f32_16x16x32_bf16 v[104:107], a[112:115], a[176:179], v[104:107]\n"
  "v_mfma_f32_16x16x32_bf16 v[104:107], a[116:119], a[180:183], v[104:107]\n"
  "buffer_load_dwordx4 v211, s[20:23], s26 offen sc1 lds\n"
  "v_mfma_f32_16x16x32_bf16 v[108:111], a[120:123], a[176:179], v[108:111]\n"
  "v_mfma_f32_16x16x32_bf16 v[108:111], a[124:127], a[180:183], v[108:111]\n"
  "v_mfma_f32_16x16x32_bf16 v[112:115], a[96:99], a[184:187], v[112:115]\n"
  "v_mfma_f32_16x16x32_bf16 v[112:115], a[100:103], a[188:191], v[112:115]\n"
  "buffer_load_dwordx4 v211, s[20:23], s26 offen offset:1024 sc1 lds\n"
  "v_mfma_f32_16x16x32_bf16 v[116:119], a[104:107], a[184:187], v[116:119]\n"
  "v_mfma_f32_16x16x32_bf16 v[116:119], a[108:111], a[188:191], v[116:119]\n"
  "v_mfma_f32_16x16x32_bf16 v[120:123], a[112:115], a[184:187], v[120:123]\n"
  "v_mfma_f32_16x16x32_bf16 v[120:123], a[116:119], a[188:191], v[120:123]\n"
  "buffer_load_dwordx4 v211, s[20:23], s26 offen offset:2048 sc1 lds\n"
  "v_mfma_f32_16x16x32_bf16 v[124:127], a[120:123], a[184:187], v[124:127]\n"
  "v_mfma_f32_16x16x32_bf16 v[124:127], a[124:127], a[188:191], v[124:127]\n"
  "s_waitcnt vmcnt(11) lgkmcnt(0)\n"
  "s_barrier\n"
  "v_mfma_f32_16x16x32_bf16 v[0:3], a[0:3], a[32:35], v[0:3]\n"
  "ds_read_b128 a[96:99], v230 offset:32768\n"
  "v_mfma_f32_16x16x32_bf16 v[0:3], a[4:7], a[36:39], v[0:3]\n"
  "buffer_load_dwordx4 v211, s[20:23], s26 offen offset:3072 sc1 lds\n"
  "s_add_u32 m0, s25, 0\n"
  "v_mfma_f32_16x16x32_bf16 v[4:7], a[8:11], a[32:35], v[4:7]\n"
  "ds_read_b128 a[100:103], v230 offset:40960\n"
  "s_xor_b32 s26, s48, 128\n"
  "v_mfma_f32_16x16x32_bf16 v[4:7], a[12:15], a[36:39], v[4:7]\n"
  "v_mfma_f32_16x16x32_bf16 v[8:11], a[16:19], a[32:35], v[8:11]\n"
  "ds_read_b128 a[104:107], v230 offset:33792\n"
  "v_mfma_f32_16x16x32_bf16 v[8:11], a[20:23], a[36:39], v[8:11]\n"
  "v_mfma_f32_16x16x32_bf16 v[12:15], a[24:27], a[32:35], v[12:15]\n"
  "ds_read_b128 a[108:111], v230 offset:41984\n"
  "v_mfma_f32_16x16x32_bf16 v[12:15], a[28:31], a[36:39], v[12:15]\n"
  "v_mfma_f32_16x16x32_bf16 v[16:19], a[0:3], a[40:43], v[16:19]\n"
  "ds_read_b128 a[112:115], v230 offset:34816\n"
  "v_mfma_f32_16x16x32_bf16 v[16:19], a[4:7], a[44:47], v[16:19]\n"
  "v_mfma_f32_16x16x32_bf16 v[20:23], a[8:11], a[40:43], v[20:23]\n"
  "ds_read_b128 a[116:119], v230 offset:43008\n"
  "v_mfma_f32_16x16x32_bf16 v[20:23], a[12:15], a[44:47], v[20:23]\n"
  "v_mfma_f32_16x16x32_bf16 v[24:27], a[16:19], a[40:43], v[24:27]\n"
  "ds_read_b128 a[120:123], v230 offset:35840\n"
  "v_mfma_f32_16x16x32_bf16 v[24:27], a[20:23], a[44:47], v[24:27]\n"
  "v_mfma_f32_16x16x32_bf16 v[28:31], a[24:27], a[40:43], v[28:31]\n"
  "ds_read_b128 a[124:127], v230 offset:44032\n"
  "v_mfma_f32_16x16x32_bf16 v[28:31], a[28:31], a[44:47], v[28:31]\n"
  "v_mfma_f32_16x16x32_bf16 v[32:35], a[0:3], a[48:51], v[32:35]\n"
  "ds_read_b128 a[128:131], v228 offset:0\n"
  "v_mfma_f32_16x16x32_bf16 v[32:35], a[4:7], a[52:55], v[32:35]\n"
  "v_mfma_f32_16x16x32_bf16 v[36:39], a[8:11], a[48:51], v[36:39]\n"
  "ds_read_b128 a[132:135], v229 offset:0\n"
  "v_mfma_f32_16x16x32_bf16 v[36:39], a[12:15], a[52:55], v[36:39]\n"
  "v_mfma_f32_16x16x32_bf16 v[40:43], a[16:19], a[48:51], v[40:43]\n"
  "ds_read_b128 a[136:139], v228 offset:2048\n"
  "v_mfma_f32_16x16x32_bf16 v[40:43], a[20:23], a[52:55], v[40:43]\n"
  "buffer_load_dwordx4 v200, s[4:7], s26 offen lds\n"
  "v_mfma_f32_16x16x32_bf16 v[44:47], a[24:27], a[48:51], v[44:47]\n"
  "ds_read_b128 a[140:143], v229 offset:2048\n"
  "v_mfma_f32_16x16x32_bf16 v[44:47], a[28:31], a[52:55], v[44:47]\n"
  "v_mfma_f32_16x16x32_bf16 v[48:51], a[0:3], a[56:59], v[48:51]\n"
  "ds_read_b128 a[144:147], v228 offset:4096\n"
  "v_mfma_f32_16x16x32_bf16 v[48:51], a[4:7], a[60:63], v[48:51]\n"
  "buffer_load_dwordx4 v201, s[4:7], s26 offen offset:1024 lds\n"
  "v_mfma_f32_16x16x32_bf16 v[52:55], a[8:11], a[56:59], v[52:55]\n"
  "ds_read_b128 a[148:151], v229 offset:4096\n"
  "v_mfma_f32_16x16x32_bf16 v[52:55], a[12:15], a[60:63], v[52:55]\n"
  "v_mfma_f32_16x16x32_bf16 v[56:59], a[16:19], a[56:59], v[56:59]\n"
  "ds_read_b128 a[152:155], v228 offset:6144\n"
  "v_mfma_f32_16x16x32_bf16 v[56:59], a[20:23], a[60:63], v[56:59]\n"
  "buffer_load_dwordx4 v202, s[4:7], s26 offen offset:2048 lds\n"
  "v_mfma_f32_16x16x32_bf16 v[60:63], a[24:27], a[56:59], v[60:63]\n"
  "ds_read_b128 a[156:159], v229 offset:6144\n"
  "v_mfma_f32_16x16x32_bf16 v[60:63], a[28:31], a[60:63], v[60:63]\n"
  "v_mfma_f32_16x16x32_bf16 v[64:67], a[0:3], a[64:67], v[64:67]\n"
  "ds_read_b128 a[160:163], v228 offset:8192\n"
  "v_mfma_f32_16x16x32_bf16 v[64:67], a[4:7], a[68:71], v[64:67]\n"
  "buffer_load_dwordx4 v203, s[4:7], s26 offen offset:3072 lds\n"
  "s_add_u32 m0, s25, 4096\n"
  "v_mfma_f32_16x16x32_bf16 v[68:71], a[8:11], a[64:67], v[68:71]\n"
  "ds_read_b128 a[164:167], v229 offset:8192\n"
  "s_xor_b32 s26, s48, 131200\n"
  "v_mfma_f32_16x16x32_bf16 v[68:71], a[12:15], a[68:71], v[68:71]\n"
  "v_mfma_f32_16x16x32_bf16 v[72:75], a[16:19], a[64:67], v[72:75]\n"
  "ds_read_b128 a[168:171], v228 offset:10240\n"
  "v_mfma_f32_16x16x32_bf16 v[72:75], a[20:23], a[68:71], v[72:75]\n"
  "buffer_load_dwordx4 v200, s[4:7], s26 offen lds\n"
  "v_mfma_f32_16x16x32_bf16 v[76:79], a[24:27], a[64:67], v[76:79]\n"
  "ds_read_b128 a[172:175], v229 offset:10240\n"
  "v_mfma_f32_16x16x32_bf16 v[76:79], a[28:31], a[68:71], v[76:79]\n"
  "v_mfma_f32_16x16x32_bf16 v[80:83], a[0:3], a[72:75], v[80:83]\n"
  "ds_read_b128 a[176:179], v228 offset:12288\n"
  "v_mfma_f32_16x16x32_bf16 v[80:83], a[4:7], a[76:79], v[80:83]\n"
  "buffer_load_dwordx4 v201, s[4:7], s26 offen offset:1024 lds\n"
  "v_mfma_f32_16x16x32_bf16 v[84:87], a[8:11], a[72:75], v[84:87]\n"
  "ds_read_b128 a[180:183], v229 offset:12288\n"
  "v_mfma_f32_16x16x32_bf16 v[84:87], a[12:15], a[76:79], v[84:87]\n"
  "v_mfma_f32_16x16x32_bf16 v[88:91], a[16:19], a[72:75], v[88:91]\n"
  "ds_read_b128 a[184:187], v228 offset:14336\n"
  "v_mfma_f32_16x16x32_bf16 v[88:91], a[20:23], a[76:79], v[88:91]\n"
  "buffer_load_dwordx4 v202, s[4:7], s26 offen offset:2048 lds\n"
  "v_mfma_f32_16x16x32_bf16 v[92:95], a[24:27], a[72:75], v[92:95]\n"
  "ds_read_b128 a[188:191], v229 offset:14336\n"
  "v_mfma_f32_16x16x32_bf16 v[92:95], a[28:31], a[76:79], v[92:95]\n"
  "v_mfma_f32_16x16x32_bf16 v[96:99], a[0:3], a[80:83], v[96:99]\n"
  "v_mfma_f32_16x16x32_bf16 v[96:99], a[4:7], a[84:87], v[96:99]\n"
  "buffer_load_dwordx4 v203, s[4:7], s26 offen offset:3072 lds\n"
  "s_add_u32 m0, s24, 32768\n"
  "v_mfma_f32_16x16x32_bf16 v[100:103], a[8:11], a[80:83], v[100:103]\n"
  "s_xor_b32 s26, s56, 16384\n"
  "DP3_%=:\n"
  "s_cbranch_execz DPE_%=\n"
  "v_mfma_f32_16x16x32_bf16 v[100:103], a[12:15], a[84:87], v[100:103]\n"
  "v_mfma_f32_16x16x32_bf16 v[104:107], a[16:19], a[80:83], v[104:107]\n"
  "v_mfma_f32_16x16x32_bf16 v[104:107], a[20:23], a[84:87], v[104:107]\n"
  "buffer_load_dwordx4 v211, s[20:23], s26 offen sc1 lds\n"
  "v_mfma_f32_16x16x32_bf16 v[108:111], a[24:27], a[80:83], v[108:111]\n"
  "v_mfma_f32_16x16x32_bf16 v[108:111], a[28:31], a[84:87], v[108:111]\n"
  "v_mfma_f32_16x16x32_bf16 v[112:115], a[0:3], a[88:91], v[112:115]\n"
  "v_mfma_f32_16x16x32_bf16 v[112:115], a[4:7], a[92:95], v[112:115]\n"
  "buffer_load_dwordx4 v211, s[20:23], s26 offen offset:1024 sc1 lds\n"
  "v_mfma_f32_16x16x32_bf16 v[116:119], a[8:11], a[88:91], v[116:119]\n"
  "v_mfma_f32_16x16x32_bf16 v[116:119], a[12:15], a[92:95], v[116:119]\n"
  "v_mfma_f32_16x16x32_bf16 v[120:123], a[16:19], a[88:91], v[120:123]\n"
  "v_mfma_f32_16x16x32_bf16 v[120:123], a[20:23], a[92:95], v[120:123]\n"
  "buffer_load_dwordx4 v211, s[20:23], s26 offen offset:2048 sc1 lds\n"
  "v_mfma_f32_16x16x32_bf16 v[124:127], a[24:27], a[88:91], v[124:127]\n"
  "v_mfma_f32_16x16x32_bf16 v[124:127], a[28:31], a[92:95], v[124:127]\n"
  "s_waitcnt vmcnt(11) lgkmcnt(0)\n"
  "s_barrier\n"
  "v_mfma_f32_16x16x32_bf16 v[0:3], a[96:99], a[128:131], v[0:3]\n"
  "ds_read_b128 a[0:3], v237 offset:32768\n"
  "v_mfma_f32_16x16x32_bf16 v[0:3], a[100:103], a[132:135], v[0:3]\n"
  "buffer_load_dwordx4 v211, s[20:23], s26 offen offset:3072 sc1 lds\n"
  "s_add_u32 m0, s25, 49152\n"
  "v_mfma_f32_16x16x32_bf16 v[4:7], a[104:107], a[128:131], v[4:7]\n"
  "ds_read_b128 a[4:7], v237 offset:40960\n"
  "s_xor_b32 s26, s49, 0\n"
  "v_mfma_f32_16x16x32_bf16 v[4:7], a[108:111], a[132:135], v[4:7]\n"
  "v_mfma_f32_16x16x32_bf16 v[8:11], a[112:115], a[128:131], v[8:11]\n"
  "ds_read_b128 a[8:11], v237 offset:33792\n"
  "v_mfma_f32_16x16x32_bf16 v[8:11], a[116:119], a[132:135], v[8:11]\n"
  "v_mfma_f32_16x16x32_bf16 v[12:15], a[120:123], a[128:131], v[12:15]\n"
  "ds_read_b128 a[12:15], v237 offset:41984\n"
  "v_mfma_f32_16x16x32_bf16 v[12:15], a[124:127], a[132:135], v[12:15]\n"
  "v_mfma_f32_16x16x32_bf16 v[16:19], a[96:99], a[136:139], v[16:19]\n"
  "ds_read_b128 a[16:19], v237 offset:34816\n"
  "v_mfma_f32_16x16x32_bf16 v[16:19], a[100:103], a[140:143], v[16:19]\n"
  "v_mfma_f32_16x16x32_bf16 v[20:23], a[104:107], a[136:139], v[20:23]\n"
  "ds_read_b128 a[20:23], v237 offset:43008\n"
  "v_mfma_f32_16x16x32_bf16 v[20:23], a[108:111], a[140:143], v[20:23]\n"
  "v_mfma_f32_16x16x32_bf16 v[24:27], a[112:115], a[136:139], v[24:27]\n"
  "ds_read_b128 a[24:27], v237 offset:35840\n"
  "v_mfma_f32_16x16x32_bf16 v[24:27], a[116:119], a[140:143], v[24:27]\n"
  "v_mfma_f32_16x16x32_bf16 v[28:31], a[120:123], a[136:139], v[28:31]\n"
  "ds_read_b128 a[28:31], v237 offset:44032\n"
  "v_mfma_f32_16x16x32_bf16 v[28:31], a[124:127], a[140:143], v[28:31]\n"
  "v_mfma_f32_16x16x32_bf16 v[32:35], a[96:99], a[144:147], v[32:35]\n"
  "ds_read_b128 a[32:35], v235 offset:0\n"
  "v_mfma_f32_16x16x32_bf16 v[32:35], a[100:103], a[148:151], v[32:35]\n"
  "v_mfma_f32_16x16x32_bf16 v[36:39], a[104:107], a[144:147], v[36:39]\n"
  "ds_read_b128 a[36:39], v236 offset:0\n"
  "v_mfma_f32_16x16x32_bf16 v[36:39], a[108:111], a[148:151], v[36:39]\n"
  "v_mfma_f32_16x16x32_bf16 v[40:43], a[112:115], a[144:147], v[40:43]\n"
  "ds_read_b128 a[40:43], v235 offset:2048\n"
  "v_mfma_f32_16x16x32_bf16 v[40:43], a[116:119], a[148:151], v[40:43]\n"
  "v_mfma_f32_16x16x32_bf16 v[44:47], a[120:123], a[144:147], v[44:47]\n"
  "ds_read_b128 a[44:47], v236 offset:2048\n"
  "v_mfma_f32_16x16x32_bf16 v[44:47], a[124:127], a[148:151], v[44:47]\n"
  "buffer_load_dwordx4 v200, s[4:7], s26 offen lds\n"
  "v_mfma_f32_16x16x32_bf16 v[48:51], a[96:99], a[152:155], v[48:51]\n"
  "ds_read_b128 a[48:51], v235 offset:4096\n"
  "v_mfma_f32_16x16x32_bf16 v[48:51], a[100:103], a[156:159], v[48:51]\n"
  "v_mfma_f32_16x16x32_bf16 v[52:55], a[104:107], a[152:155], v[52:55]\n"
  "ds_read_b128 a[52:55], v236 offset:4096\n"
  "v_mfma_f32_16x16x32_bf16 v[52:55], a[108:111], a[156:159], v[52:55]\n"
  "v_mfma_f32_16x16x32_bf16 v[56:59], a[112:115], a[152:155], v[56:59]\n"
  "ds_read_b128 a[56:59], v235 offset:6144\n"
  "v_mfma_f32_16x16x32_bf16 v[56:59], a[116:119], a[156:159], v[56:59]\n"
  "buffer_load_dwordx4 v201, s[4:7], s26 offen offset:1024 lds\n"
  "v_mfma_f32_16x16x32_bf16 v[60:63], a[120:123], a[152:155], v[60:63]\n"
  "ds_read_b128 a[60:63], v236 offset:6144\n"
  "v_mfma_f32_16x16x32_bf16 v[60:63], a[124:127], a[156:159], v[60:63]\n"
  "v_mfma_f32_16x16x32_bf16 v[64:67], a[96:99], a[160:163], v[64:67]\n"
  "ds_read_b128 a[64:67], v235 offset:8192\n"
  "v_mfma_f32_16x16x32_bf16 v[64:67], a[100:103], a[164:167], v[64:67]\n"
  "buffer_load_dwordx4 v202, s[4:7], s26 offen offset:2048 lds\n"
  "v_mfma_f32_16x16x32_bf16 v[68:71], a[104:107], a[160:163], v[68:71]\n"
  "ds_read_b128 a[68:71], v236 offset:8192\n"
  "v_mfma_f32_16x16x32_bf16 v[68:71], a[108:111], a[164:167], v[68:71]\n"
  "v_mfma_f32_16x16x32_bf16 v[72:75], a[112:115], a[160:163], v[72:75]\n"
  "ds_read_b128 a[72:75], v235 offset:10240\n"
  "v_mfma_f32_16x16x32_bf16 v[72:75], a[116:119], a[164:167], v[72:75]\n"
  "buffer_load_dwordx4 v203, s[4:7], s26 offen offset:3072 lds\n"
  "s_add_u32 m0, s25, 53248\n"
  "v_mfma_f32_16x16x32_bf16 v[76:79], a[120:123], a[160:163], v[76:79]\n"
  "ds_read_b128 a[76:79], v236 offset:10240\n"
  "s_xor_b32 s26, s49, 131072\n"
  "v_mfma_f32_16x16x32_bf16 v[76:79], a[124:127], a[164:167], v[76:79]\n"
  "v_mfma_f32_16x16x32_bf16 v[80:83], a[96:99], a[168:171], v[80:83]\n"
  "ds_read_b128 a[80:83], v235 offset:12288\n"
  "v_mfma_f32_16x16x32_bf16 v[80:83], a[100:103], a[172:175], v[80:83]\n"
  "v_mfma_f32_16x16x32_bf16 v[84:87], a[104:107], a[168:171], v[84:87]\n"
  "ds_read_b128 a[84:87], v236 offset:12288\n"
  "v_mfma_f32_16x16x32_bf16 v[84:87], a[108:111], a[172:175], v[84:87]\n"
  "v_mfma_f32_16x16x32_bf16 v[88:91], a[112:115], a[168:171], v[88:91]\n"
  "ds_read_b128 a[88:91], v235 offset:14336\n"
  "v_mfma_f32_16x16x32_bf16 v[88:91], a[116:119], a[172:175], v[88:91]\n"
  "v_mfma_f32_16x16x32_bf16 v[92:95], a[120:123], a[168:171], v[92:95]\n"
  "ds_read_b128 a[92:95], v236 offset:14336\n"
  "v_mfma_f32_16x16x32_bf16 v[92:95], a[124:127], a[172:175], v[92:95]\n"
  "v_mfma_f32_16x16x32_bf16 v[96:99], a[96:99], a[176:179], v[96:99]\n"
  "buffer_load_dwordx4 v200, s[4:7], s26 offen lds\n"
  "v_mfma_f32_16x16x32_bf16 v[96:99], a[100:103], a[180:183], v[96:99]\n"
  "v_mfma_f32_16x16x32_bf16 v[100:103], a[104:107], a[176:179], v[100:103]\n"
  "v_mfma_f32_16x16x32_bf16 v[100:103], a[108:111], a[180:183], v[100:103]\n"
  "v_mfma_f32_16x16x32_bf16 v[104:107], a[112:115], a[176:179], v[104:107]\n"
  "buffer_load_dwordx4 v201, s[4:7], s26 offen offset:1024 lds\n"
  "v_mfma_f32_16x16x32_bf16 v[104:107], a[116:119], a[180:183], v[104:107]\n"
  "v_mfma_f32_16x16x32_bf16 v[108:111], a[120:123], a[176:179], v[108:111]\n"
  "v_mfma_f32_16x16x32_bf16 v[108:111], a[124:127], a[180:183], v[108:111]\n"
  "v_mfma_f32_16x16x32_bf16 v[112:115], a[96:99], a[184:187], v[112:115]\n"
  "v_mfma_f32_16x16x32_bf16 v[112:115], a[100:103], a[188:191], v[112:115]\n"
  "buffer_load_dwordx4 v202, s[4:7], s26 offen offset:2048 lds\n"
  "v_mfma_f32_16x16x32_bf16 v[116:119], a[104:107], a[184:187], v[116:119]\n"
  "v_mfma_f32_16x16x32_bf16 v[116:119], a[108:111], a[188:191], v[116:119]\n"
  "v_mfma_f32_16x16x32_bf16 v[120:123], a[112:115], a[184:187], v[120:123]\n"
  "v_mfma_f32_16x16x32_bf16 v[120:123], a[116:119], a[188:191], v[120:123]\n"
  "v_mfma_f32_16x16x32_bf16 v[124:127], a[120:123], a[184:187], v[124:127]\n"
  "buffer_load_dwordx4 v203, s[4:7], s26 offen offset:3072 lds\n"
  "s_add_u32 m0, s24, 81920\n"
  "v_mfma_f32_16x16x32_bf16 v[124:127], a[124:127], a[188:191], v[124:127]\n"
  "s_waitcnt vmcnt(8) lgkmcnt(0)\n"
  "s_barrier\n"
  "v_mfma_f32_16x16x32_bf16 v[0:3], a[0:3], a[32:35], v[0:3]\n"
  "ds_read_b128 a[96:99], v210 offset:32768\n"
  "s_xor_b32 s26, s57, 0\n"
  "v_mfma_f32_16x16x32_bf16 v[0:3], a[4:7], a[36:39], v[0:3]\n"
  "v_mfma_f32_16x16x32_bf16 v[4:7], a[8:11], a[32:35], v[4:7]\n"
  "ds_read_b128 a[100:103], v210 offset:40960\n"
  "v_mfma_f32_16x16x32_bf16 v[4:7], a[12:15], a[36:39], v[4:7]\n"
  "buffer_load_dwordx4 v211, s[20:23], s26 offen sc1 lds\n"
  "v_mfma_f32_16x16x32_bf16 v[8:11], a[16:19], a[32:35], v[8:11]\n"
  "ds_read_b128 a[104:107], v210 offset:33792\n"
  "v_mfma_f32_16x16x32_bf16 v[8:11], a[20:23], a[36:39], v[8:11]\n"
  "v_mfma_f32_16x16x32_bf16 v[12:15], a[24:27], a[32:35], v[12:15]\n"
  "ds_read_b128 a[108:111], v210 offset:41984\n"
  "v_mfma_f32_16x16x32_bf16 v[12:15], a[28:31], a[36:39], v[12:15]\n"
  "buffer_load_dwordx4 v211, s[20:23], s26 offen offset:1024 sc1 lds\n"
  "v_mfma_f32_16x16x32_bf16 v[16:19], a[0:3], a[40:43], v[16:19]\n"
  "ds_read_b128 a[112:115], v210 offset:34816\n"
  "v_mfma_f32_16x16x32_bf16 v[16:19], a[4:7], a[44:47], v[16:19]\n"
  "v_mfma_f32_16x16x32_bf16 v[20:23], a[8:11], a[40:43], v[20:23]\n"
  "ds_read_b128 a[116:119], v210 offset:43008\n"
  "v_mfma_f32_16x16x32_bf16 v[20:23], a[12:15], a[44:47], v[20:23]\n"
  "v_mfma_f32_16x16x32_bf16 v[24:27], a[16:19], a[40:43], v[24:27]\n"
  "ds_read_b128 a[120:123], v210 offset:35840\n"
  "v_mfma_f32_16x16x32_bf16 v[24:27], a[20:23], a[44:47], v[24:27]\n"
  "buffer_load_dwordx4 v211, s[20:23], s26 offen offset:2048 sc1 lds\n"
  "v_mfma_f32_16x16x32_bf16 v[28:31], a[24:27], a[40:43], v[28:31]\n"
  "ds_read_b128 a[124:127], v210 offset:44032\n"
  "v_mfma_f32_16x16x32_bf16 v[28:31], a[28:31], a[44:47], v[28:31]\n"
  "v_mfma_f32_16x16x32_bf16 v[32:35], a[0:3], a[48:51], v[32:35]\n"
  "ds_read_b128 a[128:131], v208 offset:0\n"
  "v_mfma_f32_16x16x32_bf16 v[32:35], a[4:7], a[52:55], v[32:35]\n"
  "buffer_load_dwordx4 v211, s[20:23], s26 offen offset:3072 sc1 lds\n"
  "s_add_u32 m0, s25, 98304\n"
  "v_mfma_f32_16x16x32_bf16 v[36:39], a[8:11], a[48:51], v[36:39]\n"
  "ds_read_b128 a[132:135], v209 offset:0\n"
  "s_xor_b32 s26, s49, 128\n"
  "v_mfma_f32_16x16x32_bf16 v[36:39], a[12:15], a[52:55], v[36:39]\n"
  "v_mfma_f32_16x16x32_bf16 v[40:43], a[16:19], a[48:51], v[40:43]\n"
  "ds_read_b128 a[136:139], v208 offset:2048\n"
  "v_mfma_f32_16x16x32_bf16 v[40:43], a[20:23], a[52:55], v[40:43]\n"
  "buffer_load_dwordx4 v200, s[4:7], s26 offen lds\n"
  "v_mfma_f32_16x16x32_bf16 v[44:47], a[24:27], a[48:51], v[44:47]\n"
  "ds_read_b128 a[140:143], v209 offset:2048\n"
  "v_mfma_f32_16x16x32_bf16 v[44:47], a[28:31], a[52:55], v[44:47]\n"
  "v_mfma_f32_16x16x32_bf16 v[48:51], a[0:3], a[56:59], v[48:51]\n"
  "ds_read_b128 a[144:147], v208 offset:4096\n"
  "v_mfma_f32_16x16x32_bf16 v[48:51], a[4:7], a[60:63], v[48:51]\n"
  "buffer_load_dwordx4 v201, s[4:7], s26 offen offset:1024 lds\n"
  "v_mfma_f32_16x16x32_bf16 v[52:55], a[8:11], a[56:59], v[52:55]\n"
  "ds_read_b128 a[148:151], v209 offset:4096\n"
  "v_mfma_f32_16x16x32_bf16 v[52:55], a[12:15], a[60:63], v[52:55]\n"
  "v_mfma_f32_16x16x32_bf16 v[56:59], a[16:19], a[56:59], v[56:59]\n"
  "ds_read_b128 a[152:155], v208 offset:6144\n"
  "v_mfma_f32_16x16x32_bf16 v[56:59], a[20:23], a[60:63], v[56:59]\n"
  "buffer_load_dwordx4 v202, s[4:7], s26 offen offset:2048 lds\n"
  "v_mfma_f32_16x16x32_bf16 v[60:63], a[24:27], a[56:59], v[60:63]\n"
  "ds_read_b128 a[156:159], v209 offset:6144\n"
  "v_mfma_f32_16x16x32_bf16 v[60:63], a[28:31], a[60:63], v[60:63]\n"
  "v_mfma_f32_16x16x32_bf16 v[64:67], a[0:3], a[64:67], v[64:67]\n"
  "ds_read_b128 a[160:163], v208 offset:8192\n"
  "v_mfma_f32_16x16x32_bf16 v[64:67], a[4:7], a[68:71], v[64:67]\n"
  "buffer_load_dwordx4 v203, s[4:7], s26 offen offset:3072 lds\n"
  "s_add_u32 m0, s25, 102400\n"
  "v_mfma_f32_16x16x32_bf16 v[68:71], a[8:11], a[64:67], v[68:71]\n"
  "ds_read_b128 a[164:167], v209 offset:8192\n"
  "s_xor_b32 s26, s49, 131200\n"
  "v_mfma_f32_16x16x32_bf16 v[68:71], a[12:15], a[68:71], v[68:71]\n"
  "v_mfma_f32_16x16x32_bf16 v[72:75], a[16:19], a[64:67], v[72:75]\n"
  "ds_read_b128 a[168:171], v208 offset:10240\n"
  "v_mfma_f32_16x16x32_bf16 v[72:75], a[20:23], a[68:71], v[72:75]\n"
  "buffer_load_dwordx4 v200, s[4:7], s26 offen lds\n"
  "v_mfma_f32_16x16x32_bf16 v[76:79], a[24:27], a[64:67], v[76:79]\n"
  "ds_read_b128 a[172:175], v209 offset:10240\n"
  "v_mfma_f32_16x16x32_bf16 v[76:79], a[28:31], a[68:71], v[76:79]\n"
  "v_mfma_f32_16x16x32_bf16 v[80:83], a[0:3], a[72:75], v[80:83]\n"
  "ds_read_b128 a[176:179], v208 offset:12288\n"
  "v_mfma_f32_16x16x32_bf16 v[80:83], a[4:7], a[76:79], v[80:83]\n"
  "buffer_load_dwordx4 v201, s[4:7], s26 offen offset:1024 lds\n"
  "v_mfma_f32_16x16x32_bf16 v[84:87], a[8:11], a[72:75], v[84:87]\n"
  "ds_read_b128 a[180:183], v209 offset:12288\n"
  "v_mfma_f32_16x16x32_bf16 v[84:87], a[12:15], a[76:79], v[84:87]\n"
  "v_mfma_f32_16x16x32_bf16 v[88:91], a[16:19], a[72:75], v[88:91]\n"
  "ds_read_b128 a[184:187], v208 offset:14336\n"
  "v_mfma_f32_16x16x32_bf16 v[88:91], a[20:23], a[76:79], v[88:91]\n"
  "buffer_load_dwordx4 v202, s[4:7], s26 offen offset:2048 lds\n"
  "v_mfma_f32_16x16x32_bf16 v[92:95], a[24:27], a[72:75], v[92:95]\n"
  "ds_read_b128 a[188:191], v209 offset:14336\n"
  "v_mfma_f32_16x16x32_bf16 v[92:95], a[28:31], a[76:79], v[92:95]\n"
  "v_mfma_f32_16x16x32_bf16 v[96:99], a[0:3], a[80:83], v[96:99]\n"
  "v_mfma_f32_16x16x32_bf16 v[96:99], a[4:7], a[84:87], v[96:99]\n"
  "buffer_load_dwordx4 v203, s[4:7], s26 offen offset:3072 lds\n"
  "s_add_u32 m0, s24, 131072\n"
  "v_mfma_f32_16x16x32_bf16 v[100:103], a[8:11], a[80:83], v[100:103]\n"
  "s_xor_b32 s26, s57, 16384\n"
  "v_mfma_f32_16x16x32_bf16 v[100:103], a[12:15], a[84:87], v[100:103]\n"
  "v_mfma_f32_16x16x32_bf16 v[104:107], a[16:19], a[80:83], v[104:107]\n"
  "v_mfma_f32_16x16x32_bf16 v[104:107], a[20:23], a[84:87], v[104:107]\n"
  "buffer_load_dwordx4 v211, s[20:23], s26 offen sc1 lds\n"
  "v_mfma_f32_16x16x32_bf16 v[108:111], a[24:27], a[80:83], v[108:111]\n"
  "v_mfma_f32_16x16x32_bf16 v[108:111], a[28:31], a[84:87], v[108:111]\n"
  "v_mfma_f32_16x16x32_bf16 v[112:115], a[0:3], a[88:91], v[112:115]\n"
  "v_mfma_f32_16x16x32_bf16 v[112:115], a[4:7], a[92:95], v[112:115]\n"
  "buffer_load_dwordx4 v211, s[20:23], s26 offen offset:1024 sc1 lds\n"
  "v_mfma_f32_16x16x32_bf16 v[116:119], a[8:11], a[88:91], v[116:119]\n"
  "v_mfma_f32_16x16x32_bf16 v[116:119], a[12:15], a[92:95], v[116:119]\n"
  "v_mfma_f32_16x16x32_bf16 v[120:123], a[16:19], a[88:91], v[120:123]\n"
  "v_mfma_f32_16x16x32_bf16 v[120:123], a[20:23], a[92:95], v[120:123]\n"
  "buffer_load_dwordx4 v211, s[20:23], s26 offen offset:2048 sc1 lds\n"
  "v_mfma_f32_16x16x32_bf16 v[124:127], a[24:27], a[88:91], v[124:127]\n"
  "v_mfma_f32_16x16x32_bf16 v[124:127], a[28:31], a[92:95], v[124:127]\n"
  "s_waitcnt lgkmcnt(0)\n"
  "s_barrier\n"
  "v_mfma_f32_16x16x32_bf16 v[0:3], a[96:99], a[128:131], v[0:3]\n"
  "v_mfma_f32_16x16x32_bf16 v[0:3], a[100:103], a[132:135], v[0:3]\n"
  "buffer_load_dwordx4 v211, s[20:23], s26 offen offset:3072 sc1 lds\n"
  "s_add_u32 m0, s25, 0\n"
  "v_mfma_f32_16x16x32_bf16 v[4:7], a[104:107], a[128:131], v[4:7]\n"
  "s_xor_b32 s26, s50, 0\n"
  "v_mfma_f32_16x16x32_bf16 v[4:7], a[108:111], a[132:135], v[4:7]\n"
  "v_mfma_f32_16x16x32_bf16 v[8:11], a[112:115], a[128:131], v[8:11]\n"
  "v_mfma_f32_16x16x32_bf16 v[8:11], a[116:119], a[132:135], v[8:11]\n"
  "v_mfma_f32_16x16x32_bf16 v[12:15], a[120:123], a[128:131], v[12:15]\n"
  "v_mfma_f32_16x16x32_bf16 v[12:15], a[124:127], a[132:135], v[12:15]\n"
  "v_mfma_f32_16x16x32_bf16 v[16:19], a[96:99], a[136:139], v[16:19]\n"
  "v_mfma_f32_16x16x32_bf16 v[16:19], a[100:103], a[140:143], v[16:19]\n"
  "v_mfma_f32_16x16x32_bf16 v[20:23], a[104:107], a[136:139], v[20:23]\n"
  "v_mfma_f32_16x16x32_bf16 v[20:23], a[108:111], a[140:143], v[20:23]\n"
  "v_mfma_f32_16x16x32_bf16 v[24:27], a[112:115], a[136:139], v[24:27]\n"
  "v_mfma_f32_16x16x32_bf16 v[24:27], a[116:119], a[140:143], v[24:27]\n"
  "v_mfma_f32_16x16x32_bf16 v[28:31], a[120:123], a[136:139], v[28:31]\n"
  "buffer_load_dwordx4 v200, s[4:7], s26 offen lds\n"
  "v_mfma_f32_16x16x32_bf16 v[28:31], a[124:127], a[140:143], v[28:31]\n"
  "v_mfma_f32_16x16x32_bf16 v[32:35], a[96:99], a[144:147], v[32:35]\n"
  "v_mfma_f32_16x16x32_bf16 v[32:35], a[100:103], a[148:151], v[32:35]\n"
  "v_mfma_f32_16x16x32_bf16 v[36:39], a[104:107], a[144:147], v[36:39]\n"
  "v_mfma_f32_16x16x32_bf16 v[36:39], a[108:111], a[148:151], v[36:39]\n"
  "buffer_load_dwordx4 v201, s[4:7], s26 offen offset:1024 lds\n"
  "v_mfma_f32_16x16x32_bf16 v[40:43], a[112:115], a[144:147], v[40:43]\n"
  "v_mfma_f32_16x16x32_bf16 v[40:43], a[116:119], a[148:151], v[40:43]\n"
  "s_waitcnt vmcnt(14)\n"
  "s_barrier\n"
  "v_mfma_f32_16x16x32_bf16 v[44:47], a[120:123], a[144:147], v[44:47]\n"
  "ds_read_b128 a[0:3], v230 offset:32768\n"
  "v_mfma_f32_16x16x32_bf16 v[44:47], a[124:127], a[148:151], v[44:47]\n"
  "ds_read_b128 a[4:7], v230 offset:40960\n"
  "v_mfma_f32_16x16x32_bf16 v[48:51], a[96:99], a[152:155], v[48:51]\n"
  "ds_read_b128 a[8:11], v230 offset:33792\n"
  "v_mfma_f32_16x16x32_bf16 v[48:51], a[100:103], a[156:159], v[48:51]\n"
  "buffer_load_dwordx4 v202, s[4:7], s26 offen offset:2048 lds\n"
  "v_mfma_f32_16x16x32_bf16 v[52:55], a[104:107], a[152:155], v[52:55]\n"
  "ds_read_b128 a[12:15], v230 offset:41984\n"
  "v_mfma_f32_16x16x32_bf16 v[52:55], a[108:111], a[156:159], v[52:55]\n"
  "ds_read_b128 a[16:19], v230 offset:34816\n"
  "v_mfma_f32_16x16x32_bf16 v[56:59], a[112:115], a[152:155], v[56:59]\n"
  "ds_read_b128 a[20:23], v230 offset:43008\n"
  "v_mfma_f32_16x16x32_bf16 v[56:59], a[116:119], a[156:159], v[56:59]\n"
  "buffer_load_dwordx4 v203, s[4:7], s26 offen offset:3072 lds\n"
  "s_add_u32 m0, s25, 4096\n"
  "v_mfma_f32_16x16x32_bf16 v[60:63], a[120:123], a[152:155], v[60:63]\n"
  "ds_read_b128 a[24:27], v230 offset:35840\n"
  "s_xor_b32 s26, s50, 131072\n"
  "v_mfma_f32_16x16x32_bf16 v[60:63], a[124:127], a[156:159], v[60:63]\n"
  "ds_read_b128 a[28:31], v230 offset:44032\n"
  "v_mfma_f32_16x16x32_bf16 v[64:67], a[96:99], a[160:163], v[64:67]\n"
  "ds_read_b128 a[32:35], v228 offset:0\n"
  "v_mfma_f32_16x16x32_bf16 v[64:67], a[100:103], a[164:167], v[64:67]\n"
  "buffer_load_dwordx4 v200, s[4:7], s26 offen lds\n"
  "v_mfma_f32_16x16x32_bf16 v[68:71], a[104:107], a[160:163], v[68:71]\n"
  "ds_read_b128 a[36:39], v229 offset:0\n"
  "v_mfma_f32_16x16x32_bf16 v[68:71], a[108:111], a[164:167], v[68:71]\n"
  "ds_read_b128 a[40:43], v228 offset:2048\n"
  "v_mfma_f32_16x16x32_bf16 v[72:75], a[112:115], a[160:163], v[72:75]\n"
  "ds_read_b128 a[44:47], v229 offset:2048\n"
  "v_mfma_f32_16x16x32_bf16 v[72:75], a[116:119], a[164:167], v[72:75]\n"
  "buffer_load_dwordx4 v201, s[4:7], s26 offen offset:1024 lds\n"
  "v_mfma_f32_16x16x32_bf16 v[76:79], a[120:123], a[160:163], v[76:79]\n"
  "ds_read_b128 a[48:51], v228 offset:4096\n"
  "v_mfma_f32_16x16x32_bf16 v[76:79], a[124:127], a[164:167], v[76:79]\n"
  "ds_read_b128 a[52:55], v229 offset:4096\n"
  "v_mfma_f32_16x16x32_bf16 v[80:83], a[96:99], a[168:171], v[80:83]\n"
  "ds_read_b128 a[56:59], v228 offset:6144\n"
  "v_mfma_f32_16x16x32_bf16 v[80:83], a[100:103], a[172:175], v[80:83]\n"
  "v_mfma_f32_16x16x32_bf16 v[84:87], a[104:107], a[168:171], v[84:87]\n"
  "ds_read_b128 a[60:63], v229 offset:6144\n"
  "v_mfma_f32_16x16x32_bf16 v[84:87], a[108:111], a[172:175], v[84:87]\n"
  "buffer_load_dwordx4 v202, s[4:7], s26 offen offset:2048 lds\n"
  "v_mfma_f32_16x16x32_bf16 v[88:91], a[112:115], a[168:171], v[88:91]\n"
  "ds_read_b128 a[64:67], v228 offset:8192\n"
  "v_mfma_f32_16x16x32_bf16 v[88:91], a[116:119], a[172:175], v[88:91]\n"
  "v_mfma_f32_16x16x32_bf16 v[92:95], a[120:123], a[168:171], v[92:95]\n"
  "ds_read_b128 a[68:71], v229 offset:8192\n"
  "v_mfma_f32_16x16x32_bf16 v[92:95], a[124:127], a[172:175], v[92:95]\n"
  "buffer_load_dwordx4 v203, s[4:7], s26 offen offset:3072 lds\n"
  "s_add_u32 m0, s24, 32768\n"
  "v_mfma_f32_16x16x32_bf16 v[96:99], a[96:99], a[176:179], v[96:99]\n"
  "ds_read_b128 a[72:75], v228 offset:10240\n"
  "s_xor_b32 s26, s58, 0\n"
  "v_mfma_f32_16x16x32_bf16 v[96:99], a[100:103], a[180:183], v[96:99]\n"
  "v_mfma_f32_16x16x32_bf16 v[100:103], a[104:107], a[176:179], v[100:103]\n"
  "ds_read_b128 a[76:79], v229 offset:10240\n"
  "v_mfma_f32_16x16x32_bf16 v[100:103], a[108:111], a[180:183], v[100:103]\n"
  "v_mfma_f32_16x16x32_bf16 v[104:107], a[112:115], a[176:179], v[104:107]\n"
  "ds_read_b128 a[80:83], v228 offset:12288\n"
  "v_mfma_f32_16x16x32_bf16 v[104:107], a[116:119], a[180:183], v[104:107]\n"
  "v_mfma_f32_16x16x32_bf16 v[108:111], a[120:123], a[176:179], v[108:111]\n"
  "ds_read_b128 a[84:87], v229 offset:12288\n"
  "v_mfma_f32_16x16x32_bf16 v[108:111], a[124:127], a[180:183], v[108:111]\n"
  "v_mfma_f32_16x16x32_bf16 v[112:115], a[96:99], a[184:187], v[112:115]\n"
  "ds_read_b128 a[88:91], v228 offset:14336\n"
  "ds_read_b128 a[92:95], v229 offset:14336\n"
  "v_mfma_f32_16x16x32_bf16 v[112:115], a[100:103], a[188:191], v[112:115]\n"
  "v_mfma_f32_16x16x32_bf16 v[116:119], a[104:107], a[184:187], v[116:119]\n"
  "v_mfma_f32_16x16x32_bf16 v[116:119], a[108:111], a[188:191], v[116:119]\n"
  "v_mfma_f32_16x16x32_bf16 v[120:123], a[112:115], a[184:187], v[120:123]\n"
  "v_mfma_f32_16x16x32_bf16 v[120:123], a[116:119], a[188:191], v[120:123]\n"
  "v_mfma_f32_16x16x32_bf16 v[124:127], a[120:123], a[184:187], v[124:127]\n"
  "v_mfma_f32_16x16x32_bf16 v[124:127], a[124:127], a[188:191], v[124:127]\n"
  "s_waitcnt vmcnt(8) lgkmcnt(0)\n"
  "s_barrier\n"
  "v_mfma_f32_16x16x32_bf16 v[0:3], a[0:3], a[32:35], v[0:3]\n"
  "ds_read_b128 a[96:99], v237 offset:32768\n"
  "v_mfma_f32_16x16x32_bf16 v[0:3], a[4:7], a[36:39], v[0:3]\n"
  "v_mfma_f32_16x16x32_bf16 v[4:7], a[8:11], a[32:35], v[4:7]\n"
  "ds_read_b128 a[100:103], v237 offset:40960\n"
  "v_mfma_f32_16x16x32_bf16 v[4:7], a[12:15], a[36:39], v[4:7]\n"
  "v_mfma_f32_16x16x32_bf16 v[8:11], a[16:19], a[32:35], v[8:11]\n"
  "ds_read_b128 a[104:107], v237 offset:33792\n"
  "v_mfma_f32_16x16x32_bf16 v[8:11], a[20:23], a[36:39], v[8:11]\n"
  "v_mfma_f32_16x16x32_bf16 v[12:15], a[24:27], a[32:35], v[12:15]\n"
  "ds_read_b128 a[108:111], v237 offset:41984\n"
  "v_mfma_f32_16x16x32_bf16 v[12:15], a[28:31], a[36:39], v[12:15]\n"
  "v_mfma_f32_16x16x32_bf16 v[16:19], a[0:3], a[40:43], v[16:19]\n"
  "ds_read_b128 a[112:115], v237 offset:34816\n"
  "v_mfma_f32_16x16x32_bf16 v[16:19], a[4:7], a[44:47], v[16:19]\n"
  "v_mfma_f32_16x16x32_bf16 v[20:23], a[8:11], a[40:43], v[20:23]\n"
  "ds_read_b128 a[116:119], v237 offset:43008\n"
  "v_mfma_f32_16x16x32_bf16 v[20:23], a[12:15], a[44:47], v[20:23]\n"
  "buffer_load_dwordx4 v211, s[20:23], s26 offen sc1 lds\n"
  "v_mfma_f32_16x16x32_bf16 v[24:27], a[16:19], a[40:43], v[24:27]\n"
  "ds_read_b128 a[120:123], v237 offset:35840\n"
  "v_mfma_f32_16x16x32_bf16 v[24:27], a[20:23], a[44:47], v[24:27]\n"
  "v_mfma_f32_16x16x32_bf16 v[28:31], a[24:27], a[40:43], v[28:31]\n"
  "ds_read_b128 a[124:127], v237 offset:44032\n"
  "v_mfma_f32_16x16x32_bf16 v[28:31], a[28:31], a[44:47], v[28:31]\n"
  "buffer_load_dwordx4 v211, s[20:23], s26 offen offset:1024 sc1 lds\n"
  "v_mfma_f32_16x16x32_bf16 v[32:35], a[0:3], a[48:51], v[32:35]\n"
  "ds_read_b128 a[128:131], v235 offset:0\n"
  "v_mfma_f32_16x16x32_bf16 v[32:35], a[4:7], a[52:55], v[32:35]\n"
  "v_mfma_f32_16x16x32_bf16 v[36:39], a[8:11], a[48:51], v[36:39]\n"
  "ds_read_b128 a[132:135], v236 offset:0\n"
  "v_mfma_f32_16x16x32_bf16 v[36:39], a[12:15], a[52:55], v[36:39]\n"
  "buffer_load_dwordx4 v211, s[20:23], s26 offen offset:2048 sc1 lds\n"
  "v_mfma_f32_16x16x32_bf16 v[40:43], a[16:19], a[48:51], v[40:43]\n"
  "ds_read_b128 a[136:139], v235 offset:2048\n"
  "v_mfma_f32_16x16x32_bf16 v[40:43], a[20:23], a[52:55], v[40:43]\n"
  "v_mfma_f32_16x16x32_bf16 v[44:47], a[24:27], a[48:51], v[44:47]\n"
  "ds_read_b128 a[140:143], v236 offset:2048\n"
  "v_mfma_f32_16x16x32_bf16 v[44:47], a[28:31], a[52:55], v[44:47]\n"
  "buffer_load_dwordx4 v211, s[20:23], s26 offen offset:3072 sc1 lds\n"
  "s_add_u32 m0, s25, 49152\n"
  "v_mfma_f32_16x16x32_bf16 v[48:51], a[0:3], a[56:59], v[48:51]\n"
  "ds_read_b128 a[144:147], v235 offset:4096\n"
  "s_xor_b32 s26, s50, 128\n"
  "v_mfma_f32_16x16x32_bf16 v[48:51], a[4:7], a[60:63], v[48:51]\n"
  "v_mfma_f32_16x16x32_bf16 v[52:55], a[8:11], a[56:59], v[52:55]\n"
  "ds_read_b128 a[148:151], v236 offset:4096\n"
  "v_mfma_f32_16x16x32_bf16 v[52:55], a[12:15], a[60:63], v[52:55]\n"
  "v_mfma_f32_16x16x32_bf16 v[56:59], a[16:19], a[56:59], v[56:59]\n"
  "ds_read_b128 a[152:155], v235 offset:6144\n"
  "v_mfma_f32_16x16x32_bf16 v[56:59], a[20:23], a[60:63], v[56:59]\n"
  "v_mfma_f32_16x16x32_bf16 v[60:63], a[24:27], a[56:59], v[60:63]\n"
  "ds_read_b128 a[156:159], v236 offset:6144\n"
  "v_mfma_f32_16x16x32_bf16 v[60:63], a[28:31], a[60:63], v[60:63]\n"
  "v_mfma_f32_16x16x32_bf16 v[64:67], a[0:3], a[64:67], v[64:67]\n"
  "ds_read_b128 a[160:163], v235 offset:8192\n"
  "v_mfma_f32_16x16x32_bf16 v[64:67], a[4:7], a[68:71], v[64:67]\n"
  "v_mfma_f32_16x16x32_bf16 v[68:71], a[8:11], a[64:67], v[68:71]\n"
  "ds_read_b128 a[164:167], v236 offset:8192\n"
  "v_mfma_f32_16x16x32_bf16 v[68:71], a[12:15], a[68:71], v[68:71]\n"
  "v_mfma_f32_16x16x32_bf16 v[72:75], a[16:19], a[64:67], v[72:75]\n"
  "ds_read_b128 a[168:171], v235 offset:10240\n"
  "v_mfma_f32_16x16x32_bf16 v[72:75], a[20:23], a[68:71], v[72:75]\n"
  "v_mfma_f32_16x16x32_bf16 v[76:79], a[24:27], a[64:67], v[76:79]\n"
  "ds_read_b128 a[172:175], v236 offset:10240\n"
  "v_mfma_f32_16x16x32_bf16 v[76:79], a[28:31], a[68:71], v[76:79]\n"
  "v_mfma_f32_16x16x32_bf16 v[80:83], a[0:3], a[72:75], v[80:83]\n"
  "ds_read_b128 a[176:179], v235 offset:12288\n"
  "v_mfma_f32_16x16x32_bf16 v[80:83], a[4:7], a[76:79], v[80:83]\n"
  "v_mfma_f32_16x16x32_bf16 v[84:87], a[8:11], a[72:75], v[84:87]\n"
  "ds_read_b128 a[180:183], v236 offset:12288\n"
  "v_mfma_f32_16x16x32_bf16 v[84:87], a[12:15], a[76:79], v[84:87]\n"
  "buffer_load_dwordx4 v200, s[4:7], s26 offen lds\n"
  "v_mfma_f32_16x16x32_bf16 v[88:91], a[16:19], a[72:75], v[88:91]\n"
  "ds_read_b128 a[184:187], v235 offset:14336\n"
  "v_mfma_f32_16x16x32_bf16 v[88:91], a[20:23], a[76:79], v[88:91]\n"
  "v_mfma_f32_16x16x32_bf16 v[92:95], a[24:27], a[72:75], v[92:95]\n"
  "ds_read_b128 a[188:191], v236 offset:14336\n"
  "v_mfma_f32_16x16x32_bf16 v[92:95], a[28:31], a[76:79], v[92:95]\n"
  "buffer_load_dwordx4 v201, s[4:7], s26 offen offset:1024 lds\n"
  "v_mfma_f32_16x16x32_bf16 v[96:99], a[0:3], a[80:83], v[96:99]\n"
  "v_mfma_f32_16x16x32_bf16 v[96:99], a[4:7], a[84:87], v[96:99]\n"
  "v_mfma_f32_16x16x32_bf16 v[100:103], a[8:11], a[80:83], v[100:103]\n"
  "v_mfma_f32_16x16x32_bf16 v[100:103], a[12:15], a[84:87], v[100:103]\n"
  "buffer_load_dwordx4 v202, s[4:7], s26 offen offset:2048 lds\n"
  "v_mfma_f32_16x16x32_bf16 v[104:107], a[16:19], a[80:83], v[104:107]\n"
  "v_mfma_f32_16x16x32_bf16 v[104:107], a[20:23], a[84:87], v[104:107]\n"
  "v_mfma_f32_16x16x32_bf16 v[108:111], a[24:27], a[80:83], v[108:111]\n"
  "v_mfma_f32_16x16x32_bf16 v[108:111], a[28:31], a[84:87], v[108:111]\n"
  "buffer_load_dwordx4 v203, s[4:7], s26 offen offset:3072 lds\n"
  "s_add_u32 m0, s25, 53248\n"
  "v_mfma_f32_16x16x32_bf16 v[112:115], a[0:3], a[88:91], v[112:115]\n"
  "s_xor_b32 s26, s50, 131200\n"
  "v_mfma_f32_16x16x32_bf16 v[112:115], a[4:7], a[92:95], v[112:115]\n"
  "v_mfma_f32_16x16x32_bf16 v[116:119], a[8:11], a[88:91], v[116:119]\n"
  "v_mfma_f32_16x16x32_bf16 v[116:119], a[12:15], a[92:95], v[116:119]\n"
  "buffer_load_dwordx4 v200, s[4:7], s26 offen lds\n"
  "v_mfma_f32_16x16x32_bf16 v[120:123], a[16:19], a[88:91], v[120:123]\n"
  "v_mfma_f32_16x16x32_bf16 v[120:123], a[20:23], a[92:95], v[120:123]\n"
  "v_mfma_f32_16x16x32_bf16 v[124:127], a[24:27], a[88:91], v[124:127]\n"
  "v_mfma_f32_16x16x32_bf16 v[124:127], a[28:31], a[92:95], v[124:127]\n"
  "s_waitcnt lgkmcnt(0)\n"
  "s_barrier\n"
  "v_mfma_f32_16x16x32_bf16 v[0:3], a[96:99], a[128:131], v[0:3]\n"
  "buffer_load_dwordx4 v201, s[4:7], s26 offen offset:1024 lds\n"
  "v_mfma_f32_16x16x32_bf16 v[0:3], a[100:103], a[132:135], v[0:3]\n"
  "v_mfma_f32_16x16x32_bf16 v[4:7], a[104:107], a[128:131], v[4:7]\n"
  "v_mfma_f32_16x16x32_bf16 v[4:7], a[108:111], a[132:135], v[4:7]\n"
  "v_mfma_f32_16x16x32_bf16 v[8:11], a[112:115], a[128:131], v[8:11]\n"
  "buffer_load_dwordx4 v202, s[4:7], s26 offen offset:2048 lds\n"
  "v_mfma_f32_16x16x32_bf16 v[8:11], a[116:119], a[132:135], v[8:11]\n"
  "v_mfma_f32_16x16x32_bf16 v[12:15], a[120:123], a[128:131], v[12:15]\n"
  "v_mfma_f32_16x16x32_bf16 v[12:15], a[124:127], a[132:135], v[12:15]\n"
  "v_mfma_f32_16x16x32_bf16 v[16:19], a[96:99], a[136:139], v[16:19]\n"
  "buffer_load_dwordx4 v203, s[4:7], s26 offen offset:3072 lds\n"
  "s_add_u32 m0, s24, 81920\n"
  "v_mfma_f32_16x16x32_bf16 v[16:19], a[100:103], a[140:143], v[16:19]\n"
  "s_xor_b32 s26, s58, 16384\n"
  "v_mfma_f32_16x16x32_bf16 v[20:23], a[104:107], a[136:139], v[20:23]\n"
  "v_mfma_f32_16x16x32_bf16 v[20:23], a[108:111], a[140:143], v[20:23]\n"
  "v_mfma_f32_16x16x32_bf16 v[24:27], a[112:115], a[136:139], v[24:27]\n"
  "buffer_load_dwordx4 v211, s[20:23], s26 offen sc1 lds\n"
  "v_mfma_f32_16x16x32_bf16 v[24:27], a[116:119], a[140:143], v[24:27]\n"
  "v_mfma_f32_16x16x32_bf16 v[28:31], a[120:123], a[136:139], v[28:31]\n"
  "v_mfma_f32_16x16x32_bf16 v[28:31], a[124:127], a[140:143], v[28:31]\n"
  "v_mfma_f32_16x16x32_bf16 v[32:35], a[96:99], a[144:147], v[32:35]\n"
  "buffer_load_dwordx4 v211, s[20:23], s26 offen offset:1024 sc1 lds\n"
  "v_mfma_f32_16x16x32_bf16 v[32:35], a[100:103], a[148:151], v[32:35]\n"
  "v_mfma_f32_16x16x32_bf16 v[36:39], a[104:107], a[144:147], v[36:39]\n"
  "v_mfma_f32_16x16x32_bf16 v[36:39], a[108:111], a[148:151], v[36:39]\n"
  "v_mfma_f32_16x16x32_bf16 v[40:43], a[112:115], a[144:147], v[40:43]\n"
  "buffer_load_dwordx4 v211, s[20:23], s26 offen offset:2048 sc1 lds\n"
  "v_mfma_f32_16x16x32_bf16 v[40:43], a[116:119], a[148:151], v[40:43]\n"
  "s_waitcnt vmcnt(11)\n"
  "s_barrier\n"
  "v_mfma_f32_16x16x32_bf16 v[44:47], a[120:123], a[144:147], v[44:47]\n"
  "ds_read_b128 a[0:3], v210 offset:32768\n"
  "v_mfma_f32_16x16x32_bf16 v[44:47], a[124:127], a[148:151], v[44:47]\n"
  "ds_read_b128 a[4:7], v210 offset:40960\n"
  "v_mfma_f32_16x16x32_bf16 v[48:51], a[96:99], a[152:155], v[48:51]\n"
  "ds_read_b128 a[8:11], v210 offset:33792\n"
  "v_mfma_f32_16x16x32_bf16 v[48:51], a[100:103], a[156:159], v[48:51]\n"
  "buffer_load_dwordx4 v211, s[20:23], s26 offen offset:3072 sc1 lds\n"
  "s_add_u32 m0, s25, 98304\n"
  "v_mfma_f32_16x16x32_bf16 v[52:55], a[104:107], a[152:155], v[52:55]\n"
  "ds_read_b128 a[12:15], v210 offset:41984\n"
  "s_xor_b32 s26, s51, 0\n"
  "v_mfma_f32_16x16x32_bf16 v[52:55], a[108:111], a[156:159], v[52:55]\n"
  "ds_read_b128 a[16:19], v210 offset:34816\n"
  "v_mfma_f32_16x16x32_bf16 v[56:59], a[112:115], a[152:155], v[56:59]\n"
  "ds_read_b128 a[20:23], v210 offset:43008\n"
  "v_mfma_f32_16x16x32_bf16 v[56:59], a[116:119], a[156:159], v[56:59]\n"
  "buffer_load_dwordx4 v200, s[4:7], s26 offen lds\n"
  "v_mfma_f32_16x16x32_bf16 v[60:63], a[120:123], a[152:155], v[60:63]\n"
  "ds_read_b128 a[24:27], v210 offset:35840\n"
  "v_mfma_f32_16x16x32_bf16 v[60:63], a[124:127], a[156:159], v[60:63]\n"
  "ds_read_b128 a[28:31], v210 offset:44032\n"
  "v_mfma_f32_16x16x32_bf16 v[64:67], a[96:99], a[160:163], v[64:67]\n"
  "ds_read_b128 a[32:35], v208 offset:0\n"
  "v_mfma_f32_16x16x32_bf16 v[64:67], a[100:103], a[164:167], v[64:67]\n"
  "buffer_load_dwordx4 v201, s[4:7], s26 offen offset:1024 lds\n"
  "v_mfma_f32_16x16x32_bf16 v[68:71], a[104:107], a[160:163], v[68:71]\n"
  "ds_read_b128 a[36:39], v209 offset:0\n"
  "v_mfma_f32_16x16x32_bf16 v[68:71], a[108:111], a[164:167], v[68:71]\n"
  "ds_read_b128 a[40:43], v208 offset:2048\n"
  "v_mfma_f32_16x16x32_bf16 v[72:75], a[112:115], a[160:163], v[72:75]\n"
  "ds_read_b128 a[44:47], v209 offset:2048\n"
  "v_mfma_f32_16x16x32_bf16 v[72:75], a[116:119], a[164:167], v[72:75]\n"
  "buffer_load_dwordx4 v202, s[4:7], s26 offen offset:2048 lds\n"
  "v_mfma_f32_16x16x32_bf16 v[76:79], a[120:123], a[160:163], v[76:79]\n"
  "ds_read_b128 a[48:51], v208 offset:4096\n"
  "v_mfma_f32_16x16x32_bf16 v[76:79], a[124:127], a[164:167], v[76:79]\n"
  "ds_read_b128 a[52:55], v209 offset:4096\n"
  "v_mfma_f32_16x16x32_bf16 v[80:83], a[96:99], a[168:171], v[80:83]\n"
  "ds_read_b128 a[56:59], v208 offset:6144\n"
  "v_mfma_f32_16x16x32_bf16 v[80:83], a[100:103], a[172:175], v[80:83]\n"
  "buffer_load_dwordx4 v203, s[4:7], s26 offen offset:3072 lds\n"
  "s_add_u32 m0, s25, 102400\n"
  "v_mfma_f32_16x16x32_bf16 v[84:87], a[104:107], a[168:171], v[84:87]\n"
  "ds_read_b128 a[60:63], v209 offset:6144\n"
  "s_xor_b32 s26, s51, 131072\n"
  "DP4_%=:\n"
  "s_cbranch_execz DPE_%=\n"
  "v_mfma_f32_16x16x32_bf16 v[84:87], a[108:111], a[172:175], v[84:87]\n"
  "v_mfma_f32_16x16x32_bf16 v[88:91], a[112:115], a[168:171], v[88:91]\n"
  "ds_read_b128 a[64:67], v208 offset:8192\n"
  "v_mfma_f32_16x16x32_bf16 v[88:91], a[116:119], a[172:175], v[88:91]\n"
  "buffer_load_dwordx4 v200, s[4:7], s26 offen lds\n"
  "v_mfma_f32_16x16x32_bf16 v[92:95], a[120:123], a[168:171], v[92:95]\n"
  "ds_read_b128 a[68:71], v209 offset:8192\n"
  "v_mfma_f32_16x16x32_bf16 v[92:95], a[124:127], a[172:175], v[92:95]\n"
  "v_mfma_f32_16x16x32_bf16 v[96:99], a[96:99], a[176:179], v[96:99]\n"
  "ds_read_b128 a[72:75], v208 offset:10240\n"
  "v_mfma_f32_16x16x32_bf16 v[96:99], a[100:103], a[180:183], v[96:99]\n"
  "buffer_load_dwordx4 v201, s[4:7], s26 offen offset:1024 lds\n"
  "v_mfma_f32_16x16x32_bf16 v[100:103], a[104:107], a[176:179], v[100:103]\n"
  "ds_read_b128 a[76:79], v209 offset:10240\n"
  "v_mfma_f32_16x16x32_bf16 v[100:103], a[108:111], a[180:183], v[100:103]\n"
  "v_mfma_f32_16x16x32_bf16 v[104:107], a[112:115], a[176:179], v[104:107]\n"
  "ds_read_b128 a[80:83], v208 offset:12288\n"
  "v_mfma_f32_16x16x32_bf16 v[104:107], a[116:119], a[180:183], v[104:107]\n"
  "buffer_load_dwordx4 v202, s[4:7], s26 offen offset:2048 lds\n"
  "v_mfma_f32_16x16x32_bf16 v[108:111], a[120:123], a[176:179], v[108:111]\n"
  "ds_read_b128 a[84:87], v209 offset:12288\n"
  "v_mfma_f32_16x16x32_bf16 v[108:111], a[124:127], a[180:183], v[108:111]\n"
  "v_mfma_f32_16x16x32_bf16 v[112:115], a[96:99], a[184:187], v[112:115]\n"
  "ds_read_b128 a[88:91], v208 offset:14336\n"
  "ds_read_b128 a[92:95], v209 offset:14336\n"
  "v_mfma_f32_16x16x32_bf16 v[112:115], a[100:103], a[188:191], v[112:115]\n"
  "buffer_load_dwordx4 v203, s[4:7], s26 offen offset:3072 lds\n"
  "s_add_u32 m0, s24, 131072\n"
  "v_mfma_f32_16x16x32_bf16 v[116:119], a[104:107], a[184:187], v[116:119]\n"
  "s_xor_b32 s26, s59, 0\n"
  "v_mfma_f32_16x16x32_bf16 v[116:119], a[108:111], a[188:191], v[116:119]\n"
  "v_mfma_f32_16x16x32_bf16 v[120:123], a[112:115], a[184:187], v[120:123]\n"
  "v_mfma_f32_16x16x32_bf16 v[120:123], a[116:119], a[188:191], v[120:123]\n"
  "buffer_load_dwordx4 v211, s[20:23], s26 offen sc1 lds\n"
  "v_mfma_f32_16x16x32_bf16 v[124:127], a[120:123], a[184:187], v[124:127]\n"
  "v_mfma_f32_16x16x32_bf16 v[124:127], a[124:127], a[188:191], v[124:127]\n"
  "s_waitcnt lgkmcnt(0)\n"
  "s_barrier\n"
  "v_mfma_f32_16x16x32_bf16 v[0:3], a[0:3], a[32:35], v[0:3]\n"
  "v_mfma_f32_16x16x32_bf16 v[0:3], a[4:7], a[36:39], v[0:3]\n"
  "buffer_load_dwordx4 v211, s[20:23], s26 offen offset:1024 sc1 lds\n"
  "v_mfma_f32_16x16x32_bf16 v[4:7], a[8:11], a[32:35], v[4:7]\n"
  "v_mfma_f32_16x16x32_bf16 v[4:7], a[12:15], a[36:39], v[4:7]\n"
  "v_mfma_f32_16x16x32_bf16 v[8:11], a[16:19], a[32:35], v[8:11]\n"
  "v_mfma_f32_16x16x32_bf16 v[8:11], a[20:23], a[36:39], v[8:11]\n"
  "buffer_load_dwordx4 v211, s[20:23], s26 offen offset:2048 sc1 lds\n"
  "v_mfma_f32_16x16x32_bf16 v[12:15], a[24:27], a[32:35], v[12:15]\n"
  "v_mfma_f32_16x16x32_bf16 v[12:15], a[28:31], a[36:39], v[12:15]\n"
  "v_mfma_f32_16x16x32_bf16 v[16:19], a[0:3], a[40:43], v[16:19]\n"
  "v_mfma_f32_16x16x32_bf16 v[16:19], a[4:7], a[44:47], v[16:19]\n"
  "buffer_load_dwordx4 v211, s[20:23], s26 offen offset:3072 sc1 lds\n"
  "s_add_u32 m0, s25, 0\n"
  "v_mfma_f32_16x16x32_bf16 v[20:23], a[8:11], a[40:43], v[20:23]\n"
  "s_xor_b32 s26, s51, 128\n"
  "v_mfma_f32_16x16x32_bf16 v[20:23], a[12:15], a[44:47], v[20:23]\n"
  "v_mfma_f32_16x16x32_bf16 v[24:27], a[16:19], a[40:43], v[24:27]\n"
  "v_mfma_f32_16x16x32_bf16 v[24:27], a[20:23], a[44:47], v[24:27]\n"
  "buffer_load_dwordx4 v200, s[4:7], s26 offen lds\n"
  "v_mfma_f32_16x16x32_bf16 v[28:31], a[24:27], a[40:43], v[28:31]\n"
  "v_mfma_f32_16x16x32_bf16 v[28:31], a[28:31], a[44:47], v[28:31]\n"
  "v_mfma_f32_16x16x32_bf16 v[32:35], a[0:3], a[48:51], v[32:35]\n"
  "v_mfma_f32_16x16x32_bf16 v[32:35], a[4:7], a[52:55], v[32:35]\n"
  "buffer_load_dwordx4 v201, s[4:7], s26 offen offset:1024 lds\n"
  "v_mfma_f32_16x16x32_bf16 v[36:39], a[8:11], a[48:51], v[36:39]\n"
  "v_mfma_f32_16x16x32_bf16 v[36:39], a[12:15], a[52:55], v[36:39]\n"
  "v_mfma_f32_16x16x32_bf16 v[40:43], a[16:19], a[48:51], v[40:43]\n"
  "v_mfma_f32_16x16x32_bf16 v[40:43], a[20:23], a[52:55], v[40:43]\n"
  "s_waitcnt vmcnt(14)\n"
  "s_barrier\n"
  "v_mfma_f32_16x16x32_bf16 v[44:47], a[24:27], a[48:51], v[44:47]\n"
  "ds_read_b128 a[96:99], v230 offset:32768\n"
  "v_mfma_f32_16x16x32_bf16 v[44:47], a[28:31], a[52:55], v[44:47]\n"
  "ds_read_b128 a[100:103], v230 offset:40960\n"
  "v_mfma_f32_16x16x32_bf16 v[48:51], a[0:3], a[56:59], v[48:51]\n"
  "ds_read_b128 a[104:107], v230 offset:33792\n"
  "v_mfma_f32_16x16x32_bf16 v[48:51], a[4:7], a[60:63], v[48:51]\n"
  "buffer_load_dwordx4 v202, s[4:7], s26 offen offset:2048 lds\n"
  "v_mfma_f32_16x16x32_bf16 v[52:55], a[8:11], a[56:59], v[52:55]\n"
  "ds_read_b128 a[108:111], v230 offset:41984\n"
  "v_mfma_f32_16x16x32_bf16 v[52:55], a[12:15], a[60:63], v[52:55]\n"
  "ds_read_b128 a[112:115], v230 offset:34816\n"
  "v_mfma_f32_16x16x32_bf16 v[56:59], a[16:19], a[56:59], v[56:59]\n"
  "ds_read_b128 a[116:119], v230 offset:43008\n"
  "v_mfma_f32_16x16x32_bf16 v[56:59], a[20:23], a[60:63], v[56:59]\n"
  "buffer_load_dwordx4 v203, s[4:7], s26 offen offset:3072 lds\n"
  "s_add_u32 m0, s25, 4096\n"
  "v_mfma_f32_16x16x32_bf16 v[60:63], a[24:27], a[56:59], v[60:63]\n"
  "ds_read_b128 a[120:123], v230 offset:35840\n"
  "s_xor_b32 s26, s51, 131200\n"
  "v_mfma_f32_16x16x32_bf16 v[60:63], a[28:31], a[60:63], v[60:63]\n"
  "ds_read_b128 a[124:127], v230 offset:44032\n"
  "v_mfma_f32_16x16x32_bf16 v[64:67], a[0:3], a[64:67], v[64:67]\n"
  "ds_read_b128 a[128:131], v228 offset:0\n"
  "v_mfma_f32_16x16x32_bf16 v[64:67], a[4:7], a[68:71], v[64:67]\n"
  "buffer_load_dwordx4 v200, s[4:7], s26 offen lds\n"
  "v_mfma_f32_16x16x32_bf16 v[68:71], a[8:11], a[64:67], v[68:71]\n"
  "ds_read_b128 a[132:135], v229 offset:0\n"
  "v_mfma_f32_16x16x32_bf16 v[68:71], a[12:15], a[68:71], v[68:71]\n"
  "ds_read_b128 a[136:139], v228 offset:2048\n"
  "v_mfma_f32_16x16x32_bf16 v[72:75], a[16:19], a[64:67], v[72:75]\n"
  "ds_read_b128 a[140:143], v229 offset:2048\n"
  "v_mfma_f32_16x16x32_bf16 v[72:75], a[20:23], a[68:71], v[72:75]\n"
  "buffer_load_dwordx4 v201, s[4:7], s26 offen offset:1024 lds\n"
  "v_mfma_f32_16x16x32_bf16 v[76:79], a[24:27], a[64:67], v[76:79]\n"
  "ds_read_b128 a[144:147], v228 offset:4096\n"
  "v_mfma_f32_16x16x32_bf16 v[76:79], a[28:31], a[68:71], v[76:79]\n"
  "ds_read_b128 a[148:151], v229 offset:4096\n"
  "v_mfma_f32_16x16x32_bf16 v[80:83], a[0:3], a[72:75], v[80:83]\n"
  "ds_read_b128 a[152:155], v228 offset:6144\n"
  "v_mfma_f32_16x16x32_bf16 v[80:83], a[4:7], a[76:79], v[80:83]\n"
  "buffer_load_dwordx4 v202, s[4:7], s26 offen offset:2048 lds\n"
  "v_mfma_f32_16x16x32_bf16 v[84:87], a[8:11], a[72:75], v[84:87]\n"
  "ds_read_b128 a[156:159], v229 offset:6144\n"
  "v_mfma_f32_16x16x32_bf16 v[84:87], a[12:15], a[76:79], v[84:87]\n"
  "v_mfma_f32_16x16x32_bf16 v[88:91], a[16:19], a[72:75], v[88:91]\n"
  "ds_read_b128 a[160:163], v228 offset:8192\n"
  "v_mfma_f32_16x16x32_bf16 v[88:91], a[20:23], a[76:79], v[88:91]\n"
  "buffer_load_dwordx4 v203, s[4:7], s26 offen offset:3072 lds\n"
  "s_add_u32 m0, s24, 32768\n"
  "v_mfma_f32_16x16x32_bf16 v[92:95], a[24:27], a[72:75], v[92:95]\n"
  "ds_read_b128 a[164:167], v229 offset:8192\n"
  "s_xor_b32 s26, s59, 16384\n"
  "v_mfma_f32_16x16x32_bf16 v[92:95], a[28:31], a[76:79], v[92:95]\n"
  "v_mfma_f32_16x16x32_bf16 v[96:99], a[0:3], a[80:83], v[96:99]\n"
  "ds_read_b128 a[168:171], v228 offset:10240\n"
  "v_mfma_f32_16x16x32_bf16 v[96:99], a[4:7], a[84:87], v[96:99]\n"
  "buffer_load_dwordx4 v211, s[20:23], s26 offen sc1 lds\n"
  "v_mfma_f32_16x16x32_bf16 v[100:103], a[8:11], a[80:83], v[100:103]\n"
  "ds_read_b128 a[172:175], v229 offset:10240\n"
  "v_mfma_f32_16x16x32_bf16 v[100:103], a[12:15], a[84:87], v[100:103]\n"
  "v_mfma_f32_16x16x32_bf16 v[104:107], a[16:19], a[80:83], v[104:107]\n"
  "ds_read_b128 a[176:179], v228 offset:12288\n"
  "v_mfma_f32_16x16x32_bf16 v[104:107], a[20:23], a[84:87], v[104:107]\n"
  "buffer_load_dwordx4 v211, s[20:23], s26 offen offset:1024 sc1 lds\n"
  "v_mfma_f32_16x16x32_bf16 v[108:111], a[24:27], a[80:83], v[108:111]\n"
  "ds_read_b128 a[180:183], v229 offset:12288\n"
  "v_mfma_f32_16x16x32_bf16 v[108:111], a[28:31], a[84:87], v[108:111]\n"
  "v_mfma_f32_16x16x32_bf16 v[112:115], a[0:3], a[88:91], v[112:115]\n"
  "ds_read_b128 a[184:187], v228 offset:14336\n"
  "ds_read_b128 a[188:191], v229 offset:14336\n"
  "v_mfma_f32_16x16x32_bf16 v[112:115], a[4:7], a[92:95], v[112:115]\n"
  "buffer_load_dwordx4 v211, s[20:23], s26 offen offset:2048 sc1 lds\n"
  "v_mfma_f32_16x16x32_bf16 v[116:119], a[8:11], a[88:91], v[116:119]\n"
  "v_mfma_f32_16x16x32_bf16 v[116:119], a[12:15], a[92:95], v[116:119]\n"
  "v_mfma_f32_16x16x32_bf16 v[120:123], a[16:19], a[88:91], v[120:123]\n"
  "v_mfma_f32_16x16x32_bf16 v[120:123], a[20:23], a[92:95], v[120:123]\n"
  "buffer_load_dwordx4 v211, s[20:23], s26 offen offset:3072 sc1 lds\n"
  "v_mfma_f32_16x16x32_bf16 v[124:127], a[24:27], a[88:91], v[124:127]\n"
  "v_mfma_f32_16x16x32_bf16 v[124:127], a[28:31], a[92:95], v[124:127]\n"
  "s_waitcnt vmcnt(12) lgkmcnt(0)\n"
  "s_barrier\n"
  "v_mfma_f32_16x16x32_bf16 v[0:3], a[96:99], a[128:131], v[0:3]\n"
  "ds_read_b128 a[0:3], v237 offset:32768\n"
  "v_mfma_f32_16x16x32_bf16 v[0:3], a[100:103], a[132:135], v[0:3]\n"
  "v_mfma_f32_16x16x32_bf16 v[4:7], a[104:107], a[128:131], v[4:7]\n"
  "ds_read_b128 a[4:7], v237 offset:40960\n"
  "v_mfma_f32_16x16x32_bf16 v[4:7], a[108:111], a[132:135], v[4:7]\n"
  "ds_read_b128 a[8:11], v237 offset:33792\n"
  "v_mfma_f32_16x16x32_bf16 v[8:11], a[112:115], a[128:131], v[8:11]\n"
  "ds_read_b128 a[12:15], v237 offset:41984\n"
  "v_mfma_f32_16x16x32_bf16 v[8:11], a[116:119], a[132:135], v[8:11]\n"
  "v_mfma_f32_16x16x32_bf16 v[12:15], a[120:123], a[128:131], v[12:15]\n"
  "ds_read_b128 a[16:19], v237 offset:34816\n"
  "v_mfma_f32_16x16x32_bf16 v[12:15], a[124:127], a[132:135], v[12:15]\n"
  "ds_read_b128 a[20:23], v237 offset:43008\n"
  "v_mfma_f32_16x16x32_bf16 v[16:19], a[96:99], a[136:139], v[16:19]\n"
  "ds_read_b128 a[24:27], v237 offset:35840\n"
  "v_mfma_f32_16x16x32_bf16 v[16:19], a[100:103], a[140:143], v[16:19]\n"
  "v_mfma_f32_16x16x32_bf16 v[20:23], a[104:107], a[136:139], v[20:23]\n"
  "ds_read_b128 a[28:31], v237 offset:44032\n"
  "v_mfma_f32_16x16x32_bf16 v[20:23], a[108:111], a[140:143], v[20:23]\n"
  "ds_read_b128 a[32:35], v235 offset:0\n"
  "v_mfma_f32_16x16x32_bf16 v[24:27], a[112:115], a[136:139], v[24:27]\n"
  "ds_read_b128 a[36:39], v236 offset:0\n"
  "v_mfma_f32_16x16x32_bf16 v[24:27], a[116:119], a[140:143], v[24:27]\n"
  "v_mfma_f32_16x16x32_bf16 v[28:31], a[120:123], a[136:139], v[28:31]\n"
  "ds_read_b128 a[40:43], v235 offset:2048\n"
  "v_mfma_f32_16x16x32_bf16 v[28:31], a[124:127], a[140:143], v[28:31]\n"
  "ds_read_b128 a[44:47], v236 offset:2048\n"
  "v_mfma_f32_16x16x32_bf16 v[32:35], a[96:99], a[144:147], v[32:35]\n"
  "ds_read_b128 a[48:51], v235 offset:4096\n"
  "v_mfma_f32_16x16x32_bf16 v[32:35], a[100:103], a[148:151], v[32:35]\n"
  "v_mfma_f32_16x16x32_bf16 v[36:39], a[104:107], a[144:147], v[36:39]\n"
  "ds_read_b128 a[52:55], v236 offset:4096\n"
  "v_mfma_f32_16x16x32_bf16 v[36:39], a[108:111], a[148:151], v[36:39]\n"
  "ds_read_b128 a[56:59], v235 offset:6144\n"
  "v_mfma_f32_16x16x32_bf16 v[40:43], a[112:115], a[144:147], v[40:43]\n"
  "ds_read_b128 a[60:63], v236 offset:6144\n"
  "v_mfma_f32_16x16x32_bf16 v[40:43], a[116:119], a[148:151], v[40:43]\n"
  "v_mfma_f32_16x16x32_bf16 v[44:47], a[120:123], a[144:147], v[44:47]\n"
  "ds_read_b128 a[64:67], v235 offset:8192\n"
  "v_mfma_f32_16x16x32_bf16 v[44:47], a[124:127], a[148:151], v[44:47]\n"
  "ds_read_b128 a[68:71], v236 offset:8192\n"
  "s_waitcnt lgkmcnt(8)\n"
  "v_mfma_f32_16x16x32_bf16 v[0:3], a[0:3], a[32:35], v[0:3]\n"
  "ds_read_b128 a[72:75], v235 offset:10240\n"
  "v_mfma_f32_16x16x32_bf16 v[0:3], a[4:7], a[36:39], v[0:3]\n"
  "v_mfma_f32_16x16x32_bf16 v[4:7], a[8:11], a[32:35], v[4:7]\n"
  "ds_read_b128 a[76:79], v236 offset:10240\n"
  "v_mfma_f32_16x16x32_bf16 v[4:7], a[12:15], a[36:39], v[4:7]\n"
  "v_mfma_f32_16x16x32_bf16 v[8:11], a[16:19], a[32:35], v[8:11]\n"
  "ds_read_b128 a[80:83], v235 offset:12288\n"
  "v_mfma_f32_16x16x32_bf16 v[8:11], a[20:23], a[36:39], v[8:11]\n"
  "v_mfma_f32_16x16x32_bf16 v[12:15], a[24:27], a[32:35], v[12:15]\n"
  "ds_read_b128 a[84:87], v236 offset:12288\n"
  "v_mfma_f32_16x16x32_bf16 v[12:15], a[28:31], a[36:39], v[12:15]\n"
  "v_mfma_f32_16x16x32_bf16 v[48:51], a[96:99], a[152:155], v[48:51]\n"
  "ds_read_b128 a[88:91], v235 offset:14336\n"
  "ds_read_b128 a[92:95], v236 offset:14336\n"
  "v_mfma_f32_16x16x32_bf16 v[48:51], a[100:103], a[156:159], v[48:51]\n"
  "v_mfma_f32_16x16x32_bf16 v[52:55], a[104:107], a[152:155], v[52:55]\n"
  "v_mfma_f32_16x16x32_bf16 v[52:55], a[108:111], a[156:159], v[52:55]\n"
  "v_mfma_f32_16x16x32_bf16 v[56:59], a[112:115], a[152:155], v[56:59]\n"
  "v_mfma_f32_16x16x32_bf16 v[56:59], a[116:119], a[156:159], v[56:59]\n"
  "s_waitcnt vmcnt(0)\n"
  "s_barrier\n"
  "v_mfma_f32_16x16x32_bf16 v[60:63], a[120:123], a[152:155], v[60:63]\n"
  "ds_read_b128 a[192:195], v210 offset:32768\n"
  "v_mfma_f32_16x16x32_bf16 v[60:63], a[124:127], a[156:159], v[60:63]\n"
  "s_waitcnt lgkmcnt(1)\n"
  "v_mfma_f32_16x16x32_bf16 v[16:19], a[0:3], a[40:43], v[16:19]\n"
  "ds_read_b128 a[196:199], v210 offset:40960\n"
  "v_mfma_f32_16x16x32_bf16 v[16:19], a[4:7], a[44:47], v[16:19]\n"
  "v_mfma_f32_16x16x32_bf16 v[20:23], a[8:11], a[40:43], v[20:23]\n"
  "ds_read_b128 a[200:203], v210 offset:33792\n"
  "v_mfma_f32_16x16x32_bf16 v[20:23], a[12:15], a[44:47], v[20:23]\n"
  "v_mfma_f32_16x16x32_bf16 v[24:27], a[16:19], a[40:43], v[24:27]\n"
  "ds_read_b128 a[204:207], v210 offset:41984\n"
  "v_mfma_f32_16x16x32_bf16 v[24:27], a[20:23], a[44:47], v[24:27]\n"
  "v_mfma_f32_16x16x32_bf16 v[28:31], a[24:27], a[40:43], v[28:31]\n"
  "ds_read_b128 a[208:211], v210 offset:34816\n"
  "v_mfma_f32_16x16x32_bf16 v[28:31], a[28:31], a[44:47], v[28:31]\n"
  "v_mfma_f32_16x16x32_bf16 v[64:67], a[96:99], a[160:163], v[64:67]\n"
  "ds_read_b128 a[212:215], v210 offset:43008\n"
  "v_mfma_f32_16x16x32_bf16 v[64:67], a[100:103], a[164:167], v[64:67]\n"
  "v_mfma_f32_16x16x32_bf16 v[68:71], a[104:107], a[160:163], v[68:71]\n"
  "ds_read_b128 a[216:219], v210 offset:35840\n"
  "v_mfma_f32_16x16x32_bf16 v[68:71], a[108:111], a[164:167], v[68:71]\n"
  "v_mfma_f32_16x16x32_bf16 v[72:75], a[112:115], a[160:163], v[72:75]\n"
  "ds_read_b128 a[220:223], v210 offset:44032\n"
  "v_mfma_f32_16x16x32_bf16 v[72:75], a[116:119], a[164:167], v[72:75]\n"
  "v_mfma_f32_16x16x32_bf16 v[76:79], a[120:123], a[160:163], v[76:79]\n"
  "ds_read_b128 a[128:131], v208 offset:0\n"
  "v_mfma_f32_16x16x32_bf16 v[76:79], a[124:127], a[164:167], v[76:79]\n"
  "v_mfma_f32_16x16x32_bf16 v[32:35], a[0:3], a[48:51], v[32:35]\n"
  "ds_read_b128 a[132:135], v209 offset:0\n"
  "v_mfma_f32_16x16x32_bf16 v[32:35], a[4:7], a[52:55], v[32:35]\n"
  "v_mfma_f32_16x16x32_bf16 v[36:39], a[8:11], a[48:51], v[36:39]\n"
  "ds_read_b128 a[136:139], v208 offset:2048\n"
  "v_mfma_f32_16x16x32_bf16 v[36:39], a[12:15], a[52:55], v[36:39]\n"
  "v_mfma_f32_16x16x32_bf16 v[40:43], a[16:19], a[48:51], v[40:43]\n"
  "ds_read_b128 a[140:143], v209 offset:2048\n"
  "v_mfma_f32_16x16x32_bf16 v[40:43], a[20:23], a[52:55], v[40:43]\n"
  "v_mfma_f32_16x16x32_bf16 v[44:47], a[24:27], a[48:51], v[44:47]\n"
  "ds_read_b128 a[144:147], v208 offset:4096\n"
  "v_mfma_f32_16x16x32_bf16 v[44:47], a[28:31], a[52:55], v[44:47]\n"
  "s_waitcnt lgkmcnt(3)\n"
  "v_mfma_f32_16x16x32_bf16 v[0:3], a[192:195], a[128:131], v[0:3]\n"
  "ds_read_b128 a[148:151], v209 offset:4096\n"
  "v_mfma_f32_16x16x32_bf16 v[0:3], a[196:199], a[132:135], v[0:3]\n"
  "v_mfma_f32_16x16x32_bf16 v[4:7], a[200:203], a[128:131], v[4:7]\n"
  "ds_read_b128 a[152:155], v208 offset:6144\n"
  "v_mfma_f32_16x16x32_bf16 v[4:7], a[204:207], a[132:135], v[4:7]\n"
  "v_mfma_f32_16x16x32_bf16 v[8:11], a[208:211], a[128:131], v[8:11]\n"
  "ds_read_b128 a[156:159], v209 offset:6144\n"
  "v_mfma_f32_16x16x32_bf16 v[8:11], a[212:215], a[132:135], v[8:11]\n"
  "v_mfma_f32_16x16x32_bf16 v[12:15], a[216:219], a[128:131], v[12:15]\n"
  "ds_read_b128 a[160:163], v208 offset:8192\n"
  "v_mfma_f32_16x16x32_bf16 v[12:15], a[220:223], a[132:135], v[12:15]\n"
  "v_mfma_f32_16x16x32_bf16 v[80:83], a[96:99], a[168:171], v[80:83]\n"
  "ds_read_b128 a[164:167], v209 offset:8192\n"
  "v_mfma_f32_16x16x32_bf16 v[80:83], a[100:103], a[172:175], v[80:83]\n"
  "v_mfma_f32_16x16x32_bf16 v[84:87], a[104:107], a[168:171], v[84:87]\n"
  "v_mfma_f32_16x16x32_bf16 v[84:87], a[108:111], a[172:175], v[84:87]\n"
  "v_cvt_pk_bf16_f32 v0, v0, v1\n"
  "v_cvt_pk_bf16_f32 v1, v2, v3\n"
  "v_mfma_f32_16x16x32_bf16 v[88:91], a[112:115], a[168:171], v[88:91]\n"
  "v_cvt_pk_bf16_f32 v2, v4, v5\n"
  "v_cvt_pk_bf16_f32 v3, v6, v7\n"
  "v_mfma_f32_16x16x32_bf16 v[88:91], a[116:119], a[172:175], v[88:91]\n"
  "s_mov_b32 s44, 0\n"
  "v_mfma_f32_16x16x32_bf16 v[92:95], a[120:123], a[168:171], v[92:95]\n"
  "buffer_store_dwordx4 v[0:3], v213, s[20:23], s44 offen offset:0 sc1\n"
  "v_mfma_f32_16x16x32_bf16 v[92:95], a[124:127], a[172:175], v[92:95]\n"
  "v_mfma_f32_16x16x32_bf16 v[48:51], a[0:3], a[56:59], v[48:51]\n"
  "v_mfma_f32_16x16x32_bf16 v[48:51], a[4:7], a[60:63], v[48:51]\n"
  "v_cvt_pk_bf16_f32 v8, v8, v9\n"
  "v_cvt_pk_bf16_f32 v9, v10, v11\n"
  "v_mfma_f32_16x16x32_bf16 v[52:55], a[8:11], a[56:59], v[52:55]\n"
  "ds_read_b128 a[168:171], v208 offset:10240\n"
  "v_cvt_pk_bf16_f32 v10, v12, v13\n"
  "v_mfma_f32_16x16x32_bf16 v[52:55], a[12:15], a[60:63], v[52:55]\n"
  "v_cvt_pk_bf16_f32 v11, v14, v15\n"
  "v_mfma_f32_16x16x32_bf16 v[56:59], a[16:19], a[56:59], v[56:59]\n"
  "ds_read_b128 a[172:175], v209 offset:10240\n"
  "v_mfma_f32_16x16x32_bf16 v[56:59], a[20:23], a[60:63], v[56:59]\n"
  "v_mfma_f32_16x16x32_bf16 v[60:63], a[24:27], a[56:59], v[60:63]\n"
  "buffer_store_dwordx4 v[8:11], v213, s[20:23], s44 offen offset:1024 sc1\n"
  "v_mfma_f32_16x16x32_bf16 v[60:63], a[28:31], a[60:63], v[60:63]\n"
  "s_waitcnt lgkmcnt(8)\n"
  "v_mfma_f32_16x16x32_bf16 v[16:19], a[192:195], a[136:139], v[16:19]\n"
  "v_mfma_f32_16x16x32_bf16 v[16:19], a[196:199], a[140:143], v[16:19]\n"
  "v_mfma_f32_16x16x32_bf16 v[20:23], a[200:203], a[136:139], v[20:23]\n"
  "v_mfma_f32_16x16x32_bf16 v[20:23], a[204:207], a[140:143], v[20:23]\n"
  "v_mfma_f32_16x16x32_bf16 v[24:27], a[208:211], a[136:139], v[24:27]\n"
  "v_mfma_f32_16x16x32_bf16 v[24:27], a[212:215], a[140:143], v[24:27]\n"
  "v_mfma_f32_16x16x32_bf16 v[28:31], a[216:219], a[136:139], v[28:31]\n"
  "v_mfma_f32_16x16x32_bf16 v[28:31], a[220:223], a[140:143], v[28:31]\n"
  "v_mfma_f32_16x16x32_bf16 v[96:99], a[96:99], a[176:179], v[96:99]\n"
  "v_mfma_f32_16x16x32_bf16 v[96:99], a[100:103], a[180:183], v[96:99]\n"
  "v_mfma_f32_16x16x32_bf16 v[100:103], a[104:107], a[176:179], v[100:103]\n"
  "v_mfma_f32_16x16x32_bf16 v[100:103], a[108:111], a[180:183], v[100:103]\n"
  "v_cvt_pk_bf16_f32 v16, v16, v17\n"
  "v_cvt_pk_bf16_f32 v17, v18, v19\n"
  "v_mfma_f32_16x16x32_bf16 v[104:107], a[112:115], a[176:179], v[104:107]\n"
  "v_cvt_pk_bf16_f32 v18, v20, v21\n"
  "v_cvt_pk_bf16_f32 v19, v22, v23\n"
  "v_mfma_f32_16x16x32_bf16 v[104:107], a[116:119], a[180:183], v[104:107]\n"
  "buffer_store_dwordx4 v[16:19], v213, s[20:23], s44 offen offset:2048 sc1\n"
  "v_mfma_f32_16x16x32_bf16 v[108:111], a[120:123], a[176:179], v[108:111]\n"
  "v_mfma_f32_16x16x32_bf16 v[108:111], a[124:127], a[180:183], v[108:111]\n"
  "v_mfma_f32_16x16x32_bf16 v[64:67], a[0:3], a[64:67], v[64:67]\n"
  "v_mfma_f32_16x16x32_bf16 v[64:67], a[4:7], a[68:71], v[64:67]\n"
  "v_cvt_pk_bf16_f32 v24, v24, v25\n"
  "v_cvt_pk_bf16_f32 v25, v26, v27\n"
  "v_mfma_f32_16x16x32_bf16 v[68:71], a[8:11], a[64:67], v[68:71]\n"
  "ds_read_b128 a[176:179], v208 offset:12288\n"
  "v_cvt_pk_bf16_f32 v26, v28, v29\n"
  "v_mfma_f32_16x16x32_bf16 v[68:71], a[12:15], a[68:71], v[68:71]\n"
  "v_cvt_pk_bf16_f32 v27, v30, v31\n"
  "v_mfma_f32_16x16x32_bf16 v[72:75], a[16:19], a[64:67], v[72:75]\n"
  "ds_read_b128 a[180:183], v209 offset:12288\n"
  "v_mfma_f32_16x16x32_bf16 v[72:75], a[20:23], a[68:71], v[72:75]\n"
  "buffer_store_dwordx4 v[24:27], v213, s[20:23], s44 offen offset:3072 sc1\n"
  "v_mfma_f32_16x16x32_bf16 v[76:79], a[24:27], a[64:67], v[76:79]\n"
  "v_mfma_f32_16x16x32_bf16 v[76:79], a[28:31], a[68:71], v[76:79]\n"
  "s_waitcnt lgkmcnt(8)\n"
  "v_mfma_f32_16x16x32_bf16 v[32:35], a[192:195], a[144:147], v[32:35]\n"
  "v_mfma_f32_16x16x32_bf16 v[32:35], a[196:199], a[148:151], v[32:35]\n"
  "v_mfma_f32_16x16x32_bf16 v[36:39], a[200:203], a[144:147], v[36:39]\n"
  "v_mfma_f32_16x16x32_bf16 v[36:39], a[204:207], a[148:151], v[36:39]\n"
  "v_mfma_f32_16x16x32_bf16 v[40:43], a[208:211], a[144:147], v[40:43]\n"
  "v_mfma_f32_16x16x32_bf16 v[40:43], a[212:215], a[148:151], v[40:43]\n"
  "v_mfma_f32_16x16x32_bf16 v[44:47], a[216:219], a[144:147], v[44:47]\n"
  "v_mfma_f32_16x16x32_bf16 v[44:47], a[220:223], a[148:151], v[44:47]\n"
  "v_mfma_f32_16x16x32_bf16 v[112:115], a[96:99], a[184:187], v[112:115]\n"
  "v_mfma_f32_16x16x32_bf16 v[112:115], a[100:103], a[188:191], v[112:115]\n"
  "v_mfma_f32_16x16x32_bf16 v[116:119], a[104:107], a[184:187], v[116:119]\n"
  "v_mfma_f32_16x16x32_bf16 v[116:119], a[108:111], a[188:191], v[116:119]\n"
  "v_cvt_pk_bf16_f32 v32, v32, v33\n"
  "v_cvt_pk_bf16_f32 v33, v34, v35\n"
  "v_mfma_f32_16x16x32_bf16 v[120:123], a[112:115], a[184:187], v[120:123]\n"
  "v_cvt_pk_bf16_f32 v34, v36, v37\n"
  "v_cvt_pk_bf16_f32 v35, v38, v39\n"
  "v_mfma_f32_16x16x32_bf16 v[120:123], a[116:119], a[188:191], v[120:123]\n"
  "s_mov_b32 s45, 16384\n"
  "v_mfma_f32_16x16x32_bf16 v[124:127], a[120:123], a[184:187], v[124:127]\n"
  "buffer_store_dwordx4 v[32:35], v213, s[20:23], s45 offen offset:0 sc1\n"
  "v_mfma_f32_16x16x32_bf16 v[124:127], a[124:127], a[188:191], v[124:127]\n"
  "v_mfma_f32_16x16x32_bf16 v[80:83], a[0:3], a[72:75], v[80:83]\n"
  "v_mfma_f32_16x16x32_bf16 v[80:83], a[4:7], a[76:79], v[80:83]\n"
  "v_cvt_pk_bf16_f32 v40, v40, v41\n"
  "v_cvt_pk_bf16_f32 v41, v42, v43\n"
  "v_mfma_f32_16x16x32_bf16 v[84:87], a[8:11], a[72:75], v[84:87]\n"
  "ds_read_b128 a[184:187], v208 offset:14336\n"
  "v_cvt_pk_bf16_f32 v42, v44, v45\n"
  "v_mfma_f32_16x16x32_bf16 v[84:87], a[12:15], a[76:79], v[84:87]\n"
  "v_cvt_pk_bf16_f32 v43, v46, v47\n"
  "v_mfma_f32_16x16x32_bf16 v[88:91], a[16:19], a[72:75], v[88:91]\n"
  "ds_read_b128 a[188:191], v209 offset:14336\n"
  "v_mfma_f32_16x16x32_bf16 v[88:91], a[20:23], a[76:79], v[88:91]\n"
  "v_mfma_f32_16x16x32_bf16 v[92:95], a[24:27], a[72:75], v[92:95]\n"
  "buffer_store_dwordx4 v[40:43], v213, s[20:23], s45 offen offset:1024 sc1\n"
  "v_mfma_f32_16x16x32_bf16 v[92:95], a[28:31], a[76:79], v[92:95]\n"
  "s_waitcnt lgkmcnt(8)\n"
  "v_mfma_f32_16x16x32_bf16 v[48:51], a[192:195], a[152:155], v[48:51]\n"
  "v_mfma_f32_16x16x32_bf16 v[48:51], a[196:199], a[156:159], v[48:51]\n"
  "v_mfma_f32_16x16x32_bf16 v[52:55], a[200:203], a[152:155], v[52:55]\n"
  "v_mfma_f32_16x16x32_bf16 v[52:55], a[204:207], a[156:159], v[52:55]\n"
  "v_mfma_f32_16x16x32_bf16 v[56:59], a[208:211], a[152:155], v[56:59]\n"
  "v_mfma_f32_16x16x32_bf16 v[56:59], a[212:215], a[156:159], v[56:59]\n"
  "v_mfma_f32_16x16x32_bf16 v[60:63], a[216:219], a[152:155], v[60:63]\n"
  "v_mfma_f32_16x16x32_bf16 v[60:63], a[220:223], a[156:159], v[60:63]\n"
  "v_mfma_f32_16x16x32_bf16 v[96:99], a[0:3], a[80:83], v[96:99]\n"
  "v_mfma_f32_16x16x32_bf16 v[96:99], a[4:7], a[84:87], v[96:99]\n"
  "v_mfma_f32_16x16x32_bf16 v[100:103], a[8:11], a[80:83], v[100:103]\n"
  "v_mfma_f32_16x16x32_bf16 v[100:103], a[12:15], a[84:87], v[100:103]\n"
  "v_cvt_pk_bf16_f32 v48, v48, v49\n"
  "v_cvt_pk_bf16_f32 v49, v50, v51\n"
  "v_mfma_f32_16x16x32_bf16 v[104:107], a[16:19], a[80:83], v[104:107]\n"
  "v_cvt_pk_bf16_f32 v50, v52, v53\n"
  "v_cvt_pk_bf16_f32 v51, v54, v55\n"
  "v_mfma_f32_16x16x32_bf16 v[104:107], a[20:23], a[84:87], v[104:107]\n"
  "buffer_store_dwordx4 v[48:51], v213, s[20:23], s45 offen offset:2048 sc1\n"
  "v_mfma_f32_16x16x32_bf16 v[108:111], a[24:27], a[80:83], v[108:111]\n"
  "v_mfma_f32_16x16x32_bf16 v[108:111], a[28:31], a[84:87], v[108:111]\n"
  "s_waitcnt vmcnt(3)\n"
  "buffer_store_dwordx2 v[216:217], v215, s[20:23], s30 offen offset:0 sc1\n"
  "s_waitcnt lgkmcnt(6)\n"
  "v_mfma_f32_16x16x32_bf16 v[64:67], a[192:195], a[160:163], v[64:67]\n"
  "v_mfma_f32_16x16x32_bf16 v[64:67], a[196:199], a[164:167], v[64:67]\n"
  "v_cvt_pk_bf16_f32 v56, v56, v57\n"
  "v_cvt_pk_bf16_f32 v57, v58, v59\n"
  "v_mfma_f32_16x16x32_bf16 v[68:71], a[200:203], a[160:163], v[68:71]\n"
  "v_cvt_pk_bf16_f32 v58, v60, v61\n"
  "v_cvt_pk_bf16_f32 v59, v62, v63\n"
  "v_mfma_f32_16x16x32_bf16 v[68:71], a[204:207], a[164:167], v[68:71]\n"
  "v_mfma_f32_16x16x32_bf16 v[72:75], a[208:211], a[160:163], v[72:75]\n"
  "v_mfma_f32_16x16x32_bf16 v[72:75], a[212:215], a[164:167], v[72:75]\n"
  "buffer_store_dwordx4 v[56:59], v213, s[20:23], s45 offen offset:3072 sc1\n"
  "v_mfma_f32_16x16x32_bf16 v[76:79], a[216:219], a[160:163], v[76:79]\n"
  "v_mfma_f32_16x16x32_bf16 v[76:79], a[220:223], a[164:167], v[76:79]\n"
  "v_mfma_f32_16x16x32_bf16 v[112:115], a[0:3], a[88:91], v[112:115]\n"
  "v_mfma_f32_16x16x32_bf16 v[112:115], a[4:7], a[92:95], v[112:115]\n"
  "v_mfma_f32_16x16x32_bf16 v[116:119], a[8:11], a[88:91], v[116:119]\n"
  "v_mfma_f32_16x16x32_bf16 v[116:119], a[12:15], a[92:95], v[116:119]\n"
  "v_cvt_pk_bf16_f32 v64, v64, v65\n"
  "v_cvt_pk_bf16_f32 v65, v66, v67\n"
  "v_mfma_f32_16x16x32_bf16 v[120:123], a[16:19], a[88:91], v[120:123]\n"
  "v_cvt_pk_bf16_f32 v66, v68, v69\n"
  "v_cvt_pk_bf16_f32 v67, v70, v71\n"
  "v_mfma_f32_16x16x32_bf16 v[120:123], a[20:23], a[92:95], v[120:123]\n"
  "s_mov_b32 s46, 32768\n"
  "v_mfma_f32_16x16x32_bf16 v[124:127], a[24:27], a[88:91], v[124:127]\n"
  "buffer_store_dwordx4 v[64:67], v213, s[20:23], s46 offen offset:0 sc1\n"
  "v_mfma_f32_16x16x32_bf16 v[124:127], a[28:31], a[92:95], v[124:127]\n"
  "s_waitcnt lgkmcnt(4)\n"
  "v_mfma_f32_16x16x32_bf16 v[80:83], a[192:195], a[168:171], v[80:83]\n"
  "v_mfma_f32_16x16x32_bf16 v[80:83], a[196:199], a[172:175], v[80:83]\n"
  "v_cvt_pk_bf16_f32 v72, v72, v73\n"
  "v_cvt_pk_bf16_f32 v73, v74, v75\n"
  "v_mfma_f32_16x16x32_bf16 v[84:87], a[200:203], a[168:171], v[84:87]\n"
  "v_cvt_pk_bf16_f32 v74, v76, v77\n"
  "v_cvt_pk_bf16_f32 v75, v78, v79\n"
  "v_mfma_f32_16x16x32_bf16 v[84:87], a[204:207], a[172:175], v[84:87]\n"
  "v_mfma_f32_16x16x32_bf16 v[88:91], a[208:211], a[168:171], v[88:91]\n"
  "v_mfma_f32_16x16x32_bf16 v[88:91], a[212:215], a[172:175], v[88:91]\n"
  "v_mfma_f32_16x16x32_bf16 v[92:95], a[216:219], a[168:171], v[92:95]\n"
  "buffer_store_dwordx4 v[72:75], v213, s[20:23], s46 offen offset:1024 sc1\n"
  "v_mfma_f32_16x16x32_bf16 v[92:95], a[220:223], a[172:175], v[92:95]\n"
  "s_waitcnt lgkmcnt(2)\n"
  "v_mfma_f32_16x16x32_bf16 v[96:99], a[192:195], a[176:179], v[96:99]\n"
  "v_mfma_f32_16x16x32_bf16 v[96:99], a[196:199], a[180:183], v[96:99]\n"
  "v_mfma_f32_16x16x32_bf16 v[100:103], a[200:203], a[176:179], v[100:103]\n"
  "v_mfma_f32_16x16x32_bf16 v[100:103], a[204:207], a[180:183], v[100:103]\n"
  "v_cvt_pk_bf16_f32 v80, v80, v81\n"
  "v_cvt_pk_bf16_f32 v81, v82, v83\n"
  "v_mfma_f32_16x16x32_bf16 v[104:107], a[208:211], a[176:179], v[104:107]\n"
  "v_cvt_pk_bf16_f32 v82, v84, v85\n"
  "v_cvt_pk_bf16_f32 v83, v86, v87\n"
  "v_mfma_f32_16x16x32_bf16 v[104:107], a[212:215], a[180:183], v[104:107]\n"
  "v_mfma_f32_16x16x32_bf16 v[108:111], a[216:219], a[176:179], v[108:111]\n"
  "buffer_store_dwordx4 v[80:83], v213, s[20:23], s46 offen offset:2048 sc1\n"
  "v_mfma_f32_16x16x32_bf16 v[108:111], a[220:223], a[180:183], v[108:111]\n"
  "s_waitcnt lgkmcnt(0)\n"
  "v_mfma_f32_16x16x32_bf16 v[112:115], a[192:195], a[184:187], v[112:115]\n"
  "v_mfma_f32_16x16x32_bf16 v[112:115], a[196:199], a[188:191], v[112:115]\n"
  "v_cvt_pk_bf16_f32 v88, v88, v89\n"
  "v_cvt_pk_bf16_f32 v89, v90, v91\n"
  "v_mfma_f32_16x16x32_bf16 v[116:119], a[200:203], a[184:187], v[116:119]\n"
  "v_cvt_pk_bf16_f32 v90, v92, v93\n"
  "v_cvt_pk_bf16_f32 v91, v94, v95\n"
  "v_mfma_f32_16x16x32_bf16 v[116:119], a[204:207], a[188:191], v[116:119]\n"
  "v_cvt_pk_bf16_f32 v96, v96, v97\n"
  "v_cvt_pk_bf16_f32 v97, v98, v99\n"
  "v_mfma_f32_16x16x32_bf16 v[120:123], a[208:211], a[184:187], v[120:123]\n"
  "v_cvt_pk_bf16_f32 v98, v100, v101\n"
  "v_cvt_pk_bf16_f32 v99, v102, v103\n"
  "v_mfma_f32_16x16x32_bf16 v[120:123], a[212:215], a[188:191], v[120:123]\n"
  "s_mov_b32 s47, 49152\n"
  "v_mfma_f32_16x16x32_bf16 v[124:127], a[216:219], a[184:187], v[124:127]\n"
  "buffer_store_dwordx4 v[88:91], v213, s[20:23], s46 offen offset:3072 sc1\n"
  "v_mfma_f32_16x16x32_bf16 v[124:127], a[220:223], a[188:191], v[124:127]\n"
  "v_cvt_pk_bf16_f32 v104, v104, v105\n"
  "buffer_store_dwordx4 v[96:99], v213, s[20:23], s47 offen offset:0 sc1\n"
  "v_cvt_pk_bf16_f32 v105, v106, v107\n"
  "v_cvt_pk_bf16_f32 v106, v108, v109\n"
  "v_cvt_pk_bf16_f32 v107, v110, v111\n"
  "buffer_store_dwordx4 v[104:107], v213, s[20:23], s47 offen offset:1024 sc1\n"
  "v_cvt_pk_bf16_f32 v112, v112, v113\n"
  "v_cvt_pk_bf16_f32 v113, v114, v115\n"
  "v_cvt_pk_bf16_f32 v114, v116, v117\n"
  "v_cvt_pk_bf16_f32 v115, v118, v119\n"
  "buffer_store_dwordx4 v[112:115], v213, s[20:23], s47 offen offset:2048 sc1\n"
  "v_cvt_pk_bf16_f32 v120, v120, v121\n"
  "v_cvt_pk_bf16_f32 v121, v122, v123\n"
  "v_cvt_pk_bf16_f32 v122, v124, v125\n"
  "v_cvt_pk_bf16_f32 v123, v126, v127\n"
  "buffer_store_dwordx4 v[120:123], v213, s[20:23], s47 offen offset:3072 sc1\n"
  "s_waitcnt vmcnt(8)\n"
  "buffer_store_dwordx2 v[216:217], v215, s[20:23], s30 offen offset:64 sc1\n"
  "s_waitcnt vmcnt(5)\n"
  "buffer_store_dwordx2 v[216:217], v215, s[20:23], s30 offen offset:128 sc1\n"
  "s_waitcnt vmcnt(2)\n"
  "buffer_store_dwordx2 v[216:217], v215, s[20:23], s30 offen offset:192 sc1\n"
  "s_lshl_b32 s60, s2, 2\n"
  "s_add_u32 s60, s60, s3\n"
  "s_cmp_lg_u32 s60, 0\n"
  "s_cbranch_scc1 DONE_%=\n"
  "v_mbcnt_lo_u32_b32 v1, -1, 0\n"
  "v_mbcnt_hi_u32_b32 v1, -1, v1\n"
  "v_lshlrev_b32 v2, 3, v1\n"
  "s_mov_b32 s65, 0\n"
  "UPD_%=:\n"
  "s_mov_b32 s60, 22085632\n"
  "buffer_load_dwordx2 v[8:9], v2, s[20:23], s60 offen offset:0 sc1\n"
  "buffer_load_dwordx2 v[10:11], v2, s[20:23], s60 offen offset:512 sc1\n"
  "buffer_load_dwordx2 v[12:13], v2, s[20:23], s60 offen offset:1024 sc1\n"
  "buffer_load_dwordx2 v[14:15], v2, s[20:23], s60 offen offset:1536 sc1\n"
  "buffer_load_dwordx2 v[16:17], v2, s[20:23], s60 offen offset:2048 sc1\n"
  "buffer_load_dwordx2 v[18:19], v2, s[20:23], s60 offen offset:2560 sc1\n"
  "buffer_load_dwordx2 v[20:21], v2, s[20:23], s60 offen offset:3072 sc1\n"
  "buffer_load_dwordx2 v[22:23], v2, s[20:23], s60 offen offset:3584 sc1\n"
  "s_mov_b32 s60, 22089728\n"
  "buffer_load_dwordx2 v[24:25], v2, s[20:23], s60 offen offset:0 sc1\n"
  "buffer_load_dwordx2 v[26:27], v2, s[20:23], s60 offen offset:512 sc1\n"
  "buffer_load_dwordx2 v[28:29], v2, s[20:23], s60 offen offset:1024 sc1\n"
  "buffer_load_dwordx2 v[30:31], v2, s[20:23], s60 offen offset:1536 sc1\n"
  "buffer_load_dwordx2 v[32:33], v2, s[20:23], s60 offen offset:2048 sc1\n"
  "buffer_load_dwordx2 v[34:35], v2, s[20:23], s60 offen offset:2560 sc1\n"
  "buffer_load_dwordx2 v[36:37], v2, s[20:23], s60 offen offset:3072 sc1\n"
  "buffer_load_dwordx2 v[38:39], v2, s[20:23], s60 offen offset:3584 sc1\n"
  "s_waitcnt vmcnt(0)\n"
  "s_mov_b64 s[62:63], 0\n"
  "v_cmp_ne_u64 vcc, v[8:9], v[216:217]\n"
  "s_or_b64 s[62:63], s[62:63], vcc\n"
  "v_cmp_ne_u64 vcc, v[10:11], v[216:217]\n"
  "s_or_b64 s[62:63], s[62:63], vcc\n"
  "v_cmp_ne_u64 vcc, v[12:13], v[216:217]\n"
  "s_or_b64 s[62:63], s[62:63], vcc\n"
  "v_cmp_ne_u64 vcc, v[14:15], v[216:217]\n"
  "s_or_b64 s[62:63], s[62:63], vcc\n"
  "v_cmp_ne_u64 vcc, v[16:17], v[216:217]\n"
  "DP5_%=:\n"
  "s_cbranch_execz DPE_%=\n"
  "s_or_b64 s[62:63], s[62:63], vcc\n"
  "v_cmp_ne_u64 vcc, v[18:19], v[216:217]\n"
  "s_or_b64 s[62:63], s[62:63], vcc\n"
  "v_cmp_ne_u64 vcc, v[20:21], v[216:217]\n"
  "s_or_b64 s[62:63], s[62:63], vcc\n"
  "v_cmp_ne_u64 vcc, v[22:23], v[216:217]\n"
  "s_or_b64 s[62:63], s[62:63], vcc\n"
  "v_cmp_ne_u64 vcc, v[24:25], v[216:217]\n"
  "s_or_b64 s[62:63], s[62:63], vcc\n"
  "v_cmp_ne_u64 vcc, v[26:27], v[216:217]\n"
  "s_or_b64 s[62:63], s[62:63], vcc\n"
  "v_cmp_ne_u64 vcc, v[28:29], v[216:217]\n"
  "s_or_b64 s[62:63], s[62:63], vcc\n"
  "v_cmp_ne_u64 vcc, v[30:31], v[216:217]\n"
  "s_or_b64 s[62:63], s[62:63], vcc\n"
  "v_cmp_ne_u64 vcc, v[32:33], v[216:217]\n"
  "s_or_b64 s[62:63], s[62:63], vcc\n"
  "v_cmp_ne_u64 vcc, v[34:35], v[216:217]\n"
  "s_or_b64 s[62:63], s[62:63], vcc\n"
  "v_cmp_ne_u64 vcc, v[36:37], v[216:217]\n"
  "s_or_b64 s[62:63], s[62:63], vcc\n"
  "v_cmp_ne_u64 vcc, v[38:39], v[216:217]\n"
  "s_or_b64 s[62:63], s[62:63], vcc\n"
  "s_cbranch_scc0 UPD2_%=\n"
  "s_sleep 4\n"
  "s_add_u32 s65, s65, 1\n"
  "s_cmp_lt_u32 s65, 0x20000\n"
  "s_cbranch_scc1 UPD_%=\n"
  "UPD2_%=:\n"
  "s_mov_b32 s60, 22142976\n"
  "buffer_store_dwordx2 v[216:217], v215, s[20:23], s60 offen sc1\n"
  "DONE_%=:\n"
  "s_endpgm\n"
  "DP6_%=:\n"
  "s_cbranch_execz DPE_%=\n"
  "SLA_%=:\n"
  "s_add_u32 s65, s65, 1\n"
  "s_cmp_gt_u32 s65, 0x40000\n"
  "s_cbranch_scc1 BKA_%=\n"
  "buffer_load_dwordx2 v[218:219], v214, s[20:23], s29 offen sc1\n"
  "s_waitcnt vmcnt(0)\n"
  "v_cmp_ne_u64 vcc, v[218:219], v[216:217]\n"
  "s_cbranch_vccz BKA_%=\n"
  "s_branch SLA_%=\n"
  "SLB_%=:\n"
  "s_add_u32 s65, s65, 1\n"
  "s_cmp_gt_u32 s65, 0x40000\n"
  "s_cbranch_scc1 BKB_%=\n"
  "buffer_load_dwordx2 v[218:219], v214, s[20:23], s29 offen sc1\n"
  "s_waitcnt vmcnt(0)\n"
  "v_cmp_ne_u64 vcc, v[218:219], v[216:217]\n"
  "s_cbranch_vccz BKB_%=\n"
  "s_branch SLB_%=\n"
  "SLC_%=:\n"
  "s_add_u32 s65, s65, 1\n"
  "s_cmp_gt_u32 s65, 0x40000\n"
  "s_cbranch_scc1 BKC_%=\n"
  "buffer_load_dwordx2 v[218:219], v214, s[20:23], s29 offen sc1\n"
  "s_waitcnt vmcnt(0)\n"
  "v_cmp_ne_u64 vcc, v[218:219], v[216:217]\n"
  "s_cbranch_vccz BKC_%=\n"
  "s_branch SLC_%=\n"
  "DFTEND_%=:\n"
  ".p2align 12\n"
  "FFN_%=:\n"
  "s_getpc_b64 s[60:61]\n"
  "FFN_pc_%=:\n"
  "s_and_b32 s61, s61, 0xffff\n"
  "s_mov_b32 s62, FFNEND_%=-FFN_pc_%=\n"
  "s_mov_b32 s63, 0x20000\n"
  "s_lshl_b32 s64, s34, 2\n"
  "s_add_u32 s64, s64, s3\n"
  "s_lshl_b32 s64, s64, 7\n"
  "v_lshl_add_u32 v254, v1, 13, s64\n"
  "buffer_load_dword v255, v254, s[60:63], 0 offen\n"
  "s_and_b32 s60, s35, 1\n"
  "s_lshl_b32 s60, s60, 1\n"
  "s_and_b32 s61, s35, 4\n"
  "s_add_u32 s60, s60, s61\n"
  "s_lshr_b32 s61, s34, 3\n"
  "s_and_b32 s62, s35, 2\n"
  "s_add_u32 s35, s60, s61\n"
  "s_lshl_b32 s62, s62, 2\n"
  "s_and_b32 s34, s34, 7\n"
  "s_add_u32 s34, s34, s62\n"
  "v_lshrrev_b32 v2, 3, v1\n"
  "v_lshrrev_b32 v3, 4, v1\n"
  "v_and_b32 v4, 7, v1\n"
  "v_and_b32 v5, 1, v3\n"
  "v_and_b32 v6, 15, v1\n"
  "v_bfe_u32 v7, v1, 2, 2\n"
  "v_and_b32 v8, 3, v1\n"
  "s_and_b32 s70, s3, 1\n"
  "s_lshr_b32 s71, s3, 1\n"
  "s_lshl_b32 s60, s34, 7\n"
  "s_lshl_b32 s61, s3, 5\n"
  "s_add_u32 s60, s60, s61\n"
  "v_add_u32 v12, s60, v2\n"
  "v_lshlrev_b32 v12, 10, v12\n"
  "v_xor_b32 v13, v4, v3\n"
  "v_lshl_add_u32 v224, v13, 4, v12\n"
  "v_xor_b32 v13, 4, v13\n"
  "v_lshl_add_u32 v13, v13, 4, v12\n"
  "v_add_u32 v225, 7168, v13\n"
  "v_add_u32 v226, 14336, v224\n"
  "v_add_u32 v227, 14336, v225\n"
  "s_lshl_b32 s60, s71, 5\n"
  "v_add_u32 v12, s60, v2\n"
  "v_mul_u32_u24 v12, 0xe00, v12\n"
  "s_lshl_b32 s60, s35, 7\n"
  "v_lshlrev_b32 v13, 1, v5\n"
  "v_xor_b32 v13, v13, v4\n"
  "v_lshlrev_b32 v14, 4, v13\n"
  "v_add3_u32 v216, v12, v14, s60\n"
  "v_xor_b32 v13, 64, v216\n"
  "v_add_u32 v217, 27648, v13\n"
  "v_add_u32 v218, 55296, v216\n"
  "v_add_u32 v219, 55296, v217\n"
  "s_add_u32 s72, s34, s35\n"
  "s_add_u32 s60, s72, 0\n"
  "s_and_b32 s60, s60, 7\n"
  "s_mul_i32 s36, s60, 229376\n"
  "s_lshl_b32 s44, s60, 7\n"
  "s_add_u32 s60, s72, 1\n"
  "s_and_b32 s60, s60, 7\n"
  "s_mul_i32 s37, s60, 229376\n"
  "s_lshl_b32 s45, s60, 7\n"
  "s_add_u32 s60, s72, 2\n"
  "s_and_b32 s60, s60, 7\n"
  "s_mul_i32 s38, s60, 229376\n"
  "s_lshl_b32 s46, s60, 7\n"
  "s_add_u32 s60, s72, 3\n"
  "s_and_b32 s60, s60, 7\n"
  "s_mul_i32 s39, s60, 229376\n"
  "s_lshl_b32 s47, s60, 7\n"
  "s_add_u32 s60, s72, 4\n"
  "s_and_b32 s60, s60, 7\n"
  "s_mul_i32 s40, s60, 229376\n"
  "s_lshl_b32 s48, s60, 7\n"
  "s_add_u32 s60, s72, 5\n"
  "s_and_b32 s60, s60, 7\n"
  "s_mul_i32 s41, s60, 229376\n"
  "s_lshl_b32 s49, s60, 7\n"
  "s_add_u32 s60, s72, 6\n"
  "s_and_b32 s60, s60, 7\n"
  "s_mul_i32 s42, s60, 229376\n"
  "s_lshl_b32 s50, s60, 7\n"
  "s_add_u32 s60, s72, 7\n"
  "s_and_b32 s60, s60, 7\n"
  "s_mul_i32 s43, s60, 229376\n"
  "s_lshl_b32 s51, s60, 7\n"
  "s_lshl_b32 s24, s3, 12\n"
  "s_waitcnt lgkmcnt(0)\n"
  "s_sleep 12\n"
  "s_mov_b32 s4, s76\n"
  "s_and_b32 s5, s77, 0xffff\n"
  "s_mov_b32 s6, 2097152\n"
  "s_mov_b32 s7, 0x20000\n"
  "s_cmp_lg_u32 s70, 0\n"
  "s_cselect_b32 s8, s88, s86\n"
  "s_cselect_b32 s9, s89, s87\n"
  "s_and_b32 s9, s9, 0xffff\n"
  "s_mov_b32 s10, 1835008\n"
  "s_mov_b32 s11, 0x20000\n"
  "s_add_u32 m0, s24, 0\n"
  "s_nop 0\n"
  "buffer_load_dwordx4 v224, s[4:7], s44 offen lds\n"
  "buffer_load_dwordx4 v225, s[4:7], s44 offen offset:1024 lds\n"
  "buffer_load_dwordx4 v226, s[4:7], s44 offen offset:2048 lds\n"
  "buffer_load_dwordx4 v227, s[4:7], s44 offen offset:3072 lds\n"
  "s_add_u32 m0, s24, 16384\n"
  "s_nop 0\n"
  "buffer_load_dwordx4 v216, s[8:11], s36 offen lds\n"
  "buffer_load_dwordx4 v217, s[8:11], s36 offen offset:1024 lds\n"
  "buffer_load_dwordx4 v218, s[8:11], s36 offen offset:2048 lds\n"
  "buffer_load_dwordx4 v219, s[8:11], s36 offen offset:3072 lds\n"
  "s_mov_b32 s16, s94\n"
  "s_and_b32 s17, s95, 0xffff\n"
  "s_mov_b32 s18, 22409216\n"
  "s_mov_b32 s19, 0x20000\n"
  "v_lshlrev_b32 v12, 4, v3\n"
  "v_add_u32 v12, s44, v12\n"
  "global_load_dwordx4 v[160:163], v12, s[92:93] offset:0\n"
  "global_load_dwordx4 v[164:167], v12, s[92:93] offset:64\n"
  "s_mov_b32 s60, 22142976\n"
  "buffer_load_dwordx2 v[250:251], off, s[16:19], s60 sc1\n"
  "s_mov_b32 s12, s90\n"
  "s_and_b32 s13, s91, 0xffff\n"
  "s_mov_b32 s14, 1835008\n"
  "s_mov_b32 s15, 0x20000\n"
  "s_mov_b32 s20, s96\n"
  "s_and_b32 s21, s97, 0xffff\n"
  "s_mov_b32 s22, 2097152\n"
  "s_mov_b32 s23, 0x20000\n"
  "v_lshlrev_b32 v249, 3, v6\n"
  "v_lshlrev_b32 v244, 4, v1\n"
  "v_add_u32 v245, 57344, v244\n"
  "s_lshl_b32 s60, s35, 1\n"
  "s_add_u32 s60, s60, s70\n"
  "s_lshl_b32 s61, s60, 3\n"
  "s_lshl_b32 s62, s34, 1\n"
  "s_add_u32 s62, s62, s71\n"
  "s_lshl_b32 s62, s62, 7\n"
  "s_add_u32 s61, s61, s62\n"
  "s_add_u32 s30, s61, 22089728\n"
  "v_cmp_eq_u32 vcc, 0, v1\n"
  "v_mov_b32 v13, 1073741824\n"
  "s_nop 0\n"
  "v_cndmask_b32 v248, v13, v1, vcc\n"
  "s_lshl_b32 s60, s60, 10\n"
  "s_lshl_b32 s61, s34, 2\n"
  "s_add_u32 s61, s61, s3\n"
  "s_mul_i32 s62, s61, 114688\n"
  "s_add_u32 s27, s62, s60\n"
  "s_lshl_b32 s63, s35, 10\n"
  "s_add_u32 s73, s62, s63\n"
  "s_lshl_b32 s63, s61, 10\n"
  "s_lshl_b32 s64, s35, 6\n"
  "s_add_u32 s63, s63, s64\n"
  "s_add_u32 s58, s63, 22020096\n"
  "s_xor_b32 s61, s61, 1\n"
  "s_mul_i32 s62, s61, 114688\n"
  "s_add_u32 s28, s62, s60\n"
  "s_and_b32 s82, s2, 7\n"
  "s_bfe_u32 s83, s2, 0x40003\n"
  "s_lshr_b32 s60, s82, 1\n"
  "s_lshl_b32 s60, s60, 4\n"
  "s_and_b32 s61, s83, 12\n"
  "s_add_u32 s60, s60, s61\n"
  "s_add_u32 s74, s60, s3\n"
  "s_mul_i32 s56, s74, 114688\n"
  "s_and_b32 s64, s82, 1\n"
  "s_and_b32 s75, s83, 3\n"
  "s_lshl_b32 s75, s75, 1\n"
  "s_add_u32 s75, s75, s64\n"
  "s_lshr_b32 s61, s74, 1\n"
  "s_lshl_b32 s61, s61, 7\n"
  "s_add_u32 s31, s61, 22089728\n"
  "s_lshl_b32 s60, s60, 9\n"
  "s_lshl_b32 s61, s75, 8\n"
  "s_add_u32 s60, s60, s61\n"
  "s_lshl_b32 s61, s3, 6\n"
  "s_add_u32 s60, s60, s61\n"
  "s_add_u32 s72, s60, 22106112\n"
  "s_xor_b32 s60, s60, 256\n"
  "s_add_u32 s93, s60, 22106144\n"
  "s_lshl_b32 s60, s60, 6\n"
  "s_add_u32 s89, s60, 15728640\n"
  "s_xor_b32 s60, s60, 16384\n"
  "s_add_u32 s92, s60, 15728640\n"
  "s_lshl_b32 s60, s60, 2\n"
  "s_add_u32 s59, s60, 7340032\n"
  "s_lshr_b32 s62, s83, 2\n"
  "s_xor_b32 s62, s62, s83\n"
  "s_lshl_b32 s63, s75, 7\n"
  "s_add_u32 s61, s62, 0\n"
  "s_and_b32 s61, s61, 1\n"
  "s_lshl_b32 s60, s64, 1\n"
  "s_add_u32 s61, s61, s60\n"
  "s_lshl_b32 s60, s61, 12\n"
  "s_add_u32 s76, s60, s56\n"
  "s_lshl_b32 s60, s61, 17\n"
  "s_add_u32 s86, s60, s63\n"
  "s_xor_b32 s79, s86, 128\n"
  "s_add_u32 s61, s62, 1\n"
  "s_and_b32 s61, s61, 1\n"
  "s_lshl_b32 s60, s64, 1\n"
  "s_add_u32 s61, s61, s60\n"
  "s_lshl_b32 s60, s61, 12\n"
  "s_add_u32 s77, s60, s56\n"
  "s_lshl_b32 s60, s61, 17\n"
  "s_add_u32 s87, s60, s63\n"
  "s_xor_b32 s80, s87, 128\n"
  "s_mov_b32 s61, s64\n"
  "s_lshl_b32 s60, s61, 12\n"
  "s_add_u32 s78, s60, s56\n"
  "s_lshl_b32 s60, s61, 17\n"
  "s_add_u32 s88, s60, s63\n"
  "s_xor_b32 s81, s88, 128\n"
  "s_lshl_b32 s56, s64, 6\n"
  "v_lshrrev_b32 v12, 2, v1\n"
  "v_mul_u32_u24 v12, 0xe00, v12\n"
  "v_lshrrev_b32 v13, 5, v1\n"
  "v_lshlrev_b32 v13, 1, v13\n"
  "v_xor_b32 v13, v13, v8\n"
  "v_lshlrev_b32 v13, 4, v13\n"
  "s_lshl_b32 s60, s35, 6\n"
  "s_add_u32 s60, s60, 3072\n"
  "v_add3_u32 v220, v12, v13, s60\n"
  "v_add_u32 v221, 56320, v220\n"
  "v_add_u32 v222, 112640, v220\n"
  "v_add_u32 v223, 168960, v220\n"
  "s_cmp_lg_u32 s71, 0\n"
  "s_cselect_b32 s52, s37, s36\n"
  "s_cselect_b32 s53, s39, s38\n"
  "s_cselect_b32 s54, s41, s40\n"
  "s_cselect_b32 s55, s43, s42\n"
  "s_lshl_b32 s60, s3, 5\n"
  "v_add_u32 v12, s60, v2\n"
  "v_lshlrev_b32 v12, 10, v12\n"
  "v_lshlrev_b32 v13, 1, v3\n"
  "v_xor_b32 v13, v13, v4\n"
  "v_lshlrev_b32 v13, 4, v13\n"
  "v_add_u32 v236, v12, v13\n"
  "v_add_u32 v237, 7168, v236\n"
  "v_add_u32 v238, 14336, v236\n"
  "v_add_u32 v239, 21504, v236\n"
  "v_lshl_add_u32 v14, v3, 3, v7\n"
  "v_lshrrev_b32 v12, 1, v7\n"
  "v_lshl_add_u32 v12, v5, 1, v12\n"
  "s_lshl_b32 s60, s70, 1\n"
  "v_xor_b32 v12, s60, v12\n"
  "v_lshlrev_b32 v15, 3, v8\n"
  "v_lshl_add_u32 v13, v12, 5, v15\n"
  "v_lshl_add_u32 v228, v14, 7, v13\n"
  "v_xor_b32 v229, 32, v228\n"
  "v_lshl_add_u32 v13, v5, 5, v15\n"
  "v_lshl_add_u32 v230, v14, 6, v13\n"
  "v_xor_b32 v231, 32, v230\n"
  "v_bfe_u32 v12, v1, 1, 3\n"
  "v_xor_b32 v12, v12, v3\n"
  "v_lshlrev_b32 v13, 7, v6\n"
  "v_lshl_add_u32 v13, v12, 4, v13\n"
  "v_add_u32 v232, s24, v13\n"
  "v_xor_b32 v233, 64, v232\n"
  "v_xor_b32 v234, 0x1000, v232\n"
  "v_xor_b32 v235, 0x1000, v233\n"
  "v_lshl_add_u32 v14, v3, 2, v7\n"
  "v_bfe_u32 v12, v14, 1, 2\n"
  "v_lshl_add_u32 v13, v12, 5, v15\n"
  "v_lshl_add_u32 v252, v14, 7, v13\n"
  "v_cmp_gt_u32 vcc, 16, v1\n"
  "v_lshlrev_b32 v12, 2, v1\n"
  "v_mov_b32 v13, 1073741824\n"
  "v_cndmask_b32 v240, v13, v12, vcc\n"
  "s_mov_b32 s84, 0xbfb8aa3b\n"
  "s_mov_b32 s85, s84\n"
  "s_mov_b32 s25, 0xbfb8aa3b\n"
  "s_mov_b32 s65, 0\n"
  "s_and_b32 s60, s2, 127\n"
  "s_lshl_b32 s60, s60, 2\n"
  "s_add_u32 s60, s60, s3\n"
  "s_lshl_b32 s60, s60, 7\n"
  "v_lshl_add_u32 v241, v1, 16, s60\n"
  "s_lshl_b32 s60, s83, 1\n"
  "s_add_u32 s60, s60, s71\n"
  "s_mul_i32 s60, s60, 3584\n"
  "s_lshl_b32 s61, s35, 6\n"
  "s_add_u32 s60, s60, s61\n"
  "s_add_u32 s60, s60, 3072\n"
  "v_mul_u32_u24 v242, 114688, v1\n"
  "v_add_u32 v242, s60, v242\n"
  "s_add_u32 s26, s36, 1024\n"
  "s_add_u32 m0, s24, 32768\n"
  "s_nop 0\n"
  "buffer_load_dwordx4 v216, s[8:11], s26 offen lds\n"
  "buffer_load_dwordx4 v217, s[8:11], s26 offen offset:1024 lds\n"
  "buffer_load_dwordx4 v218, s[8:11], s26 offen offset:2048 lds\n"
  "buffer_load_dwordx4 v219, s[8:11], s26 offen offset:3072 lds\n"
  "s_mov_b64 exec, 0\n"
  "s_cmp_eq_u32 s3, 0\n"
  "s_cbranch_scc1 FP1_%=\n"
  "s_cmp_eq_u32 s3, 1\n"
  "s_cbranch_scc1 FP2_%=\n"
  "s_cmp_eq_u32 s3, 2\n"
  "s_cbranch_scc1 FP3_%=\n"
  "s_branch FP4_%=\n"
  "FPE_%=:\n"
  "s_mov_b64 exec, -1\n"
  "s_waitcnt vmcnt(7)\n"
  "s_barrier\n"
  "ds_read_b128 a[0:3], v232 offset:0\n"
  "ds_read_b128 a[8:11], v233 offset:0\n"
  "ds_read_b128 a[4:7], v232 offset:2048\n"
  "ds_read_b128 a[12:15], v233 offset:2048\n"
  "ds_read_b128 a[128:131], v234 offset:0\n"
  "ds_read_b128 a[136:139], v235 offset:0\n"
  "ds_read_b128 a[132:135], v234 offset:2048\n"
  "ds_read_b128 a[140:143], v235 offset:2048\n"
  "ds_read_b64_tr_b16 v[192:193], v228 offset:16384\n"
  "ds_read_b64_tr_b16 v[194:195], v228 offset:16896\n"
  "ds_read_b64_tr_b16 v[196:197], v228 offset:24576\n"
  "ds_read_b64_tr_b16 v[198:199], v228 offset:25088\n"
  "ds_read_b64_tr_b16 v[200:201], v228 offset:20480\n"
  "ds_read_b64_tr_b16 v[202:203], v228 offset:20992\n"
  "ds_read_b64_tr_b16 v[204:205], v228 offset:28672\n"
  "ds_read_b64_tr_b16 v[206:207], v228 offset:29184\n"
  "s_waitcnt lgkmcnt(4)\n"
  "v_mfma_f32_16x16x32_bf16 v[0:3], v[192:195], a[0:3], 0\n"
  "ds_read_b64_tr_b16 v[208:209], v229 offset:16384\n"
  "ds_read_b64_tr_b16 v[210:211], v229 offset:16896\n"
  "v_mfma_f32_16x16x32_bf16 v[0:3], v[196:199], a[8:11], v[0:3]\n"
  "s_add_u32 m0, s24, 49152\n"
  "v_mfma_f32_16x16x32_bf16 v[16:19], v[192:195], a[4:7], 0\n"
  "ds_read_b64_tr_b16 v[212:213], v229 offset:24576\n"
  "ds_read_b64_tr_b16 v[214:215], v229 offset:25088\n"
  "v_mfma_f32_16x16x32_bf16 v[16:19], v[196:199], a[12:15], v[16:19]\n"
  "buffer_load_dwordx4 v224, s[4:7], s45 offen lds\n"
  "v_mfma_f32_16x16x32_bf16 v[32:35], v[192:195], a[128:131], 0\n"
  "v_mfma_f32_16x16x32_bf16 v[32:35], v[196:199], a[136:139], v[32:35]\n"
  "v_mfma_f32_16x16x32_bf16 v[48:51], v[192:195], a[132:135], 0\n"
  "v_mfma_f32_16x16x32_bf16 v[48:51], v[196:199], a[140:143], v[48:51]\n"
  "buffer_load_dwordx4 v225, s[4:7], s45 offen offset:1024 lds\n"
  "s_waitcnt lgkmcnt(4)\n"
  "v_mfma_f32_16x16x32_bf16 v[8:11], v[200:203], a[0:3], 0\n"
  "ds_read_b64_tr_b16 v[192:193], v229 offset:20480\n"
  "ds_read_b64_tr_b16 v[194:195], v229 offset:20992\n"
  "v_mfma_f32_16x16x32_bf16 v[8:11], v[204:207], a[8:11], v[8:11]\n"
  "v_mfma_f32_16x16x32_bf16 v[24:27], v[200:203], a[4:7], 0\n"
  "ds_read_b64_tr_b16 v[196:197], v229 offset:28672\n"
  "ds_read_b64_tr_b16 v[198:199], v229 offset:29184\n"
  "v_mfma_f32_16x16x32_bf16 v[24:27], v[204:207], a[12:15], v[24:27]\n"
  "buffer_load_dwordx4 v226, s[4:7], s45 offen offset:2048 lds\n"
  "v_mfma_f32_16x16x32_bf16 v[40:43], v[200:203], a[128:131], 0\n"
  "buffer_load_dwordx4 v227, s[4:7], s45 offen offset:3072 lds\n"
  "v_mfma_f32_16x16x32_bf16 v[40:43], v[204:207], a[136:139], v[40:43]\n"
  "s_add_u32 m0, s24, 65536\n"
  "v_mfma_f32_16x16x32_bf16 v[56:59], v[200:203], a[132:135], 0\n"
  "buffer_load_dwordx4 v216, s[8:11], s37 offen lds\n"
  "v_mfma_f32_16x16x32_bf16 v[56:59], v[204:207], a[140:143], v[56:59]\n"
  "buffer_load_dwordx4 v217, s[8:11], s37 offen offset:1024 lds\n"
  "s_waitcnt vmcnt(6) lgkmcnt(4)\n"
  "s_barrier\n"
  "v_mfma_f32_16x16x32_bf16 v[4:7], v[208:211], a[0:3], 0\n"
  "ds_read_b64_tr_b16 v[200:201], v228 offset:32768\n"
  "ds_read_b64_tr_b16 v[202:203], v228 offset:33280\n"
  "v_mfma_f32_16x16x32_bf16 v[4:7], v[212:215], a[8:11], v[4:7]\n"
  "buffer_load_dwordx4 v218, s[8:11], s37 offen offset:2048 lds\n"
  "v_mfma_f32_16x16x32_bf16 v[20:23], v[208:211], a[4:7], 0\n"
  "ds_read_b64_tr_b16 v[204:205], v228 offset:40960\n"
  "ds_read_b64_tr_b16 v[206:207], v228 offset:41472\n"
  "v_mfma_f32_16x16x32_bf16 v[20:23], v[212:215], a[12:15], v[20:23]\n"
  "buffer_load_dwordx4 v219, s[8:11], s37 offen offset:3072 lds\n"
  "v_mfma_f32_16x16x32_bf16 v[36:39], v[208:211], a[128:131], 0\n"
  "v_mfma_f32_16x16x32_bf16 v[36:39], v[212:215], a[136:139], v[36:39]\n"
  "v_mfma_f32_16x16x32_bf16 v[52:55], v[208:211], a[132:135], 0\n"
  "s_add_u32 s26, s37, 1024\n"
  "s_add_u32 m0, s24, 81920\n"
  "v_mfma_f32_16x16x32_bf16 v[52:55], v[212:215], a[140:143], v[52:55]\n"
  "buffer_load_dwordx4 v216, s[8:11], s26 offen lds\n"
  "s_waitcnt lgkmcnt(4)\n"
  "v_mfma_f32_16x16x32_bf16 v[12:15], v[192:195], a[0:3], 0\n"
  "ds_read_b64_tr_b16 v[208:209], v228 offset:36864\n"
  "ds_read_b64_tr_b16 v[210:211], v228 offset:37376\n"
  "v_mfma_f32_16x16x32_bf16 v[12:15], v[196:199], a[8:11], v[12:15]\n"
  "v_mfma_f32_16x16x32_bf16 v[28:31], v[192:195], a[4:7], 0\n"
  "ds_read_b64_tr_b16 v[212:213], v228 offset:45056\n"
  "ds_read_b64_tr_b16 v[214:215], v228 offset:45568\n"
  "v_mfma_f32_16x16x32_bf16 v[28:31], v[196:199], a[12:15], v[28:31]\n"
  "buffer_load_dwordx4 v217, s[8:11], s26 offen offset:1024 lds\n"
  "v_mfma_f32_16x16x32_bf16 v[44:47], v[192:195], a[128:131], 0\n"
  "v_mfma_f32_16x16x32_bf16 v[44:47], v[196:199], a[136:139], v[44:47]\n"
  "v_mfma_f32_16x16x32_bf16 v[60:63], v[192:195], a[132:135], 0\n"
  "v_mfma_f32_16x16x32_bf16 v[60:63], v[196:199], a[140:143], v[60:63]\n"
  "buffer_load_dwordx4 v218, s[8:11], s26 offen offset:2048 lds\n"
  "s_waitcnt lgkmcnt(4)\n"
  "v_mfma_f32_16x16x32_bf16 v[64:67], v[200:203], a[0:3], 0\n"
  "ds_read_b64_tr_b16 v[192:193], v229 offset:32768\n"
  "ds_read_b64_tr_b16 v[194:195], v229 offset:33280\n"
  "v_mfma_f32_16x16x32_bf16 v[64:67], v[204:207], a[8:11], v[64:67]\n"
  "v_mfma_f32_16x16x32_bf16 v[80:83], v[200:203], a[4:7], 0\n"
  "ds_read_b64_tr_b16 v[196:197], v229 offset:40960\n"
  "ds_read_b64_tr_b16 v[198:199], v229 offset:41472\n"
  "v_mfma_f32_16x16x32_bf16 v[80:83], v[204:207], a[12:15], v[80:83]\n"
  "buffer_load_dwordx4 v219, s[8:11], s26 offen offset:3072 lds\n"
  "v_mfma_f32_16x16x32_bf16 v[96:99], v[200:203], a[128:131], 0\n"
  "v_mfma_f32_16x16x32_bf16 v[96:99], v[204:207], a[136:139], v[96:99]\n"
  "v_mfma_f32_16x16x32_bf16 v[112:115], v[200:203], a[132:135], 0\n"
  "v_mfma_f32_16x16x32_bf16 v[112:115], v[204:207], a[140:143], v[112:115]\n"
  "s_waitcnt vmcnt(4) lgkmcnt(4)\n"
  "s_barrier\n"
  "v_mfma_f32_16x16x32_bf16 v[72:75], v[208:211], a[0:3], 0\n"
  "ds_read_b64_tr_b16 v[200:201], v229 offset:36864\n"
  "ds_read_b64_tr_b16 v[202:203], v229 offset:37376\n"
  "v_mfma_f32_16x16x32_bf16 v[168:171], v[160:163], a[0:3], 0\n"
  "v_mfma_f32_16x16x32_bf16 v[168:171], v[164:167], a[8:11], v[168:171]\n"
  "v_mfma_f32_16x16x32_bf16 v[172:175], v[160:163], a[4:7], 0\n"
  "v_mfma_f32_16x16x32_bf16 v[172:175], v[164:167], a[12:15], v[172:175]\n"
  "v_add_u32 v250, 0x9e3779b9, v250\n"
  "v_mfma_f32_16x16x32_bf16 v[72:75], v[212:215], a[8:11], v[72:75]\n"
  "s_add_u32 m0, s24, 98304\n"
  "v_mfma_f32_16x16x32_bf16 v[88:91], v[208:211], a[4:7], 0\n"
  "ds_read_b64_tr_b16 v[204:205], v229 offset:45056\n"
  "ds_read_b64_tr_b16 v[206:207], v229 offset:45568\n"
  "v_mfma_f32_16x16x32_bf16 v[88:91], v[212:215], a[12:15], v[88:91]\n"
  "ds_read_b128 a[16:19], v232 offset:49152\n"
  "ds_read_b128 a[24:27], v233 offset:49152\n"
  "v_mfma_f32_16x16x32_bf16 v[104:107], v[208:211], a[128:131], 0\n"
  "ds_read_b128 a[20:23], v232 offset:51200\n"
  "ds_read_b128 a[28:31], v233 offset:51200\n"
  "v_mfma_f32_16x16x32_bf16 v[104:107], v[212:215], a[136:139], v[104:107]\n"
  "buffer_load_dwordx4 v224, s[4:7], s46 offen lds\n"
  "v_mfma_f32_16x16x32_bf16 v[120:123], v[208:211], a[132:135], 0\n"
  "ds_read_b128 a[144:147], v234 offset:49152\n"
  "ds_read_b128 a[152:155], v235 offset:49152\n"
  "buffer_store_dword v168, v240, s[16:19], s58 offen offset:0 sc1\n"
  "v_mfma_f32_16x16x32_bf16 v[120:123], v[212:215], a[140:143], v[120:123]\n"
  "buffer_store_dword v172, v240, s[16:19], s58 offen offset:512 sc1\n"
  "s_waitcnt lgkmcnt(10)\n"
  "v_mfma_f32_16x16x32_bf16 v[68:71], v[192:195], a[0:3], 0\n"
  "ds_read_b128 a[148:151], v234 offset:51200\n"
  "ds_read_b128 a[156:159], v235 offset:51200\n"
  "v_mfma_f32_16x16x32_bf16 v[68:71], v[196:199], a[8:11], v[68:71]\n"
  "v_mfma_f32_16x16x32_bf16 v[84:87], v[192:195], a[4:7], 0\n"
  "v_add_u32 v228, 0x10000, v228\n"
  "ds_read_b64_tr_b16 v[208:209], v228 offset:0\n"
  "v_mfma_f32_16x16x32_bf16 v[84:87], v[196:199], a[12:15], v[84:87]\n"
  "buffer_load_dwordx4 v225, s[4:7], s46 offen offset:1024 lds\n"
  "v_mfma_f32_16x16x32_bf16 v[100:103], v[192:195], a[128:131], 0\n"
  "ds_read_b64_tr_b16 v[210:211], v228 offset:512\n"
  "ds_read_b64_tr_b16 v[212:213], v228 offset:8192\n"
  "v_mfma_f32_16x16x32_bf16 v[100:103], v[196:199], a[136:139], v[100:103]\n"
  "v_mfma_f32_16x16x32_bf16 v[116:119], v[192:195], a[132:135], 0\n"
  "ds_read_b64_tr_b16 v[214:215], v228 offset:8704\n"
  "v_mfma_f32_16x16x32_bf16 v[116:119], v[196:199], a[140:143], v[116:119]\n"
  "buffer_load_dwordx4 v226, s[4:7], s46 offen offset:2048 lds\n"
  "s_waitcnt lgkmcnt(12)\n"
  "v_mfma_f32_16x16x32_bf16 v[76:79], v[200:203], a[0:3], 0\n"
  "ds_read_b64_tr_b16 v[192:193], v228 offset:4096\n"
  "ds_read_b64_tr_b16 v[194:195], v228 offset:4608\n"
  "v_mfma_f32_16x16x32_bf16 v[76:79], v[204:207], a[8:11], v[76:79]\n"
  "v_mfma_f32_16x16x32_bf16 v[92:95], v[200:203], a[4:7], 0\n"
  "ds_read_b64_tr_b16 v[196:197], v228 offset:12288\n"
  "ds_read_b64_tr_b16 v[198:199], v228 offset:12800\n"
  "v_mfma_f32_16x16x32_bf16 v[92:95], v[204:207], a[12:15], v[92:95]\n"
  "buffer_load_dwordx4 v227, s[4:7], s46 offen offset:3072 lds\n"
  "v_mfma_f32_16x16x32_bf16 v[108:111], v[200:203], a[128:131], 0\n"
  "v_mfma_f32_16x16x32_bf16 v[108:111], v[204:207], a[136:139], v[108:111]\n"
  "v_mfma_f32_16x16x32_bf16 v[124:127], v[200:203], a[132:135], 0\n"
  "s_add_u32 m0, s24, 114688\n"
  "v_mfma_f32_16x16x32_bf16 v[124:127], v[204:207], a[140:143], v[124:127]\n"
  "buffer_load_dwordx4 v216, s[8:11], s38 offen lds\n"
  "s_waitcnt lgkmcnt(4)\n"
  "v_mfma_f32_16x16x32_bf16 v[0:3], v[208:211], a[16:19], v[0:3]\n"
  "v_add_u32 v229, 0x10000, v229\n"
  "ds_read_b64_tr_b16 v[200:201], v229 offset:0\n"
  "v_mfma_f32_16x16x32_bf16 v[0:3], v[212:215], a[24:27], v[0:3]\n"
  "v_mfma_f32_16x16x32_bf16 v[16:19], v[208:211], a[20:23], v[16:19]\n"
  "ds_read_b64_tr_b16 v[202:203], v229 offset:512\n"
  "ds_read_b64_tr_b16 v[204:205], v229 offset:8192\n"
  "v_mfma_f32_16x16x32_bf16 v[16:19], v[212:215], a[28:31], v[16:19]\n"
  "buffer_load_dwordx4 v217, s[8:11], s38 offen offset:1024 lds\n"
  "v_mfma_f32_16x16x32_bf16 v[32:35], v[208:211], a[144:147], v[32:35]\n"
  "ds_read_b64_tr_b16 v[206:207], v229 offset:8704\n"
  "v_mfma_f32_16x16x32_bf16 v[32:35], v[212:215], a[152:155], v[32:35]\n"
  "v_mfma_f32_16x16x32_bf16 v[48:51], v[208:211], a[148:151], v[48:51]\n"
  "v_mfma_f32_16x16x32_bf16 v[48:51], v[212:215], a[156:159], v[48:51]\n"
  "buffer_load_dwordx4 v218, s[8:11], s38 offen offset:2048 lds\n"
  "s_waitcnt lgkmcnt(4)\n"
  "v_mfma_f32_16x16x32_bf16 v[8:11], v[192:195], a[16:19], v[8:11]\n"
  "ds_read_b64_tr_b16 v[208:209], v229 offset:4096\n"
  "ds_read_b64_tr_b16 v[210:211], v229 offset:4608\n"
  "v_mfma_f32_16x16x32_bf16 v[8:11], v[196:199], a[24:27], v[8:11]\n"
  "v_mfma_f32_16x16x32_bf16 v[24:27], v[192:195], a[20:23], v[24:27]\n"
  "ds_read_b64_tr_b16 v[212:213], v229 offset:12288\n"
  "ds_read_b64_tr_b16 v[214:215], v229 offset:12800\n"
  "v_mfma_f32_16x16x32_bf16 v[24:27], v[196:199], a[28:31], v[24:27]\n"
  "buffer_load_dwordx4 v219, s[8:11], s38 offen offset:3072 lds\n"
  "v_mfma_f32_16x16x32_bf16 v[40:43], v[192:195], a[144:147], v[40:43]\n"
  "v_mfma_f32_16x16x32_bf16 v[40:43], v[196:199], a[152:155], v[40:43]\n"
  "v_mfma_f32_16x16x32_bf16 v[56:59], v[192:195], a[148:151], v[56:59]\n"
  "s_add_u32 s26, s38, 1024\n"
  "s_add_u32 m0, s24, 131072\n"
  "v_mfma_f32_16x16x32_bf16 v[56:59], v[196:199], a[156:159], v[56:59]\n"
  "s_waitcnt vmcnt(10) lgkmcnt(4)\n"
  "s_barrier\n"
  "v_mfma_f32_16x16x32_bf16 v[4:7], v[200:203], a[16:19], v[4:7]\n"
  "ds_read_b64_tr_b16 v[192:193], v228 offset:16384\n"
  "ds_read_b64_tr_b16 v[194:195], v228 offset:16896\n"
  "v_mfma_f32_16x16x32_bf16 v[4:7], v[204:207], a[24:27], v[4:7]\n"
  "buffer_load_dwordx4 v216, s[8:11], s26 offen lds\n"
  "v_mfma_f32_16x16x32_bf16 v[20:23], v[200:203], a[20:23], v[20:23]\n"
  "ds_read_b64_tr_b16 v[196:197], v228 offset:24576\n"
  "ds_read_b64_tr_b16 v[198:199], v228 offset:25088\n"
  "v_mfma_f32_16x16x32_bf16 v[20:23], v[204:207], a[28:31], v[20:23]\n"
  "v_mfma_f32_16x16x32_bf16 v[36:39], v[200:203], a[144:147], v[36:39]\n"
  "v_mfma_f32_16x16x32_bf16 v[36:39], v[204:207], a[152:155], v[36:39]\n"
  "buffer_load_dwordx4 v217, s[8:11], s26 offen offset:1024 lds\n"
  "v_mfma_f32_16x16x32_bf16 v[52:55], v[200:203], a[148:151], v[52:55]\n"
  "v_mfma_f32_16x16x32_bf16 v[52:55], v[204:207], a[156:159], v[52:55]\n"
  "s_waitcnt lgkmcnt(4)\n"
  "v_mfma_f32_16x16x32_bf16 v[12:15], v[208:211], a[16:19], v[12:15]\n"
  "ds_read_b64_tr_b16 v[200:201], v228 offset:20480\n"
  "ds_read_b64_tr_b16 v[202:203], v228 offset:20992\n"
  "v_mfma_f32_16x16x32_bf16 v[12:15], v[212:215], a[24:27], v[12:15]\n"
  "buffer_load_dwordx4 v218, s[8:11], s26 offen offset:2048 lds\n"
  "v_mfma_f32_16x16x32_bf16 v[28:31], v[208:211], a[20:23], v[28:31]\n"
  "ds_read_b64_tr_b16 v[204:205], v228 offset:28672\n"
  "ds_read_b64_tr_b16 v[206:207], v228 offset:29184\n"
  "v_mfma_f32_16x16x32_bf16 v[28:31], v[212:215], a[28:31], v[28:31]\n"
  "v_mfma_f32_16x16x32_bf16 v[44:47], v[208:211], a[144:147], v[44:47]\n"
  "v_mfma_f32_16x16x32_bf16 v[44:47], v[212:215], a[152:155], v[44:47]\n"
  "buffer_load_dwordx4 v219, s[8:11], s26 offen offset:3072 lds\n"
  "v_mfma_f32_16x16x32_bf16 v[60:63], v[208:211], a[148:151], v[60:63]\n"
  "v_mfma_f32_16x16x32_bf16 v[60:63], v[212:215], a[156:159], v[60:63]\n"
  "s_waitcnt lgkmcnt(4)\n"
  "v_mfma_f32_16x16x32_bf16 v[64:67], v[192:195], a[16:19], v[64:67]\n"
  "ds_read_b64_tr_b16 v[208:209], v229 offset:16384\n"
  "ds_read_b64_tr_b16 v[210:211], v229 offset:16896\n"
  "v_mfma_f32_16x16x32_bf16 v[64:67], v[196:199], a[24:27], v[64:67]\n"
  "s_add_u32 m0, s24, 147456\n"
  "v_mfma_f32_16x16x32_bf16 v[80:83], v[192:195], a[20:23], v[80:83]\n"
  "ds_read_b64_tr_b16 v[212:213], v229 offset:24576\n"
  "ds_read_b64_tr_b16 v[214:215], v229 offset:25088\n"
  "v_mfma_f32_16x16x32_bf16 v[80:83], v[196:199], a[28:31], v[80:83]\n"
  "buffer_load_dwordx4 v224, s[4:7], s47 offen lds\n"
  "v_mfma_f32_16x16x32_bf16 v[96:99], v[192:195], a[144:147], v[96:99]\n"
  "v_mfma_f32_16x16x32_bf16 v[96:99], v[196:199], a[152:155], v[96:99]\n"
  "v_mfma_f32_16x16x32_bf16 v[112:115], v[192:195], a[148:151], v[112:115]\n"
  "v_mfma_f32_16x16x32_bf16 v[112:115], v[196:199], a[156:159], v[112:115]\n"
  "s_waitcnt vmcnt(5) lgkmcnt(4)\n"
  "s_barrier\n"
  "v_mfma_f32_16x16x32_bf16 v[72:75], v[200:203], a[16:19], v[72:75]\n"
  "ds_read_b64_tr_b16 v[192:193], v229 offset:20480\n"
  "ds_read_b64_tr_b16 v[194:195], v229 offset:20992\n"
  "v_mfma_f32_16x16x32_bf16 v[72:75], v[204:207], a[24:27], v[72:75]\n"
  "buffer_load_dwordx4 v225, s[4:7], s47 offen offset:1024 lds\n"
  "v_mfma_f32_16x16x32_bf16 v[88:91], v[200:203], a[20:23], v[88:91]\n"
  "ds_read_b64_tr_b16 v[196:197], v229 offset:28672\n"
  "ds_read_b64_tr_b16 v[198:199], v229 offset:29184\n"
  "v_mfma_f32_16x16x32_bf16 v[88:91], v[204:207], a[28:31], v[88:91]\n"
  "v_add_u32 v232, 0x10000, v232\n"
  "ds_read_b128 a[32:35], v232 offset:32768\n"
  "v_mfma_f32_16x16x32_bf16 v[104:107], v[200:203], a[144:147], v[104:107]\n"
  "v_add_u32 v233, 0x10000, v233\n"
  "ds_read_b128 a[40:43], v233 offset:32768\n"
  "v_mfma_f32_16x16x32_bf16 v[104:107], v[204:207], a[152:155], v[104:107]\n"
  "ds_read_b128 a[36:39], v232 offset:34816\n"
  "ds_read_b128 a[44:47], v233 offset:34816\n"
  "v_mfma_f32_16x16x32_bf16 v[120:123], v[200:203], a[148:151], v[120:123]\n"
  "buffer_load_dwordx4 v226, s[4:7], s47 offen offset:2048 lds\n"
  "v_mfma_f32_16x16x32_bf16 v[120:123], v[204:207], a[156:159], v[120:123]\n"
  "v_add_u32 v234, 0x10000, v234\n"
  "ds_read_b128 a[160:163], v234 offset:32768\n"
  "s_waitcnt lgkmcnt(9)\n"
  "v_mfma_f32_16x16x32_bf16 v[68:71], v[208:211], a[16:19], v[68:71]\n"
  "v_add_u32 v235, 0x10000, v235\n"
  "ds_read_b128 a[168:171], v235 offset:32768\n"
  "v_mfma_f32_16x16x32_bf16 v[68:71], v[212:215], a[24:27], v[68:71]\n"
  "ds_read_b128 a[164:167], v234 offset:34816\n"
  "ds_read_b128 a[172:175], v235 offset:34816\n"
  "v_mfma_f32_16x16x32_bf16 v[84:87], v[208:211], a[20:23], v[84:87]\n"
  "buffer_load_dwordx4 v227, s[4:7], s47 offen offset:3072 lds\n"
  "v_mfma_f32_16x16x32_bf16 v[84:87], v[212:215], a[28:31], v[84:87]\n"
  "ds_read_b64_tr_b16 v[200:201], v228 offset:49152\n"
  "ds_read_b64_tr_b16 v[202:203], v228 offset:49664\n"
  "v_mfma_f32_16x16x32_bf16 v[100:103], v[208:211], a[144:147], v[100:103]\n"
  "v_mfma_f32_16x16x32_bf16 v[100:103], v[212:215], a[152:155], v[100:103]\n"
  "ds_read_b64_tr_b16 v[204:205], v228 offset:57344\n"
  "ds_read_b64_tr_b16 v[206:207], v228 offset:57856\n"
  "v_mfma_f32_16x16x32_bf16 v[116:119], v[208:211], a[148:151], v[116:119]\n"
  "s_add_u32 m0, s24, 0\n"
  "v_mfma_f32_16x16x32_bf16 v[116:119], v[212:215], a[156:159], v[116:119]\n"
  "buffer_load_dwordx4 v216, s[8:11], s39 offen lds\n"
  "s_waitcnt lgkmcnt(12)\n"
  "v_mfma_f32_16x16x32_bf16 v[76:79], v[192:195], a[16:19], v[76:79]\n"
  "ds_read_b64_tr_b16 v[208:209], v228 offset:53248\n"
  "ds_read_b64_tr_b16 v[210:211], v228 offset:53760\n"
  "v_mfma_f32_16x16x32_bf16 v[76:79], v[196:199], a[24:27], v[76:79]\n"
  "v_mfma_f32_16x16x32_bf16 v[92:95], v[192:195], a[20:23], v[92:95]\n"
  "ds_read_b64_tr_b16 v[212:213], v228 offset:61440\n"
  "ds_read_b64_tr_b16 v[214:215], v228 offset:61952\n"
  "v_mfma_f32_16x16x32_bf16 v[92:95], v[196:199], a[28:31], v[92:95]\n"
  "buffer_load_dwordx4 v217, s[8:11], s39 offen offset:1024 lds\n"
  "v_mfma_f32_16x16x32_bf16 v[108:111], v[192:195], a[144:147], v[108:111]\n"
  "v_mfma_f32_16x16x32_bf16 v[108:111], v[196:199], a[152:155], v[108:111]\n"
  "v_mfma_f32_16x16x32_bf16 v[124:127], v[192:195], a[148:151], v[124:127]\n"
  "v_mfma_f32_16x16x32_bf16 v[124:127], v[196:199], a[156:159], v[124:127]\n"
  "buffer_load_dwordx4 v218, s[8:11], s39 offen offset:2048 lds\n"
  "s_waitcnt lgkmcnt(4)\n"
  "v_mfma_f32_16x16x32_bf16 v[0:3], v[200:203], a[32:35], v[0:3]\n"
  "ds_read_b64_tr_b16 v[192:193], v229 offset:49152\n"
  "ds_read_b64_tr_b16 v[194:195], v229 offset:49664\n"
  "v_mfma_f32_16x16x32_bf16 v[0:3], v[204:207], a[40:43], v[0:3]\n"
  "v_mfma_f32_16x16x32_bf16 v[16:19], v[200:203], a[36:39], v[16:19]\n"
  "ds_read_b64_tr_b16 v[196:197], v229 offset:57344\n"
  "ds_read_b64_tr_b16 v[198:199], v229 offset:57856\n"
  "v_mfma_f32_16x16x32_bf16 v[16:19], v[204:207], a[44:47], v[16:19]\n"
  "buffer_load_dwordx4 v219, s[8:11], s39 offen offset:3072 lds\n"
  "v_mfma_f32_16x16x32_bf16 v[32:35], v[200:203], a[160:163], v[32:35]\n"
  "v_mfma_f32_16x16x32_bf16 v[32:35], v[204:207], a[168:171], v[32:35]\n"
  "v_mfma_f32_16x16x32_bf16 v[48:51], v[200:203], a[164:167], v[48:51]\n"
  "s_add_u32 s26, s39, 1024\n"
  "s_add_u32 m0, s24, 16384\n"
  "v_mfma_f32_16x16x32_bf16 v[48:51], v[204:207], a[172:175], v[48:51]\n"
  "buffer_load_dwordx4 v216, s[8:11], s26 offen lds\n"
  "s_waitcnt lgkmcnt(4)\n"
  "v_mfma_f32_16x16x32_bf16 v[8:11], v[208:211], a[32:35], v[8:11]\n"
  "ds_read_b64_tr_b16 v[200:201], v229 offset:53248\n"
  "ds_read_b64_tr_b16 v[202:203], v229 offset:53760\n"
  "FP1_%=:\n"
  "s_cbranch_execz FP5_%=\n"
  "v_mfma_f32_16x16x32_bf16 v[8:11], v[212:215], a[40:43], v[8:11]\n"
  "v_mfma_f32_16x16x32_bf16 v[24:27], v[208:211], a[36:39], v[24:27]\n"
  "ds_read_b64_tr_b16 v[204:205], v229 offset:61440\n"
  "ds_read_b64_tr_b16 v[206:207], v229 offset:61952\n"
  "v_mfma_f32_16x16x32_bf16 v[24:27], v[212:215], a[44:47], v[24:27]\n"
  "buffer_load_dwordx4 v217, s[8:11], s26 offen offset:1024 lds\n"
  "v_mfma_f32_16x16x32_bf16 v[40:43], v[208:211], a[160:163], v[40:43]\n"
  "v_mfma_f32_16x16x32_bf16 v[40:43], v[212:215], a[168:171], v[40:43]\n"
  "v_mfma_f32_16x16x32_bf16 v[56:59], v[208:211], a[164:167], v[56:59]\n"
  "v_mfma_f32_16x16x32_bf16 v[56:59], v[212:215], a[172:175], v[56:59]\n"
  "s_waitcnt vmcnt(10) lgkmcnt(4)\n"
  "s_barrier\n"
  "v_mfma_f32_16x16x32_bf16 v[4:7], v[192:195], a[32:35], v[4:7]\n"
  "v_add_u32 v228, 0x10000, v228\n"
  "ds_read_b64_tr_b16 v[208:209], v228 offset:0\n"
  "v_mfma_f32_16x16x32_bf16 v[4:7], v[196:199], a[40:43], v[4:7]\n"
  "buffer_load_dwordx4 v218, s[8:11], s26 offen offset:2048 lds\n"
  "v_mfma_f32_16x16x32_bf16 v[20:23], v[192:195], a[36:39], v[20:23]\n"
  "ds_read_b64_tr_b16 v[210:211], v228 offset:512\n"
  "ds_read_b64_tr_b16 v[212:213], v228 offset:8192\n"
  "v_mfma_f32_16x16x32_bf16 v[20:23], v[196:199], a[44:47], v[20:23]\n"
  "v_mfma_f32_16x16x32_bf16 v[36:39], v[192:195], a[160:163], v[36:39]\n"
  "ds_read_b64_tr_b16 v[214:215], v228 offset:8704\n"
  "v_mfma_f32_16x16x32_bf16 v[36:39], v[196:199], a[168:171], v[36:39]\n"
  "buffer_load_dwordx4 v219, s[8:11], s26 offen offset:3072 lds\n"
  "v_mfma_f32_16x16x32_bf16 v[52:55], v[192:195], a[164:167], v[52:55]\n"
  "v_mfma_f32_16x16x32_bf16 v[52:55], v[196:199], a[172:175], v[52:55]\n"
  "s_waitcnt lgkmcnt(4)\n"
  "v_mfma_f32_16x16x32_bf16 v[12:15], v[200:203], a[32:35], v[12:15]\n"
  "ds_read_b64_tr_b16 v[192:193], v228 offset:4096\n"
  "ds_read_b64_tr_b16 v[194:195], v228 offset:4608\n"
  "v_mfma_f32_16x16x32_bf16 v[12:15], v[204:207], a[40:43], v[12:15]\n"
  "s_add_u32 m0, s24, 32768\n"
  "v_mfma_f32_16x16x32_bf16 v[28:31], v[200:203], a[36:39], v[28:31]\n"
  "ds_read_b64_tr_b16 v[196:197], v228 offset:12288\n"
  "ds_read_b64_tr_b16 v[198:199], v228 offset:12800\n"
  "v_mfma_f32_16x16x32_bf16 v[28:31], v[204:207], a[44:47], v[28:31]\n"
  "buffer_load_dwordx4 v224, s[4:7], s48 offen lds\n"
  "v_mfma_f32_16x16x32_bf16 v[44:47], v[200:203], a[160:163], v[44:47]\n"
  "v_mfma_f32_16x16x32_bf16 v[44:47], v[204:207], a[168:171], v[44:47]\n"
  "v_mfma_f32_16x16x32_bf16 v[60:63], v[200:203], a[164:167], v[60:63]\n"
  "v_mfma_f32_16x16x32_bf16 v[60:63], v[204:207], a[172:175], v[60:63]\n"
  "buffer_load_dwordx4 v225, s[4:7], s48 offen offset:1024 lds\n"
  "s_waitcnt lgkmcnt(4)\n"
  "v_mfma_f32_16x16x32_bf16 v[64:67], v[208:211], a[32:35], v[64:67]\n"
  "v_add_u32 v229, 0x10000, v229\n"
  "ds_read_b64_tr_b16 v[200:201], v229 offset:0\n"
  "v_mfma_f32_16x16x32_bf16 v[64:67], v[212:215], a[40:43], v[64:67]\n"
  "v_mfma_f32_16x16x32_bf16 v[80:83], v[208:211], a[36:39], v[80:83]\n"
  "ds_read_b64_tr_b16 v[202:203], v229 offset:512\n"
  "ds_read_b64_tr_b16 v[204:205], v229 offset:8192\n"
  "v_mfma_f32_16x16x32_bf16 v[80:83], v[212:215], a[44:47], v[80:83]\n"
  "buffer_load_dwordx4 v226, s[4:7], s48 offen offset:2048 lds\n"
  "v_mfma_f32_16x16x32_bf16 v[96:99], v[208:211], a[160:163], v[96:99]\n"
  "ds_read_b64_tr_b16 v[206:207], v229 offset:8704\n"
  "v_mfma_f32_16x16x32_bf16 v[96:99], v[212:215], a[168:171], v[96:99]\n"
  "v_mfma_f32_16x16x32_bf16 v[112:115], v[208:211], a[164:167], v[112:115]\n"
  "v_mfma_f32_16x16x32_bf16 v[112:115], v[212:215], a[172:175], v[112:115]\n"
  "s_waitcnt vmcnt(7) lgkmcnt(4)\n"
  "s_barrier\n"
  "v_mfma_f32_16x16x32_bf16 v[72:75], v[192:195], a[32:35], v[72:75]\n"
  "ds_read_b64_tr_b16 v[208:209], v229 offset:4096\n"
  "ds_read_b64_tr_b16 v[210:211], v229 offset:4608\n"
  "v_mfma_f32_16x16x32_bf16 v[72:75], v[196:199], a[40:43], v[72:75]\n"
  "buffer_load_dwordx4 v227, s[4:7], s48 offen offset:3072 lds\n"
  "v_mfma_f32_16x16x32_bf16 v[88:91], v[192:195], a[36:39], v[88:91]\n"
  "ds_read_b64_tr_b16 v[212:213], v229 offset:12288\n"
  "ds_read_b64_tr_b16 v[214:215], v229 offset:12800\n"
  "v_mfma_f32_16x16x32_bf16 v[88:91], v[196:199], a[44:47], v[88:91]\n"
  "v_add_u32 v232, 0x10000, v232\n"
  "ds_read_b128 a[48:51], v232 offset:16384\n"
  "v_mfma_f32_16x16x32_bf16 v[104:107], v[192:195], a[160:163], v[104:107]\n"
  "v_add_u32 v233, 0x10000, v233\n"
  "ds_read_b128 a[56:59], v233 offset:16384\n"
  "v_mfma_f32_16x16x32_bf16 v[104:107], v[196:199], a[168:171], v[104:107]\n"
  "ds_read_b128 a[52:55], v232 offset:18432\n"
  "ds_read_b128 a[60:63], v233 offset:18432\n"
  "v_mfma_f32_16x16x32_bf16 v[120:123], v[192:195], a[164:167], v[120:123]\n"
  "s_add_u32 m0, s24, 49152\n"
  "v_mfma_f32_16x16x32_bf16 v[120:123], v[196:199], a[172:175], v[120:123]\n"
  "v_add_u32 v234, 0x10000, v234\n"
  "ds_read_b128 a[176:179], v234 offset:16384\n"
  "s_waitcnt lgkmcnt(9)\n"
  "v_mfma_f32_16x16x32_bf16 v[68:71], v[200:203], a[32:35], v[68:71]\n"
  "v_add_u32 v235, 0x10000, v235\n"
  "ds_read_b128 a[184:187], v235 offset:16384\n"
  "v_mfma_f32_16x16x32_bf16 v[68:71], v[204:207], a[40:43], v[68:71]\n"
  "ds_read_b128 a[180:183], v234 offset:18432\n"
  "ds_read_b128 a[188:191], v235 offset:18432\n"
  "v_mfma_f32_16x16x32_bf16 v[84:87], v[200:203], a[36:39], v[84:87]\n"
  "buffer_load_dwordx4 v216, s[8:11], s40 offen lds\n"
  "v_mfma_f32_16x16x32_bf16 v[84:87], v[204:207], a[44:47], v[84:87]\n"
  "v_add_u32 v228, 0xfffe0000, v228\n"
  "ds_read_b64_tr_b16 v[192:193], v228 offset:0\n"
  "v_mfma_f32_16x16x32_bf16 v[100:103], v[200:203], a[160:163], v[100:103]\n"
  "v_mfma_f32_16x16x32_bf16 v[100:103], v[204:207], a[168:171], v[100:103]\n"
  "ds_read_b64_tr_b16 v[194:195], v228 offset:512\n"
  "ds_read_b64_tr_b16 v[196:197], v228 offset:8192\n"
  "v_mfma_f32_16x16x32_bf16 v[116:119], v[200:203], a[164:167], v[116:119]\n"
  "buffer_load_dwordx4 v217, s[8:11], s40 offen offset:1024 lds\n"
  "v_mfma_f32_16x16x32_bf16 v[116:119], v[204:207], a[172:175], v[116:119]\n"
  "ds_read_b64_tr_b16 v[198:199], v228 offset:8704\n"
  "s_waitcnt lgkmcnt(12)\n"
  "v_mfma_f32_16x16x32_bf16 v[76:79], v[208:211], a[32:35], v[76:79]\n"
  "v_mfma_f32_16x16x32_bf16 v[76:79], v[212:215], a[40:43], v[76:79]\n"
  "ds_read_b64_tr_b16 v[200:201], v228 offset:4096\n"
  "ds_read_b64_tr_b16 v[202:203], v228 offset:4608\n"
  "v_mfma_f32_16x16x32_bf16 v[92:95], v[208:211], a[36:39], v[92:95]\n"
  "buffer_load_dwordx4 v218, s[8:11], s40 offen offset:2048 lds\n"
  "v_mfma_f32_16x16x32_bf16 v[92:95], v[212:215], a[44:47], v[92:95]\n"
  "ds_read_b64_tr_b16 v[204:205], v228 offset:12288\n"
  "ds_read_b64_tr_b16 v[206:207], v228 offset:12800\n"
  "v_mfma_f32_16x16x32_bf16 v[108:111], v[208:211], a[160:163], v[108:111]\n"
  "v_mfma_f32_16x16x32_bf16 v[108:111], v[212:215], a[168:171], v[108:111]\n"
  "v_mfma_f32_16x16x32_bf16 v[124:127], v[208:211], a[164:167], v[124:127]\n"
  "buffer_load_dwordx4 v219, s[8:11], s40 offen offset:3072 lds\n"
  "v_mfma_f32_16x16x32_bf16 v[124:127], v[212:215], a[172:175], v[124:127]\n"
  "s_waitcnt lgkmcnt(4)\n"
  "v_mfma_f32_16x16x32_bf16 v[0:3], v[192:195], a[48:51], v[0:3]\n"
  "v_add_u32 v229, 0xfffe0000, v229\n"
  "ds_read_b64_tr_b16 v[208:209], v229 offset:0\n"
  "v_mfma_f32_16x16x32_bf16 v[0:3], v[196:199], a[56:59], v[0:3]\n"
  "s_add_u32 s26, s40, 1024\n"
  "s_add_u32 m0, s24, 65536\n"
  "v_mfma_f32_16x16x32_bf16 v[16:19], v[192:195], a[52:55], v[16:19]\n"
  "ds_read_b64_tr_b16 v[210:211], v229 offset:512\n"
  "ds_read_b64_tr_b16 v[212:213], v229 offset:8192\n"
  "v_mfma_f32_16x16x32_bf16 v[16:19], v[196:199], a[60:63], v[16:19]\n"
  "buffer_load_dwordx4 v216, s[8:11], s26 offen lds\n"
  "v_mfma_f32_16x16x32_bf16 v[32:35], v[192:195], a[176:179], v[32:35]\n"
  "ds_read_b64_tr_b16 v[214:215], v229 offset:8704\n"
  "v_mfma_f32_16x16x32_bf16 v[32:35], v[196:199], a[184:187], v[32:35]\n"
  "v_mfma_f32_16x16x32_bf16 v[48:51], v[192:195], a[180:183], v[48:51]\n"
  "v_mfma_f32_16x16x32_bf16 v[48:51], v[196:199], a[188:191], v[48:51]\n"
  "buffer_load_dwordx4 v217, s[8:11], s26 offen offset:1024 lds\n"
  "s_waitcnt lgkmcnt(4)\n"
  "v_mfma_f32_16x16x32_bf16 v[8:11], v[200:203], a[48:51], v[8:11]\n"
  "ds_read_b64_tr_b16 v[192:193], v229 offset:4096\n"
  "ds_read_b64_tr_b16 v[194:195], v229 offset:4608\n"
  "v_mfma_f32_16x16x32_bf16 v[8:11], v[204:207], a[56:59], v[8:11]\n"
  "v_mfma_f32_16x16x32_bf16 v[24:27], v[200:203], a[52:55], v[24:27]\n"
  "ds_read_b64_tr_b16 v[196:197], v229 offset:12288\n"
  "ds_read_b64_tr_b16 v[198:199], v229 offset:12800\n"
  "v_mfma_f32_16x16x32_bf16 v[24:27], v[204:207], a[60:63], v[24:27]\n"
  "buffer_load_dwordx4 v218, s[8:11], s26 offen offset:2048 lds\n"
  "v_mfma_f32_16x16x32_bf16 v[40:43], v[200:203], a[176:179], v[40:43]\n"
  "v_mfma_f32_16x16x32_bf16 v[40:43], v[204:207], a[184:187], v[40:43]\n"
  "v_mfma_f32_16x16x32_bf16 v[56:59], v[200:203], a[180:183], v[56:59]\n"
  "v_mfma_f32_16x16x32_bf16 v[56:59], v[204:207], a[188:191], v[56:59]\n"
  "s_waitcnt vmcnt(11) lgkmcnt(4)\n"
  "s_barrier\n"
  "v_mfma_f32_16x16x32_bf16 v[4:7], v[208:211], a[48:51], v[4:7]\n"
  "ds_read_b64_tr_b16 v[200:201], v228 offset:16384\n"
  "ds_read_b64_tr_b16 v[202:203], v228 offset:16896\n"
  "v_mfma_f32_16x16x32_bf16 v[4:7], v[212:215], a[56:59], v[4:7]\n"
  "buffer_load_dwordx4 v219, s[8:11], s26 offen offset:3072 lds\n"
  "v_mfma_f32_16x16x32_bf16 v[20:23], v[208:211], a[52:55], v[20:23]\n"
  "ds_read_b64_tr_b16 v[204:205], v228 offset:24576\n"
  "ds_read_b64_tr_b16 v[206:207], v228 offset:25088\n"
  "v_mfma_f32_16x16x32_bf16 v[20:23], v[212:215], a[60:63], v[20:23]\n"
  "v_mfma_f32_16x16x32_bf16 v[36:39], v[208:211], a[176:179], v[36:39]\n"
  "s_add_u32 m0, s24, 81920\n"
  "v_mfma_f32_16x16x32_bf16 v[36:39], v[212:215], a[184:187], v[36:39]\n"
  "buffer_load_dwordx4 v224, s[4:7], s49 offen lds\n"
  "v_mfma_f32_16x16x32_bf16 v[52:55], v[208:211], a[180:183], v[52:55]\n"
  "v_mfma_f32_16x16x32_bf16 v[52:55], v[212:215], a[188:191], v[52:55]\n"
  "s_waitcnt lgkmcnt(4)\n"
  "v_mfma_f32_16x16x32_bf16 v[12:15], v[192:195], a[48:51], v[12:15]\n"
  "ds_read_b64_tr_b16 v[208:209], v228 offset:20480\n"
  "ds_read_b64_tr_b16 v[210:211], v228 offset:20992\n"
  "v_mfma_f32_16x16x32_bf16 v[12:15], v[196:199], a[56:59], v[12:15]\n"
  "buffer_load_dwordx4 v225, s[4:7], s49 offen offset:1024 lds\n"
  "v_mfma_f32_16x16x32_bf16 v[28:31], v[192:195], a[52:55], v[28:31]\n"
  "ds_read_b64_tr_b16 v[212:213], v228 offset:28672\n"
  "ds_read_b64_tr_b16 v[214:215], v228 offset:29184\n"
  "v_mfma_f32_16x16x32_bf16 v[28:31], v[196:199], a[60:63], v[28:31]\n"
  "v_mfma_f32_16x16x32_bf16 v[44:47], v[192:195], a[176:179], v[44:47]\n"
  "v_mfma_f32_16x16x32_bf16 v[44:47], v[196:199], a[184:187], v[44:47]\n"
  "buffer_load_dwordx4 v226, s[4:7], s49 offen offset:2048 lds\n"
  "v_mfma_f32_16x16x32_bf16 v[60:63], v[192:195], a[180:183], v[60:63]\n"
  "v_mfma_f32_16x16x32_bf16 v[60:63], v[196:199], a[188:191], v[60:63]\n"
  "s_waitcnt lgkmcnt(4)\n"
  "v_mfma_f32_16x16x32_bf16 v[64:67], v[200:203], a[48:51], v[64:67]\n"
  "ds_read_b64_tr_b16 v[192:193], v229 offset:16384\n"
  "ds_read_b64_tr_b16 v[194:195], v229 offset:16896\n"
  "v_mfma_f32_16x16x32_bf16 v[64:67], v[204:207], a[56:59], v[64:67]\n"
  "buffer_load_dwordx4 v227, s[4:7], s49 offen offset:3072 lds\n"
  "v_mfma_f32_16x16x32_bf16 v[80:83], v[200:203], a[52:55], v[80:83]\n"
  "ds_read_b64_tr_b16 v[196:197], v229 offset:24576\n"
  "ds_read_b64_tr_b16 v[198:199], v229 offset:25088\n"
  "v_mfma_f32_16x16x32_bf16 v[80:83], v[204:207], a[60:63], v[80:83]\n"
  "v_mfma_f32_16x16x32_bf16 v[96:99], v[200:203], a[176:179], v[96:99]\n"
  "s_add_u32 m0, s24, 98304\n"
  "v_mfma_f32_16x16x32_bf16 v[96:99], v[204:207], a[184:187], v[96:99]\n"
  "buffer_load_dwordx4 v216, s[8:11], s41 offen lds\n"
  "v_mfma_f32_16x16x32_bf16 v[112:115], v[200:203], a[180:183], v[112:115]\n"
  "v_mfma_f32_16x16x32_bf16 v[112:115], v[204:207], a[188:191], v[112:115]\n"
  "s_waitcnt vmcnt(9) lgkmcnt(4)\n"
  "s_barrier\n"
  "v_mfma_f32_16x16x32_bf16 v[72:75], v[208:211], a[48:51], v[72:75]\n"
  "ds_read_b64_tr_b16 v[200:201], v229 offset:20480\n"
  "ds_read_b64_tr_b16 v[202:203], v229 offset:20992\n"
  "v_mfma_f32_16x16x32_bf16 v[72:75], v[212:215], a[56:59], v[72:75]\n"
  "buffer_load_dwordx4 v217, s[8:11], s41 offen offset:1024 lds\n"
  "v_mfma_f32_16x16x32_bf16 v[88:91], v[208:211], a[52:55], v[88:91]\n"
  "ds_read_b64_tr_b16 v[204:205], v229 offset:28672\n"
  "ds_read_b64_tr_b16 v[206:207], v229 offset:29184\n"
  "v_mfma_f32_16x16x32_bf16 v[88:91], v[212:215], a[60:63], v[88:91]\n"
  "v_add_u32 v232, 0xfffe0000, v232\n"
  "ds_read_b128 a[64:67], v232 offset:32768\n"
  "v_mfma_f32_16x16x32_bf16 v[104:107], v[208:211], a[176:179], v[104:107]\n"
  "v_add_u32 v233, 0xfffe0000, v233\n"
  "ds_read_b128 a[72:75], v233 offset:32768\n"
  "v_mfma_f32_16x16x32_bf16 v[104:107], v[212:215], a[184:187], v[104:107]\n"
  "ds_read_b128 a[68:71], v232 offset:34816\n"
  "ds_read_b128 a[76:79], v233 offset:34816\n"
  "v_mfma_f32_16x16x32_bf16 v[120:123], v[208:211], a[180:183], v[120:123]\n"
  "buffer_load_dwordx4 v218, s[8:11], s41 offen offset:2048 lds\n"
  "v_mfma_f32_16x16x32_bf16 v[120:123], v[212:215], a[188:191], v[120:123]\n"
  "v_add_u32 v234, 0xfffe0000, v234\n"
  "ds_read_b128 a[192:195], v234 offset:32768\n"
  "s_waitcnt lgkmcnt(9)\n"
  "v_mfma_f32_16x16x32_bf16 v[68:71], v[192:195], a[48:51], v[68:71]\n"
  "v_add_u32 v235, 0xfffe0000, v235\n"
  "ds_read_b128 a[200:203], v235 offset:32768\n"
  "v_mfma_f32_16x16x32_bf16 v[68:71], v[196:199], a[56:59], v[68:71]\n"
  "ds_read_b128 a[196:199], v234 offset:34816\n"
  "ds_read_b128 a[204:207], v235 offset:34816\n"
  "v_mfma_f32_16x16x32_bf16 v[84:87], v[192:195], a[52:55], v[84:87]\n"
  "buffer_load_dwordx4 v219, s[8:11], s41 offen offset:3072 lds\n"
  "v_mfma_f32_16x16x32_bf16 v[84:87], v[196:199], a[60:63], v[84:87]\n"
  "ds_read_b64_tr_b16 v[208:209], v228 offset:49152\n"
  "ds_read_b64_tr_b16 v[210:211], v228 offset:49664\n"
  "v_mfma_f32_16x16x32_bf16 v[100:103], v[192:195], a[176:179], v[100:103]\n"
  "v_mfma_f32_16x16x32_bf16 v[100:103], v[196:199], a[184:187], v[100:103]\n"
  "ds_read_b64_tr_b16 v[212:213], v228 offset:57344\n"
  "ds_read_b64_tr_b16 v[214:215], v228 offset:57856\n"
  "v_mfma_f32_16x16x32_bf16 v[116:119], v[192:195], a[180:183], v[116:119]\n"
  "s_add_u32 s26, s41, 1024\n"
  "s_add_u32 m0, s24, 114688\n"
  "v_mfma_f32_16x16x32_bf16 v[116:119], v[196:199], a[188:191], v[116:119]\n"
  "buffer_load_dwordx4 v216, s[8:11], s26 offen lds\n"
  "s_waitcnt lgkmcnt(12)\n"
  "v_mfma_f32_16x16x32_bf16 v[76:79], v[200:203], a[48:51], v[76:79]\n"
  "ds_read_b64_tr_b16 v[192:193], v228 offset:53248\n"
  "ds_read_b64_tr_b16 v[194:195], v228 offset:53760\n"
  "v_mfma_f32_16x16x32_bf16 v[76:79], v[204:207], a[56:59], v[76:79]\n"
  "v_mfma_f32_16x16x32_bf16 v[92:95], v[200:203], a[52:55], v[92:95]\n"
  "ds_read_b64_tr_b16 v[196:197], v228 offset:61440\n"
  "ds_read_b64_tr_b16 v[198:199], v228 offset:61952\n"
  "v_mfma_f32_16x16x32_bf16 v[92:95], v[204:207], a[60:63], v[92:95]\n"
  "buffer_load_dwordx4 v217, s[8:11], s26 offen offset:1024 lds\n"
  "v_mfma_f32_16x16x32_bf16 v[108:111], v[200:203], a[176:179], v[108:111]\n"
  "v_mfma_f32_16x16x32_bf16 v[108:111], v[204:207], a[184:187], v[108:111]\n"
  "v_mfma_f32_16x16x32_bf16 v[124:127], v[200:203], a[180:183], v[124:127]\n"
  "v_mfma_f32_16x16x32_bf16 v[124:127], v[204:207], a[188:191], v[124:127]\n"
  "buffer_load_dwordx4 v218, s[8:11], s26 offen offset:2048 lds\n"
  "s_waitcnt lgkmcnt(4)\n"
  "v_mfma_f32_16x16x32_bf16 v[0:3], v[208:211], a[64:67], v[0:3]\n"
  "ds_read_b64_tr_b16 v[200:201], v229 offset:49152\n"
  "ds_read_b64_tr_b16 v[202:203], v229 offset:49664\n"
  "v_mfma_f32_16x16x32_bf16 v[0:3], v[212:215], a[72:75], v[0:3]\n"
  "v_mfma_f32_16x16x32_bf16 v[16:19], v[208:211], a[68:71], v[16:19]\n"
  "ds_read_b64_tr_b16 v[204:205], v229 offset:57344\n"
  "ds_read_b64_tr_b16 v[206:207], v229 offset:57856\n"
  "v_mfma_f32_16x16x32_bf16 v[16:19], v[212:215], a[76:79], v[16:19]\n"
  "buffer_load_dwordx4 v219, s[8:11], s26 offen offset:3072 lds\n"
  "v_mfma_f32_16x16x32_bf16 v[32:35], v[208:211], a[192:195], v[32:35]\n"
  "v_mfma_f32_16x16x32_bf16 v[32:35], v[212:215], a[200:203], v[32:35]\n"
  "v_mfma_f32_16x16x32_bf16 v[48:51], v[208:211], a[196:199], v[48:51]\n"
  "s_add_u32 m0, s24, 131072\n"
  "v_mfma_f32_16x16x32_bf16 v[48:51], v[212:215], a[204:207], v[48:51]\n"
  "buffer_load_dwordx4 v224, s[4:7], s50 offen lds\n"
  "s_waitcnt lgkmcnt(4)\n"
  "v_mfma_f32_16x16x32_bf16 v[8:11], v[192:195], a[64:67], v[8:11]\n"
  "ds_read_b64_tr_b16 v[208:209], v229 offset:53248\n"
  "ds_read_b64_tr_b16 v[210:211], v229 offset:53760\n"
  "v_mfma_f32_16x16x32_bf16 v[8:11], v[196:199], a[72:75], v[8:11]\n"
  "v_mfma_f32_16x16x32_bf16 v[24:27], v[192:195], a[68:71], v[24:27]\n"
  "ds_read_b64_tr_b16 v[212:213], v229 offset:61440\n"
  "ds_read_b64_tr_b16 v[214:215], v229 offset:61952\n"
  "v_mfma_f32_16x16x32_bf16 v[24:27], v[196:199], a[76:79], v[24:27]\n"
  "buffer_load_dwordx4 v225, s[4:7], s50 offen offset:1024 lds\n"
  "v_mfma_f32_16x16x32_bf16 v[40:43], v[192:195], a[192:195], v[40:43]\n"
  "v_mfma_f32_16x16x32_bf16 v[40:43], v[196:199], a[200:203], v[40:43]\n"
  "v_mfma_f32_16x16x32_bf16 v[56:59], v[192:195], a[196:199], v[56:59]\n"
  "v_mfma_f32_16x16x32_bf16 v[56:59], v[196:199], a[204:207], v[56:59]\n"
  "s_waitcnt vmcnt(14) lgkmcnt(4)\n"
  "s_barrier\n"
  "v_mfma_f32_16x16x32_bf16 v[4:7], v[200:203], a[64:67], v[4:7]\n"
  "v_add_u32 v228, 0x10000, v228\n"
  "ds_read_b64_tr_b16 v[192:193], v228 offset:0\n"
  "v_mfma_f32_16x16x32_bf16 v[4:7], v[204:207], a[72:75], v[4:7]\n"
  "buffer_load_dwordx4 v226, s[4:7], s50 offen offset:2048 lds\n"
  "v_mfma_f32_16x16x32_bf16 v[20:23], v[200:203], a[68:71], v[20:23]\n"
  "ds_read_b64_tr_b16 v[194:195], v228 offset:512\n"
  "ds_read_b64_tr_b16 v[196:197], v228 offset:8192\n"
  "v_mfma_f32_16x16x32_bf16 v[20:23], v[204:207], a[76:79], v[20:23]\n"
  "v_mfma_f32_16x16x32_bf16 v[36:39], v[200:203], a[192:195], v[36:39]\n"
  "ds_read_b64_tr_b16 v[198:199], v228 offset:8704\n"
  "v_mfma_f32_16x16x32_bf16 v[36:39], v[204:207], a[200:203], v[36:39]\n"
  "buffer_load_dwordx4 v227, s[4:7], s50 offen offset:3072 lds\n"
  "v_mfma_f32_16x16x32_bf16 v[52:55], v[200:203], a[196:199], v[52:55]\n"
  "v_mfma_f32_16x16x32_bf16 v[52:55], v[204:207], a[204:207], v[52:55]\n"
  "s_waitcnt lgkmcnt(4)\n"
  "v_mfma_f32_16x16x32_bf16 v[12:15], v[208:211], a[64:67], v[12:15]\n"
  "ds_read_b64_tr_b16 v[200:201], v228 offset:4096\n"
  "ds_read_b64_tr_b16 v[202:203], v228 offset:4608\n"
  "v_mfma_f32_16x16x32_bf16 v[12:15], v[212:215], a[72:75], v[12:15]\n"
  "s_add_u32 m0, s24, 147456\n"
  "v_mfma_f32_16x16x32_bf16 v[28:31], v[208:211], a[68:71], v[28:31]\n"
  "ds_read_b64_tr_b16 v[204:205], v228 offset:12288\n"
  "ds_read_b64_tr_b16 v[206:207], v228 offset:12800\n"
  "v_mfma_f32_16x16x32_bf16 v[28:31], v[212:215], a[76:79], v[28:31]\n"
  "buffer_load_dwordx4 v216, s[8:11], s42 offen lds\n"
  "v_mfma_f32_16x16x32_bf16 v[44:47], v[208:211], a[192:195], v[44:47]\n"
  "v_mfma_f32_16x16x32_bf16 v[44:47], v[212:215], a[200:203], v[44:47]\n"
  "v_mfma_f32_16x16x32_bf16 v[60:63], v[208:211], a[196:199], v[60:63]\n"
  "v_mfma_f32_16x16x32_bf16 v[60:63], v[212:215], a[204:207], v[60:63]\n"
  "buffer_load_dwordx4 v217, s[8:11], s42 offen offset:1024 lds\n"
  "s_waitcnt lgkmcnt(4)\n"
  "v_mfma_f32_16x16x32_bf16 v[64:67], v[192:195], a[64:67], v[64:67]\n"
  "v_add_u32 v229, 0x10000, v229\n"
  "ds_read_b64_tr_b16 v[208:209], v229 offset:0\n"
  "v_mfma_f32_16x16x32_bf16 v[64:67], v[196:199], a[72:75], v[64:67]\n"
  "v_mfma_f32_16x16x32_bf16 v[80:83], v[192:195], a[68:71], v[80:83]\n"
  "ds_read_b64_tr_b16 v[210:211], v229 offset:512\n"
  "ds_read_b64_tr_b16 v[212:213], v229 offset:8192\n"
  "v_mfma_f32_16x16x32_bf16 v[80:83], v[196:199], a[76:79], v[80:83]\n"
  "buffer_load_dwordx4 v218, s[8:11], s42 offen offset:2048 lds\n"
  "v_mfma_f32_16x16x32_bf16 v[96:99], v[192:195], a[192:195], v[96:99]\n"
  "ds_read_b64_tr_b16 v[214:215], v229 offset:8704\n"
  "v_mfma_f32_16x16x32_bf16 v[96:99], v[196:199], a[200:203], v[96:99]\n"
  "v_mfma_f32_16x16x32_bf16 v[112:115], v[192:195], a[196:199], v[112:115]\n"
  "v_mfma_f32_16x16x32_bf16 v[112:115], v[196:199], a[204:207], v[112:115]\n"
  "s_waitcnt vmcnt(11) lgkmcnt(4)\n"
  "s_barrier\n"
  "v_mfma_f32_16x16x32_bf16 v[72:75], v[200:203], a[64:67], v[72:75]\n"
  "ds_read_b64_tr_b16 v[192:193], v229 offset:4096\n"
  "ds_read_b64_tr_b16 v[194:195], v229 offset:4608\n"
  "v_mfma_f32_16x16x32_bf16 v[72:75], v[204:207], a[72:75], v[72:75]\n"
  "buffer_load_dwordx4 v219, s[8:11], s42 offen offset:3072 lds\n"
  "v_mfma_f32_16x16x32_bf16 v[88:91], v[200:203], a[68:71], v[88:91]\n"
  "ds_read_b64_tr_b16 v[196:197], v229 offset:12288\n"
  "ds_read_b64_tr_b16 v[198:199], v229 offset:12800\n"
  "v_mfma_f32_16x16x32_bf16 v[88:91], v[204:207], a[76:79], v[88:91]\n"
  "v_add_u32 v232, 0x10000, v232\n"
  "ds_read_b128 a[80:83], v232 offset:16384\n"
  "v_mfma_f32_16x16x32_bf16 v[104:107], v[200:203], a[192:195], v[104:107]\n"
  "v_add_u32 v233, 0x10000, v233\n"
  "ds_read_b128 a[88:91], v233 offset:16384\n"
  "v_mfma_f32_16x16x32_bf16 v[104:107], v[204:207], a[200:203], v[104:107]\n"
  "ds_read_b128 a[84:87], v232 offset:18432\n"
  "ds_read_b128 a[92:95], v233 offset:18432\n"
  "v_mfma_f32_16x16x32_bf16 v[120:123], v[200:203], a[196:199], v[120:123]\n"
  "s_add_u32 s26, s42, 1024\n"
  "s_add_u32 m0, s24, 0\n"
  "v_mfma_f32_16x16x32_bf16 v[120:123], v[204:207], a[204:207], v[120:123]\n"
  "v_add_u32 v234, 0x10000, v234\n"
  "ds_read_b128 a[208:211], v234 offset:16384\n"
  "s_waitcnt lgkmcnt(9)\n"
  "v_mfma_f32_16x16x32_bf16 v[68:71], v[208:211], a[64:67], v[68:71]\n"
  "v_add_u32 v235, 0x10000, v235\n"
  "ds_read_b128 a[216:219], v235 offset:16384\n"
  "v_mfma_f32_16x16x32_bf16 v[68:71], v[212:215], a[72:75], v[68:71]\n"
  "ds_read_b128 a[212:215], v234 offset:18432\n"
  "ds_read_b128 a[220:223], v235 offset:18432\n"
  "v_mfma_f32_16x16x32_bf16 v[84:87], v[208:211], a[68:71], v[84:87]\n"
  "buffer_load_dwordx4 v216, s[8:11], s26 offen lds\n"
  "v_mfma_f32_16x16x32_bf16 v[84:87], v[212:215], a[76:79], v[84:87]\n"
  "ds_read_b64_tr_b16 v[200:201], v228 offset:32768\n"
  "ds_read_b64_tr_b16 v[202:203], v228 offset:33280\n"
  "v_mfma_f32_16x16x32_bf16 v[100:103], v[208:211], a[192:195], v[100:103]\n"
  "v_mfma_f32_16x16x32_bf16 v[100:103], v[212:215], a[200:203], v[100:103]\n"
  "ds_read_b64_tr_b16 v[204:205], v228 offset:40960\n"
  "ds_read_b64_tr_b16 v[206:207], v228 offset:41472\n"
  "v_mfma_f32_16x16x32_bf16 v[116:119], v[208:211], a[196:199], v[116:119]\n"
  "buffer_load_dwordx4 v217, s[8:11], s26 offen offset:1024 lds\n"
  "v_mfma_f32_16x16x32_bf16 v[116:119], v[212:215], a[204:207], v[116:119]\n"
  "s_waitcnt lgkmcnt(12)\n"
  "v_mfma_f32_16x16x32_bf16 v[76:79], v[192:195], a[64:67], v[76:79]\n"
  "ds_read_b64_tr_b16 v[208:209], v228 offset:36864\n"
  "ds_read_b64_tr_b16 v[210:211], v228 offset:37376\n"
  "v_mfma_f32_16x16x32_bf16 v[76:79], v[196:199], a[72:75], v[76:79]\n"
  "v_mfma_f32_16x16x32_bf16 v[92:95], v[192:195], a[68:71], v[92:95]\n"
  "ds_read_b64_tr_b16 v[212:213], v228 offset:45056\n"
  "ds_read_b64_tr_b16 v[214:215], v228 offset:45568\n"
  "v_mfma_f32_16x16x32_bf16 v[92:95], v[196:199], a[76:79], v[92:95]\n"
  "buffer_load_dwordx4 v218, s[8:11], s26 offen offset:2048 lds\n"
  "v_mfma_f32_16x16x32_bf16 v[108:111], v[192:195], a[192:195], v[108:111]\n"
  "v_mfma_f32_16x16x32_bf16 v[108:111], v[196:199], a[200:203], v[108:111]\n"
  "v_mfma_f32_16x16x32_bf16 v[124:127], v[192:195], a[196:199], v[124:127]\n"
  "v_mfma_f32_16x16x32_bf16 v[124:127], v[196:199], a[204:207], v[124:127]\n"
  "buffer_load_dwordx4 v219, s[8:11], s26 offen offset:3072 lds\n"
  "s_waitcnt lgkmcnt(4)\n"
  "v_mfma_f32_16x16x32_bf16 v[0:3], v[200:203], a[80:83], v[0:3]\n"
  "ds_read_b64_tr_b16 v[192:193], v229 offset:32768\n"
  "ds_read_b64_tr_b16 v[194:195], v229 offset:33280\n"
  "v_mfma_f32_16x16x32_bf16 v[0:3], v[204:207], a[88:91], v[0:3]\n"
  "v_mfma_f32_16x16x32_bf16 v[16:19], v[200:203], a[84:87], v[16:19]\n"
  "ds_read_b64_tr_b16 v[196:197], v229 offset:40960\n"
  "ds_read_b64_tr_b16 v[198:199], v229 offset:41472\n"
  "v_mfma_f32_16x16x32_bf16 v[16:19], v[204:207], a[92:95], v[16:19]\n"
  "s_add_u32 m0, s24, 16384\n"
  "v_mfma_f32_16x16x32_bf16 v[32:35], v[200:203], a[208:211], v[32:35]\n"
  "buffer_load_dwordx4 v224, s[4:7], s51 offen lds\n"
  "v_mfma_f32_16x16x32_bf16 v[32:35], v[204:207], a[216:219], v[32:35]\n"
  "v_mfma_f32_16x16x32_bf16 v[48:51], v[200:203], a[212:215], v[48:51]\n"
  "v_mfma_f32_16x16x32_bf16 v[48:51], v[204:207], a[220:223], v[48:51]\n"
  "s_waitcnt lgkmcnt(4)\n"
  "v_mfma_f32_16x16x32_bf16 v[8:11], v[208:211], a[80:83], v[8:11]\n"
  "ds_read_b64_tr_b16 v[200:201], v229 offset:36864\n"
  "ds_read_b64_tr_b16 v[202:203], v229 offset:37376\n"
  "v_mfma_f32_16x16x32_bf16 v[8:11], v[212:215], a[88:91], v[8:11]\n"
  "buffer_load_dwordx4 v225, s[4:7], s51 offen offset:1024 lds\n"
  "v_mfma_f32_16x16x32_bf16 v[24:27], v[208:211], a[84:87], v[24:27]\n"
  "ds_read_b64_tr_b16 v[204:205], v229 offset:45056\n"
  "ds_read_b64_tr_b16 v[206:207], v229 offset:45568\n"
  "v_mfma_f32_16x16x32_bf16 v[24:27], v[212:215], a[92:95], v[24:27]\n"
  "v_mfma_f32_16x16x32_bf16 v[40:43], v[208:211], a[208:211], v[40:43]\n"
  "v_mfma_f32_16x16x32_bf16 v[40:43], v[212:215], a[216:219], v[40:43]\n"
  "buffer_load_dwordx4 v226, s[4:7], s51 offen offset:2048 lds\n"
  "v_mfma_f32_16x16x32_bf16 v[56:59], v[208:211], a[212:215], v[56:59]\n"
  "v_mfma_f32_16x16x32_bf16 v[56:59], v[212:215], a[220:223], v[56:59]\n"
  "s_waitcnt vmcnt(15) lgkmcnt(4)\n"
  "s_barrier\n"
  "v_mfma_f32_16x16x32_bf16 v[4:7], v[192:195], a[80:83], v[4:7]\n"
  "ds_read_b64_tr_b16 v[208:209], v228 offset:49152\n"
  "ds_read_b64_tr_b16 v[210:211], v228 offset:49664\n"
  "v_mfma_f32_16x16x32_bf16 v[4:7], v[196:199], a[88:91], v[4:7]\n"
  "buffer_load_dwordx4 v227, s[4:7], s51 offen offset:3072 lds\n"
  "v_mfma_f32_16x16x32_bf16 v[20:23], v[192:195], a[84:87], v[20:23]\n"
  "ds_read_b64_tr_b16 v[212:213], v228 offset:57344\n"
  "ds_read_b64_tr_b16 v[214:215], v228 offset:57856\n"
  "v_mfma_f32_16x16x32_bf16 v[20:23], v[196:199], a[92:95], v[20:23]\n"
  "v_mfma_f32_16x16x32_bf16 v[36:39], v[192:195], a[208:211], v[36:39]\n"
  "s_add_u32 m0, s24, 32768\n"
  "v_mfma_f32_16x16x32_bf16 v[36:39], v[196:199], a[216:219], v[36:39]\n"
  "buffer_load_dwordx4 v216, s[8:11], s43 offen lds\n"
  "v_mfma_f32_16x16x32_bf16 v[52:55], v[192:195], a[212:215], v[52:55]\n"
  "v_mfma_f32_16x16x32_bf16 v[52:55], v[196:199], a[220:223], v[52:55]\n"
  "s_waitcnt lgkmcnt(4)\n"
  "v_mfma_f32_16x16x32_bf16 v[12:15], v[200:203], a[80:83], v[12:15]\n"
  "ds_read_b64_tr_b16 v[192:193], v228 offset:53248\n"
  "ds_read_b64_tr_b16 v[194:195], v228 offset:53760\n"
  "v_mfma_f32_16x16x32_bf16 v[12:15], v[204:207], a[88:91], v[12:15]\n"
  "buffer_load_dwordx4 v217, s[8:11], s43 offen offset:1024 lds\n"
  "v_mfma_f32_16x16x32_bf16 v[28:31], v[200:203], a[84:87], v[28:31]\n"
  "ds_read_b64_tr_b16 v[196:197], v228 offset:61440\n"
  "ds_read_b64_tr_b16 v[198:199], v228 offset:61952\n"
  "v_mfma_f32_16x16x32_bf16 v[28:31], v[204:207], a[92:95], v[28:31]\n"
  "v_mfma_f32_16x16x32_bf16 v[44:47], v[200:203], a[208:211], v[44:47]\n"
  "v_mfma_f32_16x16x32_bf16 v[44:47], v[204:207], a[216:219], v[44:47]\n"
  "buffer_load_dwordx4 v218, s[8:11], s43 offen offset:2048 lds\n"
  "v_mfma_f32_16x16x32_bf16 v[60:63], v[200:203], a[212:215], v[60:63]\n"
  "v_mfma_f32_16x16x32_bf16 v[60:63], v[204:207], a[220:223], v[60:63]\n"
  "s_waitcnt lgkmcnt(4)\n"
  "v_mfma_f32_16x16x32_bf16 v[64:67], v[208:211], a[80:83], v[64:67]\n"
  "ds_read_b64_tr_b16 v[200:201], v229 offset:49152\n"
  "ds_read_b64_tr_b16 v[202:203], v229 offset:49664\n"
  "v_mfma_f32_16x16x32_bf16 v[64:67], v[212:215], a[88:91], v[64:67]\n"
  "buffer_load_dwordx4 v219, s[8:11], s43 offen offset:3072 lds\n"
  "v_mfma_f32_16x16x32_bf16 v[80:83], v[208:211], a[84:87], v[80:83]\n"
  "ds_read_b64_tr_b16 v[204:205], v229 offset:57344\n"
  "ds_read_b64_tr_b16 v[206:207], v229 offset:57856\n"
  "v_mfma_f32_16x16x32_bf16 v[80:83], v[212:215], a[92:95], v[80:83]\n"
  "v_mfma_f32_16x16x32_bf16 v[96:99], v[208:211], a[208:211], v[96:99]\n"
  "s_add_u32 s26, s43, 1024\n"
  "s_add_u32 m0, s24, 49152\n"
  "v_mfma_f32_16x16x32_bf16 v[96:99], v[212:215], a[216:219], v[96:99]\n"
  "buffer_load_dwordx4 v216, s[8:11], s26 offen lds\n"
  "v_mfma_f32_16x16x32_bf16 v[112:115], v[208:211], a[212:215], v[112:115]\n"
  "v_mfma_f32_16x16x32_bf16 v[112:115], v[212:215], a[220:223], v[112:115]\n"
  "s_waitcnt vmcnt(13) lgkmcnt(4)\n"
  "s_barrier\n"
  "v_mfma_f32_16x16x32_bf16 v[72:75], v[192:195], a[80:83], v[72:75]\n"
  "ds_read_b64_tr_b16 v[208:209], v229 offset:53248\n"
  "ds_read_b64_tr_b16 v[210:211], v229 offset:53760\n"
  "v_mfma_f32_16x16x32_bf16 v[72:75], v[196:199], a[88:91], v[72:75]\n"
  "buffer_load_dwordx4 v217, s[8:11], s26 offen offset:1024 lds\n"
  "v_mfma_f32_16x16x32_bf16 v[88:91], v[192:195], a[84:87], v[88:91]\n"
  "ds_read_b64_tr_b16 v[212:213], v229 offset:61440\n"
  "ds_read_b64_tr_b16 v[214:215], v229 offset:61952\n"
  "v_mfma_f32_16x16x32_bf16 v[88:91], v[196:199], a[92:95], v[88:91]\n"
  "v_add_u32 v232, 0x10000, v232\n"
  "ds_read_b128 a[96:99], v232 offset:0\n"
  "v_mfma_f32_16x16x32_bf16 v[104:107], v[192:195], a[208:211], v[104:107]\n"
  "v_add_u32 v233, 0x10000, v233\n"
  "ds_read_b128 a[104:107], v233 offset:0\n"
  "v_mfma_f32_16x16x32_bf16 v[104:107], v[196:199], a[216:219], v[104:107]\n"
  "ds_read_b128 a[100:103], v232 offset:2048\n"
  "ds_read_b128 a[108:111], v233 offset:2048\n"
  "v_mfma_f32_16x16x32_bf16 v[120:123], v[192:195], a[212:215], v[120:123]\n"
  "buffer_load_dwordx4 v218, s[8:11], s26 offen offset:2048 lds\n"
  "v_mfma_f32_16x16x32_bf16 v[120:123], v[196:199], a[220:223], v[120:123]\n"
  "v_add_u32 v234, 0x10000, v234\n"
  "ds_read_b128 a[224:227], v234 offset:0\n"
  "s_waitcnt lgkmcnt(9)\n"
  "v_mfma_f32_16x16x32_bf16 v[68:71], v[200:203], a[80:83], v[68:71]\n"
  "v_add_u32 v235, 0x10000, v235\n"
  "ds_read_b128 a[232:235], v235 offset:0\n"
  "v_mfma_f32_16x16x32_bf16 v[68:71], v[204:207], a[88:91], v[68:71]\n"
  "ds_read_b128 a[228:231], v234 offset:2048\n"
  "ds_read_b128 a[236:239], v235 offset:2048\n"
  "v_mfma_f32_16x16x32_bf16 v[84:87], v[200:203], a[84:87], v[84:87]\n"
  "buffer_load_dwordx4 v219, s[8:11], s26 offen offset:3072 lds\n"
  "v_mfma_f32_16x16x32_bf16 v[84:87], v[204:207], a[92:95], v[84:87]\n"
  "v_add_u32 v228, 0x10000, v228\n"
  "ds_read_b64_tr_b16 v[192:193], v228 offset:16384\n"
  "v_mfma_f32_16x16x32_bf16 v[100:103], v[200:203], a[208:211], v[100:103]\n"
  "v_mfma_f32_16x16x32_bf16 v[100:103], v[204:207], a[216:219], v[100:103]\n"
  "ds_read_b64_tr_b16 v[194:195], v228 offset:16896\n"
  "ds_read_b64_tr_b16 v[196:197], v228 offset:24576\n"
  "v_mfma_f32_16x16x32_bf16 v[116:119], v[200:203], a[212:215], v[116:119]\n"
  "v_mfma_f32_16x16x32_bf16 v[116:119], v[204:207], a[220:223], v[116:119]\n"
  "ds_read_b64_tr_b16 v[198:199], v228 offset:25088\n"
  "s_waitcnt lgkmcnt(12)\n"
  "v_mfma_f32_16x16x32_bf16 v[76:79], v[208:211], a[80:83], v[76:79]\n"
  "s_add_u32 s26, s36, 2048\n"
  "s_add_u32 m0, s24, 65536\n"
  "v_mfma_f32_16x16x32_bf16 v[76:79], v[212:215], a[88:91], v[76:79]\n"
  "ds_read_b64_tr_b16 v[200:201], v228 offset:20480\n"
  "ds_read_b64_tr_b16 v[202:203], v228 offset:20992\n"
  "v_mfma_f32_16x16x32_bf16 v[92:95], v[208:211], a[84:87], v[92:95]\n"
  "buffer_load_dwordx4 v216, s[8:11], s26 offen lds\n"
  "v_mfma_f32_16x16x32_bf16 v[92:95], v[212:215], a[92:95], v[92:95]\n"
  "ds_read_b64_tr_b16 v[204:205], v228 offset:28672\n"
  "ds_read_b64_tr_b16 v[206:207], v228 offset:29184\n"
  "v_mfma_f32_16x16x32_bf16 v[108:111], v[208:211], a[208:211], v[108:111]\n"
  "v_mfma_f32_16x16x32_bf16 v[108:111], v[212:215], a[216:219], v[108:111]\n"
  "v_mfma_f32_16x16x32_bf16 v[124:127], v[208:211], a[212:215], v[124:127]\n"
  "FP2_%=:\n"
  "s_cbranch_execz FP6_%=\n"
  "buffer_load_dwordx4 v217, s[8:11], s26 offen offset:1024 lds\n"
  "v_mfma_f32_16x16x32_bf16 v[124:127], v[212:215], a[220:223], v[124:127]\n"
  "s_waitcnt lgkmcnt(4)\n"
  "v_mfma_f32_16x16x32_bf16 v[0:3], v[192:195], a[96:99], v[0:3]\n"
  "v_add_u32 v229, 0x10000, v229\n"
  "ds_read_b64_tr_b16 v[208:209], v229 offset:16384\n"
  "v_mfma_f32_16x16x32_bf16 v[0:3], v[196:199], a[104:107], v[0:3]\n"
  "v_mfma_f32_16x16x32_bf16 v[16:19], v[192:195], a[100:103], v[16:19]\n"
  "ds_read_b64_tr_b16 v[210:211], v229 offset:16896\n"
  "ds_read_b64_tr_b16 v[212:213], v229 offset:24576\n"
  "v_mfma_f32_16x16x32_bf16 v[16:19], v[196:199], a[108:111], v[16:19]\n"
  "buffer_load_dwordx4 v218, s[8:11], s26 offen offset:2048 lds\n"
  "v_mfma_f32_16x16x32_bf16 v[32:35], v[192:195], a[224:227], v[32:35]\n"
  "ds_read_b64_tr_b16 v[214:215], v229 offset:25088\n"
  "v_mfma_f32_16x16x32_bf16 v[32:35], v[196:199], a[232:235], v[32:35]\n"
  "v_mfma_f32_16x16x32_bf16 v[48:51], v[192:195], a[228:231], v[48:51]\n"
  "v_mfma_f32_16x16x32_bf16 v[48:51], v[196:199], a[236:239], v[48:51]\n"
  "buffer_load_dwordx4 v219, s[8:11], s26 offen offset:3072 lds\n"
  "s_waitcnt lgkmcnt(4)\n"
  "v_mfma_f32_16x16x32_bf16 v[8:11], v[200:203], a[96:99], v[8:11]\n"
  "ds_read_b64_tr_b16 v[192:193], v229 offset:20480\n"
  "ds_read_b64_tr_b16 v[194:195], v229 offset:20992\n"
  "v_mfma_f32_16x16x32_bf16 v[8:11], v[204:207], a[104:107], v[8:11]\n"
  "v_mfma_f32_16x16x32_bf16 v[24:27], v[200:203], a[100:103], v[24:27]\n"
  "ds_read_b64_tr_b16 v[196:197], v229 offset:28672\n"
  "ds_read_b64_tr_b16 v[198:199], v229 offset:29184\n"
  "v_mfma_f32_16x16x32_bf16 v[24:27], v[204:207], a[108:111], v[24:27]\n"
  "v_mfma_f32_16x16x32_bf16 v[40:43], v[200:203], a[224:227], v[40:43]\n"
  "v_mfma_f32_16x16x32_bf16 v[40:43], v[204:207], a[232:235], v[40:43]\n"
  "v_mfma_f32_16x16x32_bf16 v[56:59], v[200:203], a[228:231], v[56:59]\n"
  "v_mfma_f32_16x16x32_bf16 v[56:59], v[204:207], a[236:239], v[56:59]\n"
  "s_waitcnt vmcnt(16) lgkmcnt(4)\n"
  "s_barrier\n"
  "v_mfma_f32_16x16x32_bf16 v[4:7], v[208:211], a[96:99], v[4:7]\n"
  "v_add_u32 v228, 0xfffe0000, v228\n"
  "ds_read_b64_tr_b16 v[200:201], v228 offset:0\n"
  "v_mfma_f32_16x16x32_bf16 v[4:7], v[212:215], a[104:107], v[4:7]\n"
  "v_mfma_f32_16x16x32_bf16 v[20:23], v[208:211], a[100:103], v[20:23]\n"
  "ds_read_b64_tr_b16 v[202:203], v228 offset:512\n"
  "ds_read_b64_tr_b16 v[204:205], v228 offset:8192\n"
  "v_mfma_f32_16x16x32_bf16 v[20:23], v[212:215], a[108:111], v[20:23]\n"
  "v_mfma_f32_16x16x32_bf16 v[36:39], v[208:211], a[224:227], v[36:39]\n"
  "ds_read_b64_tr_b16 v[206:207], v228 offset:8704\n"
  "v_mfma_f32_16x16x32_bf16 v[36:39], v[212:215], a[232:235], v[36:39]\n"
  "v_mfma_f32_16x16x32_bf16 v[52:55], v[208:211], a[228:231], v[52:55]\n"
  "v_mfma_f32_16x16x32_bf16 v[52:55], v[212:215], a[236:239], v[52:55]\n"
  "s_waitcnt lgkmcnt(4)\n"
  "v_mfma_f32_16x16x32_bf16 v[12:15], v[192:195], a[96:99], v[12:15]\n"
  "ds_read_b64_tr_b16 v[208:209], v228 offset:4096\n"
  "ds_read_b64_tr_b16 v[210:211], v228 offset:4608\n"
  "v_mfma_f32_16x16x32_bf16 v[12:15], v[196:199], a[104:107], v[12:15]\n"
  "s_add_u32 s26, s37, 2048\n"
  "s_add_u32 m0, s24, 81920\n"
  "v_mfma_f32_16x16x32_bf16 v[28:31], v[192:195], a[100:103], v[28:31]\n"
  "ds_read_b64_tr_b16 v[212:213], v228 offset:12288\n"
  "ds_read_b64_tr_b16 v[214:215], v228 offset:12800\n"
  "v_mfma_f32_16x16x32_bf16 v[28:31], v[196:199], a[108:111], v[28:31]\n"
  "buffer_load_dwordx4 v216, s[8:11], s26 offen lds\n"
  "v_mfma_f32_16x16x32_bf16 v[44:47], v[192:195], a[224:227], v[44:47]\n"
  "v_mfma_f32_16x16x32_bf16 v[44:47], v[196:199], a[232:235], v[44:47]\n"
  "v_mfma_f32_16x16x32_bf16 v[60:63], v[192:195], a[228:231], v[60:63]\n"
  "v_mfma_f32_16x16x32_bf16 v[60:63], v[196:199], a[236:239], v[60:63]\n"
  "buffer_load_dwordx4 v217, s[8:11], s26 offen offset:1024 lds\n"
  "s_waitcnt lgkmcnt(4)\n"
  "v_mfma_f32_16x16x32_bf16 v[64:67], v[200:203], a[96:99], v[64:67]\n"
  "v_add_u32 v229, 0xfffe0000, v229\n"
  "ds_read_b64_tr_b16 v[192:193], v229 offset:0\n"
  "v_mfma_f32_16x16x32_bf16 v[64:67], v[204:207], a[104:107], v[64:67]\n"
  "v_mfma_f32_16x16x32_bf16 v[80:83], v[200:203], a[100:103], v[80:83]\n"
  "ds_read_b64_tr_b16 v[194:195], v229 offset:512\n"
  "ds_read_b64_tr_b16 v[196:197], v229 offset:8192\n"
  "v_mfma_f32_16x16x32_bf16 v[80:83], v[204:207], a[108:111], v[80:83]\n"
  "buffer_load_dwordx4 v218, s[8:11], s26 offen offset:2048 lds\n"
  "v_mfma_f32_16x16x32_bf16 v[96:99], v[200:203], a[224:227], v[96:99]\n"
  "ds_read_b64_tr_b16 v[198:199], v229 offset:8704\n"
  "v_mfma_f32_16x16x32_bf16 v[96:99], v[204:207], a[232:235], v[96:99]\n"
  "v_mfma_f32_16x16x32_bf16 v[112:115], v[200:203], a[228:231], v[112:115]\n"
  "v_mfma_f32_16x16x32_bf16 v[112:115], v[204:207], a[236:239], v[112:115]\n"
  "s_waitcnt vmcnt(11) lgkmcnt(4)\n"
  "s_barrier\n"
  "v_mfma_f32_16x16x32_bf16 v[72:75], v[208:211], a[96:99], v[72:75]\n"
  "ds_read_b64_tr_b16 v[200:201], v229 offset:4096\n"
  "ds_read_b64_tr_b16 v[202:203], v229 offset:4608\n"
  "v_mfma_f32_16x16x32_bf16 v[72:75], v[212:215], a[104:107], v[72:75]\n"
  "buffer_load_dwordx4 v219, s[8:11], s26 offen offset:3072 lds\n"
  "v_mfma_f32_16x16x32_bf16 v[88:91], v[208:211], a[100:103], v[88:91]\n"
  "ds_read_b64_tr_b16 v[204:205], v229 offset:12288\n"
  "ds_read_b64_tr_b16 v[206:207], v229 offset:12800\n"
  "v_mfma_f32_16x16x32_bf16 v[88:91], v[212:215], a[108:111], v[88:91]\n"
  "v_add_u32 v232, 0xfffe0000, v232\n"
  "ds_read_b128 a[112:115], v232 offset:16384\n"
  "v_mfma_f32_16x16x32_bf16 v[104:107], v[208:211], a[224:227], v[104:107]\n"
  "v_add_u32 v233, 0xfffe0000, v233\n"
  "ds_read_b128 a[120:123], v233 offset:16384\n"
  "v_mfma_f32_16x16x32_bf16 v[104:107], v[212:215], a[232:235], v[104:107]\n"
  "ds_read_b128 a[116:119], v232 offset:18432\n"
  "ds_read_b128 a[124:127], v233 offset:18432\n"
  "v_mfma_f32_16x16x32_bf16 v[120:123], v[208:211], a[228:231], v[120:123]\n"
  "v_mfma_f32_16x16x32_bf16 v[120:123], v[212:215], a[236:239], v[120:123]\n"
  "v_add_u32 v234, 0xfffe0000, v234\n"
  "ds_read_b128 a[240:243], v234 offset:16384\n"
  "s_waitcnt lgkmcnt(9)\n"
  "v_mfma_f32_16x16x32_bf16 v[68:71], v[192:195], a[96:99], v[68:71]\n"
  "v_add_u32 v235, 0xfffe0000, v235\n"
  "ds_read_b128 a[248:251], v235 offset:16384\n"
  "v_mfma_f32_16x16x32_bf16 v[68:71], v[196:199], a[104:107], v[68:71]\n"
  "ds_read_b128 a[244:247], v234 offset:18432\n"
  "ds_read_b128 a[252:255], v235 offset:18432\n"
  "v_mfma_f32_16x16x32_bf16 v[84:87], v[192:195], a[100:103], v[84:87]\n"
  "v_mfma_f32_16x16x32_bf16 v[84:87], v[196:199], a[108:111], v[84:87]\n"
  "ds_read_b64_tr_b16 v[208:209], v228 offset:32768\n"
  "ds_read_b64_tr_b16 v[210:211], v228 offset:33280\n"
  "v_mfma_f32_16x16x32_bf16 v[100:103], v[192:195], a[224:227], v[100:103]\n"
  "v_mfma_f32_16x16x32_bf16 v[100:103], v[196:199], a[232:235], v[100:103]\n"
  "ds_read_b64_tr_b16 v[212:213], v228 offset:40960\n"
  "ds_read_b64_tr_b16 v[214:215], v228 offset:41472\n"
  "v_mfma_f32_16x16x32_bf16 v[116:119], v[192:195], a[228:231], v[116:119]\n"
  "v_mfma_f32_16x16x32_bf16 v[116:119], v[196:199], a[236:239], v[116:119]\n"
  "s_waitcnt lgkmcnt(12)\n"
  "v_mfma_f32_16x16x32_bf16 v[76:79], v[200:203], a[96:99], v[76:79]\n"
  "ds_read_b64_tr_b16 v[192:193], v228 offset:36864\n"
  "ds_read_b64_tr_b16 v[194:195], v228 offset:37376\n"
  "v_mfma_f32_16x16x32_bf16 v[76:79], v[204:207], a[104:107], v[76:79]\n"
  "s_add_u32 s26, s38, 2048\n"
  "s_add_u32 m0, s24, 98304\n"
  "v_mfma_f32_16x16x32_bf16 v[92:95], v[200:203], a[100:103], v[92:95]\n"
  "ds_read_b64_tr_b16 v[196:197], v228 offset:45056\n"
  "ds_read_b64_tr_b16 v[198:199], v228 offset:45568\n"
  "v_mfma_f32_16x16x32_bf16 v[92:95], v[204:207], a[108:111], v[92:95]\n"
  "buffer_load_dwordx4 v216, s[8:11], s26 offen lds\n"
  "v_mfma_f32_16x16x32_bf16 v[108:111], v[200:203], a[224:227], v[108:111]\n"
  "v_mfma_f32_16x16x32_bf16 v[108:111], v[204:207], a[232:235], v[108:111]\n"
  "v_mfma_f32_16x16x32_bf16 v[124:127], v[200:203], a[228:231], v[124:127]\n"
  "v_mfma_f32_16x16x32_bf16 v[124:127], v[204:207], a[236:239], v[124:127]\n"
  "buffer_load_dwordx4 v217, s[8:11], s26 offen offset:1024 lds\n"
  "s_waitcnt lgkmcnt(4)\n"
  "v_mfma_f32_16x16x32_bf16 v[0:3], v[208:211], a[112:115], v[0:3]\n"
  "ds_read_b64_tr_b16 v[200:201], v229 offset:32768\n"
  "ds_read_b64_tr_b16 v[202:203], v229 offset:33280\n"
  "v_mfma_f32_16x16x32_bf16 v[0:3], v[212:215], a[120:123], v[0:3]\n"
  "v_xor_b32 v224, 0, v252\n"
  "v_xor_b32 v225, 32, v252\n"
  "v_mfma_f32_16x16x32_bf16 v[16:19], v[208:211], a[116:119], v[16:19]\n"
  "ds_read_b64_tr_b16 v[204:205], v229 offset:40960\n"
  "ds_read_b64_tr_b16 v[206:207], v229 offset:41472\n"
  "v_mfma_f32_16x16x32_bf16 v[16:19], v[212:215], a[124:127], v[16:19]\n"
  "buffer_load_dwordx4 v218, s[8:11], s26 offen offset:2048 lds\n"
  "v_xor_b32 v226, 64, v252\n"
  "v_mfma_f32_16x16x32_bf16 v[32:35], v[208:211], a[240:243], v[32:35]\n"
  "v_xor_b32 v227, 96, v252\n"
  "v_mfma_f32_16x16x32_bf16 v[32:35], v[212:215], a[248:251], v[32:35]\n"
  "v_mfma_f32_16x16x32_bf16 v[48:51], v[208:211], a[244:247], v[48:51]\n"
  "v_mfma_f32_16x16x32_bf16 v[48:51], v[212:215], a[252:255], v[48:51]\n"
  "buffer_load_dwordx4 v219, s[8:11], s26 offen offset:3072 lds\n"
  "s_waitcnt lgkmcnt(4)\n"
  "v_mfma_f32_16x16x32_bf16 v[8:11], v[192:195], a[112:115], v[8:11]\n"
  "ds_read_b64_tr_b16 v[208:209], v229 offset:36864\n"
  "ds_read_b64_tr_b16 v[210:211], v229 offset:37376\n"
  "v_mfma_f32_16x16x32_bf16 v[8:11], v[196:199], a[120:123], v[8:11]\n"
  "v_mfma_f32_16x16x32_bf16 v[24:27], v[192:195], a[116:119], v[24:27]\n"
  "ds_read_b64_tr_b16 v[212:213], v229 offset:45056\n"
  "ds_read_b64_tr_b16 v[214:215], v229 offset:45568\n"
  "v_mfma_f32_16x16x32_bf16 v[24:27], v[196:199], a[124:127], v[24:27]\n"
  "v_mfma_f32_16x16x32_bf16 v[40:43], v[192:195], a[240:243], v[40:43]\n"
  "v_mfma_f32_16x16x32_bf16 v[40:43], v[196:199], a[248:251], v[40:43]\n"
  "v_mfma_f32_16x16x32_bf16 v[56:59], v[192:195], a[244:247], v[56:59]\n"
  "v_mfma_f32_16x16x32_bf16 v[56:59], v[196:199], a[252:255], v[56:59]\n"
  "s_waitcnt vmcnt(12) lgkmcnt(4)\n"
  "s_barrier\n"
  "v_mfma_f32_16x16x32_bf16 v[4:7], v[200:203], a[112:115], v[4:7]\n"
  "ds_read_b64_tr_b16 v[192:193], v228 offset:49152\n"
  "ds_read_b64_tr_b16 v[194:195], v228 offset:49664\n"
  "v_mfma_f32_16x16x32_bf16 v[4:7], v[204:207], a[120:123], v[4:7]\n"
  "v_mfma_f32_16x16x32_bf16 v[20:23], v[200:203], a[116:119], v[20:23]\n"
  "ds_read_b64_tr_b16 v[196:197], v228 offset:57344\n"
  "ds_read_b64_tr_b16 v[198:199], v228 offset:57856\n"
  "v_mfma_f32_16x16x32_bf16 v[20:23], v[204:207], a[124:127], v[20:23]\n"
  "v_mfma_f32_16x16x32_bf16 v[36:39], v[200:203], a[240:243], v[36:39]\n"
  "v_mfma_f32_16x16x32_bf16 v[36:39], v[204:207], a[248:251], v[36:39]\n"
  "v_mfma_f32_16x16x32_bf16 v[52:55], v[200:203], a[244:247], v[52:55]\n"
  "v_mfma_f32_16x16x32_bf16 v[52:55], v[204:207], a[252:255], v[52:55]\n"
  "s_waitcnt lgkmcnt(4)\n"
  "v_mfma_f32_16x16x32_bf16 v[12:15], v[208:211], a[112:115], v[12:15]\n"
  "ds_read_b64_tr_b16 v[200:201], v228 offset:53248\n"
  "ds_read_b64_tr_b16 v[202:203], v228 offset:53760\n"
  "v_mfma_f32_16x16x32_bf16 v[12:15], v[212:215], a[120:123], v[12:15]\n"
  "s_add_u32 s26, s39, 2048\n"
  "s_add_u32 m0, s24, 114688\n"
  "v_mfma_f32_16x16x32_bf16 v[28:31], v[208:211], a[116:119], v[28:31]\n"
  "ds_read_b64_tr_b16 v[204:205], v228 offset:61440\n"
  "ds_read_b64_tr_b16 v[206:207], v228 offset:61952\n"
  "v_mfma_f32_16x16x32_bf16 v[28:31], v[212:215], a[124:127], v[28:31]\n"
  "buffer_load_dwordx4 v216, s[8:11], s26 offen lds\n"
  "v_mfma_f32_16x16x32_bf16 v[44:47], v[208:211], a[240:243], v[44:47]\n"
  "v_mfma_f32_16x16x32_bf16 v[44:47], v[212:215], a[248:251], v[44:47]\n"
  "v_pk_mul_f32 v[8:9], v[8:9], v[0:1]\n"
  "v_pk_mul_f32 v[10:11], v[10:11], v[2:3]\n"
  "v_pk_mul_f32 v[12:13], v[12:13], v[4:5]\n"
  "v_pk_mul_f32 v[14:15], v[14:15], v[6:7]\n"
  "v_pk_mul_f32 v[0:1], v[0:1], s[84:85]\n"
  "v_pk_mul_f32 v[2:3], v[2:3], s[84:85]\n"
  "v_pk_mul_f32 v[4:5], v[4:5], s[84:85]\n"
  "v_pk_mul_f32 v[6:7], v[6:7], s[84:85]\n"
  "v_mfma_f32_16x16x32_bf16 v[60:63], v[208:211], a[244:247], v[60:63]\n"
  "buffer_load_dwordx4 v217, s[8:11], s26 offen offset:1024 lds\n"
  "v_exp_f32 v0, v0\n"
  "v_mfma_f32_16x16x32_bf16 v[60:63], v[212:215], a[252:255], v[60:63]\n"
  "v_exp_f32 v1, v1\n"
  "s_waitcnt lgkmcnt(4)\n"
  "v_mfma_f32_16x16x32_bf16 v[64:67], v[192:195], a[112:115], v[64:67]\n"
  "ds_read_b64_tr_b16 v[208:209], v229 offset:49152\n"
  "ds_read_b64_tr_b16 v[210:211], v229 offset:49664\n"
  "v_mfma_f32_16x16x32_bf16 v[64:67], v[196:199], a[120:123], v[64:67]\n"
  "v_exp_f32 v2, v2\n"
  "v_mfma_f32_16x16x32_bf16 v[80:83], v[192:195], a[116:119], v[80:83]\n"
  "ds_read_b64_tr_b16 v[212:213], v229 offset:57344\n"
  "ds_read_b64_tr_b16 v[214:215], v229 offset:57856\n"
  "v_mfma_f32_16x16x32_bf16 v[80:83], v[196:199], a[124:127], v[80:83]\n"
  "buffer_load_dwordx4 v218, s[8:11], s26 offen offset:2048 lds\n"
  "v_exp_f32 v3, v3\n"
  "v_mfma_f32_16x16x32_bf16 v[96:99], v[192:195], a[240:243], v[96:99]\n"
  "v_exp_f32 v4, v4\n"
  "v_mfma_f32_16x16x32_bf16 v[96:99], v[196:199], a[248:251], v[96:99]\n"
  "v_exp_f32 v5, v5\n"
  "v_mfma_f32_16x16x32_bf16 v[112:115], v[192:195], a[244:247], v[112:115]\n"
  "v_exp_f32 v6, v6\n"
  "v_mfma_f32_16x16x32_bf16 v[112:115], v[196:199], a[252:255], v[112:115]\n"
  "buffer_load_dwordx4 v219, s[8:11], s26 offen offset:3072 lds\n"
  "v_exp_f32 v7, v7\n"
  "v_pk_mul_f32 v[24:25], v[24:25], v[16:17]\n"
  "v_pk_mul_f32 v[26:27], v[26:27], v[18:19]\n"
  "v_pk_mul_f32 v[28:29], v[28:29], v[20:21]\n"
  "v_pk_mul_f32 v[30:31], v[30:31], v[22:23]\n"
  "v_pk_add_f32 v[0:1], v[0:1], 1.0 op_sel_hi:[1,0]\n"
  "v_pk_add_f32 v[2:3], v[2:3], 1.0 op_sel_hi:[1,0]\n"
  "v_pk_add_f32 v[4:5], v[4:5], 1.0 op_sel_hi:[1,0]\n"
  "v_pk_add_f32 v[6:7], v[6:7], 1.0 op_sel_hi:[1,0]\n"
  "v_pk_mul_f32 v[16:17], v[16:17], s[84:85]\n"
  "v_pk_mul_f32 v[18:19], v[18:19], s[84:85]\n"
  "v_pk_mul_f32 v[20:21], v[20:21], s[84:85]\n"
  "v_pk_mul_f32 v[22:23], v[22:23], s[84:85]\n"
  "s_waitcnt lgkmcnt(4)\n"
  "v_mfma_f32_16x16x32_bf16 v[72:75], v[200:203], a[112:115], v[72:75]\n"
  "ds_read_b64_tr_b16 v[192:193], v229 offset:53248\n"
  "ds_read_b64_tr_b16 v[194:195], v229 offset:53760\n"
  "v_mfma_f32_16x16x32_bf16 v[72:75], v[204:207], a[120:123], v[72:75]\n"
  "v_rcp_f32 v0, v0\n"
  "v_mfma_f32_16x16x32_bf16 v[88:91], v[200:203], a[116:119], v[88:91]\n"
  "ds_read_b64_tr_b16 v[196:197], v229 offset:61440\n"
  "ds_read_b64_tr_b16 v[198:199], v229 offset:61952\n"
  "v_mfma_f32_16x16x32_bf16 v[88:91], v[204:207], a[124:127], v[88:91]\n"
  "v_exp_f32 v16, v16\n"
  "v_mfma_f32_16x16x32_bf16 v[104:107], v[200:203], a[240:243], v[104:107]\n"
  "v_rcp_f32 v1, v1\n"
  "v_mfma_f32_16x16x32_bf16 v[104:107], v[204:207], a[248:251], v[104:107]\n"
  "v_exp_f32 v17, v17\n"
  "v_mfma_f32_16x16x32_bf16 v[120:123], v[200:203], a[244:247], v[120:123]\n"
  "v_rcp_f32 v2, v2\n"
  "v_mfma_f32_16x16x32_bf16 v[120:123], v[204:207], a[252:255], v[120:123]\n"
  "s_waitcnt vmcnt(12) lgkmcnt(4)\n"
  "s_barrier\n"
  "v_mfma_f32_16x16x32_bf16 v[68:71], v[208:211], a[112:115], v[68:71]\n"
  "v_add_u32 v228, 0x10000, v228\n"
  "ds_read_b64_tr_b16 v[200:201], v228 offset:0\n"
  "v_mfma_f32_16x16x32_bf16 v[68:71], v[212:215], a[120:123], v[68:71]\n"
  "v_exp_f32 v18, v18\n"
  "v_mfma_f32_16x16x32_bf16 v[84:87], v[208:211], a[116:119], v[84:87]\n"
  "ds_read_b64_tr_b16 v[202:203], v228 offset:512\n"
  "ds_read_b64_tr_b16 v[204:205], v228 offset:8192\n"
  "v_mfma_f32_16x16x32_bf16 v[84:87], v[212:215], a[124:127], v[84:87]\n"
  "v_rcp_f32 v3, v3\n"
  "v_mfma_f32_16x16x32_bf16 v[100:103], v[208:211], a[240:243], v[100:103]\n"
  "ds_read_b64_tr_b16 v[206:207], v228 offset:8704\n"
  "buffer_load_dword v255, v241, s[12:15], 0 offen\n"
  "buffer_load_dword v255, v242, s[8:11], 0 offen\n"
  "v_mfma_f32_16x16x32_bf16 v[100:103], v[212:215], a[248:251], v[100:103]\n"
  "v_exp_f32 v19, v19\n"
  "v_mfma_f32_16x16x32_bf16 v[116:119], v[208:211], a[244:247], v[116:119]\n"
  "v_rcp_f32 v4, v4\n"
  "v_mfma_f32_16x16x32_bf16 v[116:119], v[212:215], a[252:255], v[116:119]\n"
  "v_exp_f32 v20, v20\n"
  "s_waitcnt lgkmcnt(4)\n"
  "v_mfma_f32_16x16x32_bf16 v[76:79], v[192:195], a[112:115], v[76:79]\n"
  "ds_read_b64_tr_b16 v[208:209], v228 offset:4096\n"
  "ds_read_b64_tr_b16 v[210:211], v228 offset:4608\n"
  "v_mfma_f32_16x16x32_bf16 v[76:79], v[196:199], a[120:123], v[76:79]\n"
  "s_add_u32 s26, s40, 2048\n"
  "s_add_u32 m0, s24, 131072\n"
  "v_mfma_f32_16x16x32_bf16 v[92:95], v[192:195], a[116:119], v[92:95]\n"
  "ds_read_b64_tr_b16 v[212:213], v228 offset:12288\n"
  "ds_read_b64_tr_b16 v[214:215], v228 offset:12800\n"
  "v_mfma_f32_16x16x32_bf16 v[92:95], v[196:199], a[124:127], v[92:95]\n"
  "buffer_load_dwordx4 v216, s[8:11], s26 offen lds\n"
  "v_rcp_f32 v5, v5\n"
  "v_mfma_f32_16x16x32_bf16 v[108:111], v[192:195], a[240:243], v[108:111]\n"
  "v_exp_f32 v21, v21\n"
  "v_mfma_f32_16x16x32_bf16 v[108:111], v[196:199], a[248:251], v[108:111]\n"
  "v_rcp_f32 v6, v6\n"
  "v_mfma_f32_16x16x32_bf16 v[124:127], v[192:195], a[244:247], v[124:127]\n"
  "v_exp_f32 v22, v22\n"
  "v_mfma_f32_16x16x32_bf16 v[124:127], v[196:199], a[252:255], v[124:127]\n"
  "buffer_load_dwordx4 v217, s[8:11], s26 offen offset:1024 lds\n"
  "s_waitcnt lgkmcnt(4)\n"
  "v_mfma_f32_16x16x32_bf16 v[128:131], v[200:203], a[0:3], 0\n"
  "v_add_u32 v229, 0x10000, v229\n"
  "ds_read_b64_tr_b16 v[192:193], v229 offset:0\n"
  "v_mfma_f32_16x16x32_bf16 v[128:131], v[204:207], a[8:11], v[128:131]\n"
  "v_rcp_f32 v7, v7\n"
  "v_exp_f32 v23, v23\n"
  "v_pk_mul_f32 v[8:9], v[8:9], v[0:1]\n"
  "v_pk_mul_f32 v[10:11], v[10:11], v[2:3]\n"
  "v_pk_mul_f32 v[12:13], v[12:13], v[4:5]\n"
  "v_pk_mul_f32 v[14:15], v[14:15], v[6:7]\n"
  "v_pk_mul_f32 v[40:41], v[40:41], v[32:33]\n"
  "v_pk_mul_f32 v[42:43], v[42:43], v[34:35]\n"
  "v_pk_mul_f32 v[44:45], v[44:45], v[36:37]\n"
  "v_pk_mul_f32 v[46:47], v[46:47], v[38:39]\n"
  "v_pk_add_f32 v[16:17], v[16:17], 1.0 op_sel_hi:[1,0]\n"
  "v_pk_add_f32 v[18:19], v[18:19], 1.0 op_sel_hi:[1,0]\n"
  "v_pk_add_f32 v[20:21], v[20:21], 1.0 op_sel_hi:[1,0]\n"
  "v_pk_add_f32 v[22:23], v[22:23], 1.0 op_sel_hi:[1,0]\n"
  "v_pk_mul_f32 v[32:33], v[32:33], s[84:85]\n"
  "v_pk_mul_f32 v[34:35], v[34:35], s[84:85]\n"
  "v_pk_mul_f32 v[36:37], v[36:37], s[84:85]\n"
  "v_pk_mul_f32 v[38:39], v[38:39], s[84:85]\n"
  "v_mfma_f32_16x16x32_bf16 v[144:147], v[200:203], a[4:7], 0\n"
  "ds_read_b64_tr_b16 v[194:195], v229 offset:512\n"
  "ds_read_b64_tr_b16 v[196:197], v229 offset:8192\n"
  "v_cvt_pk_bf16_f32 v0, v8, v9\n"
  "v_mfma_f32_16x16x32_bf16 v[144:147], v[204:207], a[12:15], v[144:147]\n"
  "buffer_load_dwordx4 v218, s[8:11], s26 offen offset:2048 lds\n"
  "v_rcp_f32 v16, v16\n"
  "v_mfma_f32_16x16x32_bf16 v[160:163], v[200:203], a[128:131], 0\n"
  "ds_read_b64_tr_b16 v[198:199], v229 offset:8704\n"
  "v_exp_f32 v32, v32\n"
  "v_mfma_f32_16x16x32_bf16 v[160:163], v[204:207], a[136:139], v[160:163]\n"
  "v_rcp_f32 v17, v17\n"
  "v_cvt_pk_bf16_f32 v1, v10, v11\n"
  "v_mfma_f32_16x16x32_bf16 v[176:179], v[200:203], a[132:135], 0\n"
  "v_exp_f32 v33, v33\n"
  "v_cvt_pk_bf16_f32 v2, v12, v13\n"
  "v_mfma_f32_16x16x32_bf16 v[176:179], v[204:207], a[140:143], v[176:179]\n"
  "v_rcp_f32 v18, v18\n"
  "s_waitcnt lgkmcnt(4)\n"
  "v_mfma_f32_16x16x32_bf16 v[136:139], v[208:211], a[0:3], 0\n"
  "ds_read_b64_tr_b16 v[200:201], v229 offset:4096\n"
  "ds_read_b64_tr_b16 v[202:203], v229 offset:4608\n"
  "v_cvt_pk_bf16_f32 v3, v14, v15\n"
  "v_mfma_f32_16x16x32_bf16 v[136:139], v[212:215], a[8:11], v[136:139]\n"
  "v_exp_f32 v34, v34\n"
  "v_mfma_f32_16x16x32_bf16 v[152:155], v[208:211], a[4:7], 0\n"
  "ds_read_b64_tr_b16 v[204:205], v229 offset:12288\n"
  "ds_read_b64_tr_b16 v[206:207], v229 offset:12800\n"
  "v_mfma_f32_16x16x32_bf16 v[152:155], v[212:215], a[12:15], v[152:155]\n"
  "v_rcp_f32 v19, v19\n"
  "s_add_u32 s29, s27, 0\n"
  "v_mfma_f32_16x16x32_bf16 v[168:171], v[208:211], a[128:131], 0\n"
  "v_exp_f32 v35, v35\n"
  "buffer_store_dwordx4 v[0:3], v244, s[16:19], s29 offen sc1\n"
  "v_mfma_f32_16x16x32_bf16 v[168:171], v[212:215], a[136:139], v[168:171]\n"
  "v_rcp_f32 v20, v20\n"
  "v_mfma_f32_16x16x32_bf16 v[184:187], v[208:211], a[132:135], 0\n"
  "v_exp_f32 v36, v36\n"
  "v_mfma_f32_16x16x32_bf16 v[184:187], v[212:215], a[140:143], v[184:187]\n"
  "s_waitcnt vmcnt(14) lgkmcnt(4)\n"
  "s_barrier\n"
  "v_mfma_f32_16x16x32_bf16 v[132:135], v[192:195], a[0:3], 0\n"
  "ds_read_b64_tr_b16 v[208:209], v228 offset:16384\n"
  "ds_read_b64_tr_b16 v[210:211], v228 offset:16896\n"
  "v_mfma_f32_16x16x32_bf16 v[132:135], v[196:199], a[8:11], v[132:135]\n"
  "v_rcp_f32 v21, v21\n"
  "v_mfma_f32_16x16x32_bf16 v[148:151], v[192:195], a[4:7], 0\n"
  "ds_read_b64_tr_b16 v[212:213], v228 offset:24576\n"
  "ds_read_b64_tr_b16 v[214:215], v228 offset:25088\n"
  "v_mfma_f32_16x16x32_bf16 v[148:151], v[196:199], a[12:15], v[148:151]\n"
  "v_exp_f32 v37, v37\n"
  "v_mfma_f32_16x16x32_bf16 v[164:167], v[192:195], a[128:131], 0\n"
  "v_rcp_f32 v22, v22\n"
  "v_mfma_f32_16x16x32_bf16 v[164:167], v[196:199], a[136:139], v[164:167]\n"
  "v_exp_f32 v38, v38\n"
  "v_mfma_f32_16x16x32_bf16 v[180:183], v[192:195], a[132:135], 0\n"
  "v_rcp_f32 v23, v23\n"
  "v_mfma_f32_16x16x32_bf16 v[180:183], v[196:199], a[140:143], v[180:183]\n"
  "v_exp_f32 v39, v39\n"
  "v_pk_mul_f32 v[24:25], v[24:25], v[16:17]\n"
  "v_pk_mul_f32 v[26:27], v[26:27], v[18:19]\n"
  "v_pk_mul_f32 v[28:29], v[28:29], v[20:21]\n"
  "v_pk_mul_f32 v[30:31], v[30:31], v[22:23]\n"
  "v_pk_mul_f32 v[56:57], v[56:57], v[48:49]\n"
  "v_pk_mul_f32 v[58:59], v[58:59], v[50:51]\n"
  "v_pk_mul_f32 v[60:61], v[60:61], v[52:53]\n"
  "v_pk_mul_f32 v[62:63], v[62:63], v[54:55]\n"
  "v_pk_add_f32 v[32:33], v[32:33], 1.0 op_sel_hi:[1,0]\n"
  "v_pk_add_f32 v[34:35], v[34:35], 1.0 op_sel_hi:[1,0]\n"
  "v_pk_add_f32 v[36:37], v[36:37], 1.0 op_sel_hi:[1,0]\n"
  "v_pk_add_f32 v[38:39], v[38:39], 1.0 op_sel_hi:[1,0]\n"
  "v_pk_mul_f32 v[48:49], v[48:49], s[84:85]\n"
  "v_pk_mul_f32 v[50:51], v[50:51], s[84:85]\n"
  "v_pk_mul_f32 v[52:53], v[52:53], s[84:85]\n"
  "v_pk_mul_f32 v[54:55], v[54:55], s[84:85]\n"
  "s_waitcnt lgkmcnt(4)\n"
  "v_mfma_f32_16x16x32_bf16 v[140:143], v[200:203], a[0:3], 0\n"
  "ds_read_b64_tr_b16 v[192:193], v228 offset:20480\n"
  "ds_read_b64_tr_b16 v[194:195], v228 offset:20992\n"
  "v_cvt_pk_bf16_f32 v16, v24, v25\n"
  "v_mfma_f32_16x16x32_bf16 v[140:143], v[204:207], a[8:11], v[140:143]\n"
  "v_rcp_f32 v32, v32\n"
  "v_cvt_pk_bf16_f32 v17, v26, v27\n"
  "v_mfma_f32_16x16x32_bf16 v[156:159], v[200:203], a[4:7], 0\n"
  "ds_read_b64_tr_b16 v[196:197], v228 offset:28672\n"
  "ds_read_b64_tr_b16 v[198:199], v228 offset:29184\n"
  "v_mfma_f32_16x16x32_bf16 v[156:159], v[204:207], a[12:15], v[156:159]\n"
  "v_exp_f32 v48, v48\n"
  "v_mfma_f32_16x16x32_bf16 v[172:175], v[200:203], a[128:131], 0\n"
  "v_rcp_f32 v33, v33\n"
  "v_cvt_pk_bf16_f32 v18, v28, v29\n"
  "v_mfma_f32_16x16x32_bf16 v[172:175], v[204:207], a[136:139], v[172:175]\n"
  "v_exp_f32 v49, v49\n"
  "v_mfma_f32_16x16x32_bf16 v[188:191], v[200:203], a[132:135], 0\n"
  "v_rcp_f32 v34, v34\n"
  "v_cvt_pk_bf16_f32 v19, v30, v31\n"
  "v_mfma_f32_16x16x32_bf16 v[188:191], v[204:207], a[140:143], v[188:191]\n"
  "v_exp_f32 v50, v50\n"
  "s_waitcnt lgkmcnt(4)\n"
  "v_mfma_f32_16x16x32_bf16 v[128:131], v[208:211], a[16:19], v[128:131]\n"
  "ds_read_b64_tr_b16 v[200:201], v229 offset:16384\n"
  "ds_read_b64_tr_b16 v[202:203], v229 offset:16896\n"
  "v_mfma_f32_16x16x32_bf16 v[128:131], v[212:215], a[24:27], v[128:131]\n"
  "v_rcp_f32 v35, v35\n"
  "buffer_store_dwordx4 v[16:19], v245, s[16:19], s29 offen sc1\n"
  "v_mfma_f32_16x16x32_bf16 v[144:147], v[208:211], a[20:23], v[144:147]\n"
  "ds_read_b64_tr_b16 v[204:205], v229 offset:24576\n"
  "ds_read_b64_tr_b16 v[206:207], v229 offset:25088\n"
  "v_mfma_f32_16x16x32_bf16 v[144:147], v[212:215], a[28:31], v[144:147]\n"
  "v_exp_f32 v51, v51\n"
  "v_mfma_f32_16x16x32_bf16 v[160:163], v[208:211], a[144:147], v[160:163]\n"
  "v_rcp_f32 v36, v36\n"
  "v_mfma_f32_16x16x32_bf16 v[160:163], v[212:215], a[152:155], v[160:163]\n"
  "v_exp_f32 v52, v52\n"
  "v_mfma_f32_16x16x32_bf16 v[176:179], v[208:211], a[148:151], v[176:179]\n"
  "v_rcp_f32 v37, v37\n"
  "v_mfma_f32_16x16x32_bf16 v[176:179], v[212:215], a[156:159], v[176:179]\n"
  "v_exp_f32 v53, v53\n"
  "s_waitcnt lgkmcnt(4)\n"
  "v_mfma_f32_16x16x32_bf16 v[136:139], v[192:195], a[16:19], v[136:139]\n"
  "ds_read_b64_tr_b16 v[208:209], v229 offset:20480\n"
  "ds_read_b64_tr_b16 v[210:211], v229 offset:20992\n"
  "v_mfma_f32_16x16x32_bf16 v[136:139], v[196:199], a[24:27], v[136:139]\n"
  "v_rcp_f32 v38, v38\n"
  "v_mfma_f32_16x16x32_bf16 v[152:155], v[192:195], a[20:23], v[152:155]\n"
  "ds_read_b64_tr_b16 v[212:213], v229 offset:28672\n"
  "ds_read_b64_tr_b16 v[214:215], v229 offset:29184\n"
  "v_mfma_f32_16x16x32_bf16 v[152:155], v[196:199], a[28:31], v[152:155]\n"
  "v_exp_f32 v54, v54\n"
  "v_mfma_f32_16x16x32_bf16 v[168:171], v[192:195], a[144:147], v[168:171]\n"
  "v_rcp_f32 v39, v39\n"
  "v_mfma_f32_16x16x32_bf16 v[168:171], v[196:199], a[152:155], v[168:171]\n"
  "v_exp_f32 v55, v55\n"
  "v_pk_mul_f32 v[40:41], v[40:41], v[32:33]\n"
  "v_pk_mul_f32 v[42:43], v[42:43], v[34:35]\n"
  "v_pk_mul_f32 v[44:45], v[44:45], v[36:37]\n"
  "v_pk_mul_f32 v[46:47], v[46:47], v[38:39]\n"
  "v_pk_add_f32 v[48:49], v[48:49], 1.0 op_sel_hi:[1,0]\n"
  "v_pk_add_f32 v[50:51], v[50:51], 1.0 op_sel_hi:[1,0]\n"
  "v_pk_add_f32 v[52:53], v[52:53], 1.0 op_sel_hi:[1,0]\n"
  "v_pk_add_f32 v[54:55], v[54:55], 1.0 op_sel_hi:[1,0]\n"
  "v_mfma_f32_16x16x32_bf16 v[184:187], v[192:195], a[148:151], v[184:187]\n"
  "v_rcp_f32 v48, v48\n"
  "v_cvt_pk_bf16_f32 v32, v40, v41\n"
  "v_mfma_f32_16x16x32_bf16 v[184:187], v[196:199], a[156:159], v[184:187]\n"
  "v_cvt_pk_bf16_f32 v33, v42, v43\n"
  "s_waitcnt vmcnt(11) lgkmcnt(4)\n"
  "s_barrier\n"
  "v_mfma_f32_16x16x32_bf16 v[132:135], v[200:203], a[16:19], v[132:135]\n"
  "ds_read_b64_tr_b16 v[192:193], v228 offset:32768\n"
  "ds_read_b64_tr_b16 v[194:195], v228 offset:33280\n"
  "v_cvt_pk_bf16_f32 v34, v44, v45\n"
  "v_mfma_f32_16x16x32_bf16 v[132:135], v[204:207], a[24:27], v[132:135]\n"
  "v_rcp_f32 v49, v49\n"
  "v_cvt_pk_bf16_f32 v35, v46, v47\n"
  "v_mfma_f32_16x16x32_bf16 v[148:151], v[200:203], a[20:23], v[148:151]\n"
  "ds_read_b64_tr_b16 v[196:197], v228 offset:40960\n"
  "ds_read_b64_tr_b16 v[198:199], v228 offset:41472\n"
  "v_mfma_f32_16x16x32_bf16 v[148:151], v[204:207], a[28:31], v[148:151]\n"
  "v_rcp_f32 v50, v50\n"
  "s_add_u32 s29, s28, 0\n"
  "v_mfma_f32_16x16x32_bf16 v[164:167], v[200:203], a[144:147], v[164:167]\n"
  "buffer_load_dwordx4 v219, s[8:11], s26 offen offset:3072 lds\n"
  "v_rcp_f32 v51, v51\n"
  "v_mfma_f32_16x16x32_bf16 v[164:167], v[204:207], a[152:155], v[164:167]\n"
  "v_rcp_f32 v52, v52\n"
  "buffer_store_dwordx4 v[32:35], v244, s[16:19], s29 offen sc1\n"
  "v_mfma_f32_16x16x32_bf16 v[180:183], v[200:203], a[148:151], v[180:183]\n"
  "v_rcp_f32 v53, v53\n"
  "v_mfma_f32_16x16x32_bf16 v[180:183], v[204:207], a[156:159], v[180:183]\n"
  "v_rcp_f32 v54, v54\n"
  "v_rcp_f32 v55, v55\n"
  "v_pk_mul_f32 v[56:57], v[56:57], v[48:49]\n"
  "v_pk_mul_f32 v[58:59], v[58:59], v[50:51]\n"
  "v_pk_mul_f32 v[60:61], v[60:61], v[52:53]\n"
  "v_pk_mul_f32 v[62:63], v[62:63], v[54:55]\n"
  "s_waitcnt lgkmcnt(4)\n"
  "v_mfma_f32_16x16x32_bf16 v[140:143], v[208:211], a[16:19], v[140:143]\n"
  "ds_read_b64_tr_b16 v[200:201], v228 offset:36864\n"
  "ds_read_b64_tr_b16 v[202:203], v228 offset:37376\n"
  "v_cvt_pk_bf16_f32 v48, v56, v57\n"
  "v_mfma_f32_16x16x32_bf16 v[140:143], v[212:215], a[24:27], v[140:143]\n"
  "s_add_u32 s26, s41, 2048\n"
  "s_add_u32 m0, s24, 147456\n"
  "v_cvt_pk_bf16_f32 v49, v58, v59\n"
  "v_mfma_f32_16x16x32_bf16 v[156:159], v[208:211], a[20:23], v[156:159]\n"
  "ds_read_b64_tr_b16 v[204:205], v228 offset:45056\n"
  "ds_read_b64_tr_b16 v[206:207], v228 offset:45568\n"
  "v_cvt_pk_bf16_f32 v50, v60, v61\n"
  "v_mfma_f32_16x16x32_bf16 v[156:159], v[212:215], a[28:31], v[156:159]\n"
  "buffer_load_dwordx4 v216, s[8:11], s26 offen lds\n"
  "v_cvt_pk_bf16_f32 v51, v62, v63\n"
  "buffer_store_dwordx4 v[48:51], v245, s[16:19], s29 offen sc1\n"
  "v_pk_mul_f32 v[72:73], v[72:73], v[64:65]\n"
  "v_pk_mul_f32 v[74:75], v[74:75], v[66:67]\n"
  "v_pk_mul_f32 v[76:77], v[76:77], v[68:69]\n"
  "v_pk_mul_f32 v[78:79], v[78:79], v[70:71]\n"
  "v_pk_mul_f32 v[64:65], v[64:65], s[84:85]\n"
  "v_pk_mul_f32 v[66:67], v[66:67], s[84:85]\n"
  "v_pk_mul_f32 v[68:69], v[68:69], s[84:85]\n"
  "v_pk_mul_f32 v[70:71], v[70:71], s[84:85]\n"
  "v_mfma_f32_16x16x32_bf16 v[172:175], v[208:211], a[144:147], v[172:175]\n"
  "v_exp_f32 v64, v64\n"
  "v_exp_f32 v65, v65\n"
  "v_mfma_f32_16x16x32_bf16 v[172:175], v[212:215], a[152:155], v[172:175]\n"
  "buffer_load_dwordx4 v217, s[8:11], s26 offen offset:1024 lds\n"
  "v_exp_f32 v66, v66\n"
  "v_mfma_f32_16x16x32_bf16 v[188:191], v[208:211], a[148:151], v[188:191]\n"
  "v_exp_f32 v67, v67\n"
  "v_exp_f32 v68, v68\n"
  "v_mfma_f32_16x16x32_bf16 v[188:191], v[212:215], a[156:159], v[188:191]\n"
  "v_exp_f32 v69, v69\n"
  "s_waitcnt lgkmcnt(4)\n"
  "v_mfma_f32_16x16x32_bf16 v[128:131], v[192:195], a[32:35], v[128:131]\n"
  "ds_read_b64_tr_b16 v[208:209], v229 offset:32768\n"
  "ds_read_b64_tr_b16 v[210:211], v229 offset:33280\n"
  "v_exp_f32 v70, v70\n"
  "v_mfma_f32_16x16x32_bf16 v[128:131], v[196:199], a[40:43], v[128:131]\n"
  "v_exp_f32 v71, v71\n"
  "v_pk_mul_f32 v[88:89], v[88:89], v[80:81]\n"
  "v_pk_mul_f32 v[90:91], v[90:91], v[82:83]\n"
  "v_pk_mul_f32 v[92:93], v[92:93], v[84:85]\n"
  "v_pk_mul_f32 v[94:95], v[94:95], v[86:87]\n"
  "v_pk_add_f32 v[64:65], v[64:65], 1.0 op_sel_hi:[1,0]\n"
  "v_pk_add_f32 v[66:67], v[66:67], 1.0 op_sel_hi:[1,0]\n"
  "v_pk_add_f32 v[68:69], v[68:69], 1.0 op_sel_hi:[1,0]\n"
  "v_pk_add_f32 v[70:71], v[70:71], 1.0 op_sel_hi:[1,0]\n"
  "v_pk_mul_f32 v[80:81], v[80:81], s[84:85]\n"
  "v_pk_mul_f32 v[82:83], v[82:83], s[84:85]\n"
  "v_pk_mul_f32 v[84:85], v[84:85], s[84:85]\n"
  "v_pk_mul_f32 v[86:87], v[86:87], s[84:85]\n"
  "v_mfma_f32_16x16x32_bf16 v[144:147], v[192:195], a[36:39], v[144:147]\n"
  "ds_read_b64_tr_b16 v[212:213], v229 offset:40960\n"
  "ds_read_b64_tr_b16 v[214:215], v229 offset:41472\n"
  "v_rcp_f32 v64, v64\n"
  "v_mfma_f32_16x16x32_bf16 v[144:147], v[196:199], a[44:47], v[144:147]\n"
  "buffer_load_dwordx4 v218, s[8:11], s26 offen offset:2048 lds\n"
  "v_exp_f32 v80, v80\n"
  "v_mfma_f32_16x16x32_bf16 v[160:163], v[192:195], a[160:163], v[160:163]\n"
  "v_rcp_f32 v65, v65\n"
  "v_exp_f32 v81, v81\n"
  "v_mfma_f32_16x16x32_bf16 v[160:163], v[196:199], a[168:171], v[160:163]\n"
  "FP3_%=:\n"
  "s_cbranch_execz FP7_%=\n"
  "v_rcp_f32 v66, v66\n"
  "v_exp_f32 v82, v82\n"
  "v_mfma_f32_16x16x32_bf16 v[176:179], v[192:195], a[164:167], v[176:179]\n"
  "v_rcp_f32 v67, v67\n"
  "v_exp_f32 v83, v83\n"
  "v_mfma_f32_16x16x32_bf16 v[176:179], v[196:199], a[172:175], v[176:179]\n"
  "buffer_load_dwordx4 v219, s[8:11], s26 offen offset:3072 lds\n"
  "v_rcp_f32 v68, v68\n"
  "s_waitcnt lgkmcnt(4)\n"
  "v_mfma_f32_16x16x32_bf16 v[136:139], v[200:203], a[32:35], v[136:139]\n"
  "ds_read_b64_tr_b16 v[192:193], v229 offset:36864\n"
  "ds_read_b64_tr_b16 v[194:195], v229 offset:37376\n"
  "v_exp_f32 v84, v84\n"
  "v_mfma_f32_16x16x32_bf16 v[136:139], v[204:207], a[40:43], v[136:139]\n"
  "v_rcp_f32 v69, v69\n"
  "v_exp_f32 v85, v85\n"
  "v_mfma_f32_16x16x32_bf16 v[152:155], v[200:203], a[36:39], v[152:155]\n"
  "ds_read_b64_tr_b16 v[196:197], v229 offset:45056\n"
  "ds_read_b64_tr_b16 v[198:199], v229 offset:45568\n"
  "v_rcp_f32 v70, v70\n"
  "v_mfma_f32_16x16x32_bf16 v[152:155], v[204:207], a[44:47], v[152:155]\n"
  "s_add_u32 s26, s42, 2048\n"
  "s_add_u32 m0, s24, 0\n"
  "v_exp_f32 v86, v86\n"
  "v_mfma_f32_16x16x32_bf16 v[168:171], v[200:203], a[160:163], v[168:171]\n"
  "buffer_load_dwordx4 v216, s[8:11], s26 offen lds\n"
  "v_rcp_f32 v71, v71\n"
  "v_mfma_f32_16x16x32_bf16 v[168:171], v[204:207], a[168:171], v[168:171]\n"
  "v_exp_f32 v87, v87\n"
  "v_pk_mul_f32 v[72:73], v[72:73], v[64:65]\n"
  "v_pk_mul_f32 v[74:75], v[74:75], v[66:67]\n"
  "v_pk_mul_f32 v[76:77], v[76:77], v[68:69]\n"
  "v_pk_mul_f32 v[78:79], v[78:79], v[70:71]\n"
  "v_pk_mul_f32 v[104:105], v[104:105], v[96:97]\n"
  "v_pk_mul_f32 v[106:107], v[106:107], v[98:99]\n"
  "v_pk_mul_f32 v[108:109], v[108:109], v[100:101]\n"
  "v_pk_mul_f32 v[110:111], v[110:111], v[102:103]\n"
  "v_pk_add_f32 v[80:81], v[80:81], 1.0 op_sel_hi:[1,0]\n"
  "v_pk_add_f32 v[82:83], v[82:83], 1.0 op_sel_hi:[1,0]\n"
  "v_pk_add_f32 v[84:85], v[84:85], 1.0 op_sel_hi:[1,0]\n"
  "v_pk_add_f32 v[86:87], v[86:87], 1.0 op_sel_hi:[1,0]\n"
  "v_pk_mul_f32 v[96:97], v[96:97], s[84:85]\n"
  "v_pk_mul_f32 v[98:99], v[98:99], s[84:85]\n"
  "v_pk_mul_f32 v[100:101], v[100:101], s[84:85]\n"
  "v_pk_mul_f32 v[102:103], v[102:103], s[84:85]\n"
  "v_mfma_f32_16x16x32_bf16 v[184:187], v[200:203], a[164:167], v[184:187]\n"
  "buffer_load_dwordx4 v217, s[8:11], s26 offen offset:1024 lds\n"
  "v_rcp_f32 v80, v80\n"
  "v_cvt_pk_bf16_f32 v64, v72, v73\n"
  "v_mfma_f32_16x16x32_bf16 v[184:187], v[204:207], a[172:175], v[184:187]\n"
  "v_exp_f32 v96, v96\n"
  "s_waitcnt vmcnt(16) lgkmcnt(4)\n"
  "s_barrier\n"
  "v_mfma_f32_16x16x32_bf16 v[132:135], v[208:211], a[32:35], v[132:135]\n"
  "ds_read_b64_tr_b16 v[200:201], v228 offset:49152\n"
  "ds_read_b64_tr_b16 v[202:203], v228 offset:49664\n"
  "v_rcp_f32 v81, v81\n"
  "v_mfma_f32_16x16x32_bf16 v[132:135], v[212:215], a[40:43], v[132:135]\n"
  "v_cvt_pk_bf16_f32 v65, v74, v75\n"
  "v_exp_f32 v97, v97\n"
  "v_cvt_pk_bf16_f32 v66, v76, v77\n"
  "v_mfma_f32_16x16x32_bf16 v[148:151], v[208:211], a[36:39], v[148:151]\n"
  "ds_read_b64_tr_b16 v[204:205], v228 offset:57344\n"
  "ds_read_b64_tr_b16 v[206:207], v228 offset:57856\n"
  "v_rcp_f32 v82, v82\n"
  "v_mfma_f32_16x16x32_bf16 v[148:151], v[212:215], a[44:47], v[148:151]\n"
  "buffer_load_dwordx4 v218, s[8:11], s26 offen offset:2048 lds\n"
  "v_exp_f32 v98, v98\n"
  "v_cvt_pk_bf16_f32 v67, v78, v79\n"
  "v_mfma_f32_16x16x32_bf16 v[164:167], v[208:211], a[160:163], v[164:167]\n"
  "v_rcp_f32 v83, v83\n"
  "v_exp_f32 v99, v99\n"
  "v_mfma_f32_16x16x32_bf16 v[164:167], v[212:215], a[168:171], v[164:167]\n"
  "v_rcp_f32 v84, v84\n"
  "s_add_u32 s29, s27, 16384\n"
  "buffer_store_dwordx4 v[64:67], v244, s[16:19], s29 offen sc1\n"
  "v_mfma_f32_16x16x32_bf16 v[180:183], v[208:211], a[164:167], v[180:183]\n"
  "v_exp_f32 v100, v100\n"
  "v_rcp_f32 v85, v85\n"
  "v_mfma_f32_16x16x32_bf16 v[180:183], v[212:215], a[172:175], v[180:183]\n"
  "v_exp_f32 v101, v101\n"
  "s_waitcnt lgkmcnt(4)\n"
  "v_mfma_f32_16x16x32_bf16 v[140:143], v[192:195], a[32:35], v[140:143]\n"
  "ds_read_b64_tr_b16 v[208:209], v228 offset:53248\n"
  "ds_read_b64_tr_b16 v[210:211], v228 offset:53760\n"
  "v_rcp_f32 v86, v86\n"
  "v_mfma_f32_16x16x32_bf16 v[140:143], v[196:199], a[40:43], v[140:143]\n"
  "buffer_load_dwordx4 v219, s[8:11], s26 offen offset:3072 lds\n"
  "v_exp_f32 v102, v102\n"
  "v_mfma_f32_16x16x32_bf16 v[156:159], v[192:195], a[36:39], v[156:159]\n"
  "ds_read_b64_tr_b16 v[212:213], v228 offset:61440\n"
  "ds_read_b64_tr_b16 v[214:215], v228 offset:61952\n"
  "v_rcp_f32 v87, v87\n"
  "v_mfma_f32_16x16x32_bf16 v[156:159], v[196:199], a[44:47], v[156:159]\n"
  "s_waitcnt vmcnt(8)\n"
  "buffer_store_dwordx2 v[250:251], v248, s[16:19], s30 offen sc1\n"
  "s_add_u32 s30, s30, 4096\n"
  "v_exp_f32 v103, v103\n"
  "v_pk_mul_f32 v[88:89], v[88:89], v[80:81]\n"
  "v_pk_mul_f32 v[90:91], v[90:91], v[82:83]\n"
  "v_pk_mul_f32 v[92:93], v[92:93], v[84:85]\n"
  "v_pk_mul_f32 v[94:95], v[94:95], v[86:87]\n"
  "v_pk_mul_f32 v[120:121], v[120:121], v[112:113]\n"
  "v_pk_mul_f32 v[122:123], v[122:123], v[114:115]\n"
  "v_pk_mul_f32 v[124:125], v[124:125], v[116:117]\n"
  "v_pk_mul_f32 v[126:127], v[126:127], v[118:119]\n"
  "v_pk_add_f32 v[96:97], v[96:97], 1.0 op_sel_hi:[1,0]\n"
  "v_pk_add_f32 v[98:99], v[98:99], 1.0 op_sel_hi:[1,0]\n"
  "v_pk_add_f32 v[100:101], v[100:101], 1.0 op_sel_hi:[1,0]\n"
  "v_pk_add_f32 v[102:103], v[102:103], 1.0 op_sel_hi:[1,0]\n"
  "v_pk_mul_f32 v[112:113], v[112:113], s[84:85]\n"
  "v_pk_mul_f32 v[114:115], v[114:115], s[84:85]\n"
  "v_pk_mul_f32 v[116:117], v[116:117], s[84:85]\n"
  "v_pk_mul_f32 v[118:119], v[118:119], s[84:85]\n"
  "v_mfma_f32_16x16x32_bf16 v[172:175], v[192:195], a[160:163], v[172:175]\n"
  "s_add_u32 s26, s43, 2048\n"
  "s_add_u32 m0, s24, 16384\n"
  "v_rcp_f32 v96, v96\n"
  "v_mfma_f32_16x16x32_bf16 v[172:175], v[196:199], a[168:171], v[172:175]\n"
  "buffer_load_dwordx4 v216, s[8:11], s26 offen lds\n"
  "v_cvt_pk_bf16_f32 v80, v88, v89\n"
  "v_exp_f32 v112, v112\n"
  "v_mfma_f32_16x16x32_bf16 v[188:191], v[192:195], a[164:167], v[188:191]\n"
  "v_rcp_f32 v97, v97\n"
  "v_cvt_pk_bf16_f32 v81, v90, v91\n"
  "v_cvt_pk_bf16_f32 v82, v92, v93\n"
  "v_mfma_f32_16x16x32_bf16 v[188:191], v[196:199], a[172:175], v[188:191]\n"
  "v_exp_f32 v113, v113\n"
  "s_waitcnt lgkmcnt(4)\n"
  "v_mfma_f32_16x16x32_bf16 v[128:131], v[200:203], a[48:51], v[128:131]\n"
  "ds_read_b64_tr_b16 v[192:193], v229 offset:49152\n"
  "ds_read_b64_tr_b16 v[194:195], v229 offset:49664\n"
  "v_rcp_f32 v98, v98\n"
  "v_mfma_f32_16x16x32_bf16 v[128:131], v[204:207], a[56:59], v[128:131]\n"
  "buffer_load_dwordx4 v217, s[8:11], s26 offen offset:1024 lds\n"
  "v_exp_f32 v114, v114\n"
  "v_cvt_pk_bf16_f32 v83, v94, v95\n"
  "v_mfma_f32_16x16x32_bf16 v[144:147], v[200:203], a[52:55], v[144:147]\n"
  "ds_read_b64_tr_b16 v[196:197], v229 offset:57344\n"
  "ds_read_b64_tr_b16 v[198:199], v229 offset:57856\n"
  "v_rcp_f32 v99, v99\n"
  "v_mfma_f32_16x16x32_bf16 v[144:147], v[204:207], a[60:63], v[144:147]\n"
  "v_exp_f32 v115, v115\n"
  "v_rcp_f32 v100, v100\n"
  "v_mfma_f32_16x16x32_bf16 v[160:163], v[200:203], a[176:179], v[160:163]\n"
  "buffer_store_dwordx4 v[80:83], v245, s[16:19], s29 offen sc1\n"
  "v_exp_f32 v116, v116\n"
  "v_mfma_f32_16x16x32_bf16 v[160:163], v[204:207], a[184:187], v[160:163]\n"
  "v_rcp_f32 v101, v101\n"
  "v_exp_f32 v117, v117\n"
  "v_mfma_f32_16x16x32_bf16 v[176:179], v[200:203], a[180:183], v[176:179]\n"
  "v_rcp_f32 v102, v102\n"
  "v_exp_f32 v118, v118\n"
  "v_mfma_f32_16x16x32_bf16 v[176:179], v[204:207], a[188:191], v[176:179]\n"
  "v_rcp_f32 v103, v103\n"
  "v_exp_f32 v119, v119\n"
  "v_pk_mul_f32 v[104:105], v[104:105], v[96:97]\n"
  "v_pk_mul_f32 v[106:107], v[106:107], v[98:99]\n"
  "v_pk_mul_f32 v[108:109], v[108:109], v[100:101]\n"
  "v_pk_mul_f32 v[110:111], v[110:111], v[102:103]\n"
  "v_pk_add_f32 v[112:113], v[112:113], 1.0 op_sel_hi:[1,0]\n"
  "v_pk_add_f32 v[114:115], v[114:115], 1.0 op_sel_hi:[1,0]\n"
  "v_pk_add_f32 v[116:117], v[116:117], 1.0 op_sel_hi:[1,0]\n"
  "v_pk_add_f32 v[118:119], v[118:119], 1.0 op_sel_hi:[1,0]\n"
  "s_waitcnt lgkmcnt(4)\n"
  "v_mfma_f32_16x16x32_bf16 v[136:139], v[208:211], a[48:51], v[136:139]\n"
  "ds_read_b64_tr_b16 v[200:201], v229 offset:53248\n"
  "ds_read_b64_tr_b16 v[202:203], v229 offset:53760\n"
  "v_rcp_f32 v112, v112\n"
  "v_mfma_f32_16x16x32_bf16 v[136:139], v[212:215], a[56:59], v[136:139]\n"
  "buffer_load_dwordx4 v218, s[8:11], s26 offen offset:2048 lds\n"
  "v_cvt_pk_bf16_f32 v96, v104, v105\n"
  "v_rcp_f32 v113, v113\n"
  "v_mfma_f32_16x16x32_bf16 v[152:155], v[208:211], a[52:55], v[152:155]\n"
  "ds_read_b64_tr_b16 v[204:205], v229 offset:61440\n"
  "ds_read_b64_tr_b16 v[206:207], v229 offset:61952\n"
  "v_cvt_pk_bf16_f32 v97, v106, v107\n"
  "v_cvt_pk_bf16_f32 v98, v108, v109\n"
  "v_mfma_f32_16x16x32_bf16 v[152:155], v[212:215], a[60:63], v[152:155]\n"
  "v_rcp_f32 v114, v114\n"
  "v_rcp_f32 v115, v115\n"
  "v_mfma_f32_16x16x32_bf16 v[168:171], v[208:211], a[176:179], v[168:171]\n"
  "v_cvt_pk_bf16_f32 v99, v110, v111\n"
  "v_rcp_f32 v116, v116\n"
  "s_add_u32 s29, s28, 16384\n"
  "v_mfma_f32_16x16x32_bf16 v[168:171], v[212:215], a[184:187], v[168:171]\n"
  "buffer_load_dwordx4 v219, s[8:11], s26 offen offset:3072 lds\n"
  "buffer_store_dwordx4 v[96:99], v244, s[16:19], s29 offen sc1\n"
  "v_rcp_f32 v117, v117\n"
  "v_mfma_f32_16x16x32_bf16 v[184:187], v[208:211], a[180:183], v[184:187]\n"
  "v_rcp_f32 v118, v118\n"
  "v_rcp_f32 v119, v119\n"
  "v_mfma_f32_16x16x32_bf16 v[184:187], v[212:215], a[188:191], v[184:187]\n"
  "v_pk_mul_f32 v[120:121], v[120:121], v[112:113]\n"
  "v_pk_mul_f32 v[122:123], v[122:123], v[114:115]\n"
  "v_pk_mul_f32 v[124:125], v[124:125], v[116:117]\n"
  "v_pk_mul_f32 v[126:127], v[126:127], v[118:119]\n"
  "s_waitcnt lgkmcnt(4)\n"
  "s_barrier\n"
  "v_mfma_f32_16x16x32_bf16 v[132:135], v[192:195], a[48:51], v[132:135]\n"
  "v_add_u32 v228, 0x10000, v228\n"
  "ds_read_b64_tr_b16 v[208:209], v228 offset:0\n"
  "v_cvt_pk_bf16_f32 v112, v120, v121\n"
  "v_cvt_pk_bf16_f32 v113, v122, v123\n"
  "v_mfma_f32_16x16x32_bf16 v[132:135], v[196:199], a[56:59], v[132:135]\n"
  "s_add_u32 m0, s24, 32768\n"
  "v_cvt_pk_bf16_f32 v114, v124, v125\n"
  "v_cvt_pk_bf16_f32 v115, v126, v127\n"
  "buffer_store_dwordx4 v[112:115], v245, s[16:19], s29 offen sc1\n"
  "v_mfma_f32_16x16x32_bf16 v[148:151], v[192:195], a[52:55], v[148:151]\n"
  "ds_read_b64_tr_b16 v[210:211], v228 offset:512\n"
  "ds_read_b64_tr_b16 v[212:213], v228 offset:8192\n"
  "v_mfma_f32_16x16x32_bf16 v[148:151], v[196:199], a[60:63], v[148:151]\n"
  "buffer_load_dwordx4 v220, s[8:11], s52 offen lds\n"
  "v_mfma_f32_16x16x32_bf16 v[164:167], v[192:195], a[176:179], v[164:167]\n"
  "ds_read_b64_tr_b16 v[214:215], v228 offset:8704\n"
  "v_mfma_f32_16x16x32_bf16 v[164:167], v[196:199], a[184:187], v[164:167]\n"
  "v_mfma_f32_16x16x32_bf16 v[180:183], v[192:195], a[180:183], v[180:183]\n"
  "v_mfma_f32_16x16x32_bf16 v[180:183], v[196:199], a[188:191], v[180:183]\n"
  "s_waitcnt lgkmcnt(4)\n"
  "v_mfma_f32_16x16x32_bf16 v[140:143], v[200:203], a[48:51], v[140:143]\n"
  "ds_read_b64_tr_b16 v[192:193], v228 offset:4096\n"
  "ds_read_b64_tr_b16 v[194:195], v228 offset:4608\n"
  "v_mfma_f32_16x16x32_bf16 v[140:143], v[204:207], a[56:59], v[140:143]\n"
  "buffer_load_dwordx4 v221, s[8:11], s52 offen offset:1024 lds\n"
  "v_mfma_f32_16x16x32_bf16 v[156:159], v[200:203], a[52:55], v[156:159]\n"
  "ds_read_b64_tr_b16 v[196:197], v228 offset:12288\n"
  "ds_read_b64_tr_b16 v[198:199], v228 offset:12800\n"
  "v_mfma_f32_16x16x32_bf16 v[156:159], v[204:207], a[60:63], v[156:159]\n"
  "v_mfma_f32_16x16x32_bf16 v[172:175], v[200:203], a[176:179], v[172:175]\n"
  "v_mfma_f32_16x16x32_bf16 v[172:175], v[204:207], a[184:187], v[172:175]\n"
  "v_mfma_f32_16x16x32_bf16 v[188:191], v[200:203], a[180:183], v[188:191]\n"
  "v_mfma_f32_16x16x32_bf16 v[188:191], v[204:207], a[188:191], v[188:191]\n"
  "buffer_load_dwordx4 v222, s[8:11], s52 offen offset:2048 lds\n"
  "s_waitcnt lgkmcnt(4)\n"
  "v_mfma_f32_16x16x32_bf16 v[128:131], v[208:211], a[64:67], v[128:131]\n"
  "v_add_u32 v229, 0x10000, v229\n"
  "ds_read_b64_tr_b16 v[200:201], v229 offset:0\n"
  "v_mfma_f32_16x16x32_bf16 v[128:131], v[212:215], a[72:75], v[128:131]\n"
  "v_mfma_f32_16x16x32_bf16 v[144:147], v[208:211], a[68:71], v[144:147]\n"
  "ds_read_b64_tr_b16 v[202:203], v229 offset:512\n"
  "ds_read_b64_tr_b16 v[204:205], v229 offset:8192\n"
  "v_mfma_f32_16x16x32_bf16 v[144:147], v[212:215], a[76:79], v[144:147]\n"
  "v_mfma_f32_16x16x32_bf16 v[160:163], v[208:211], a[192:195], v[160:163]\n"
  "ds_read_b64_tr_b16 v[206:207], v229 offset:8704\n"
  "v_mfma_f32_16x16x32_bf16 v[160:163], v[212:215], a[200:203], v[160:163]\n"
  "buffer_load_dwordx4 v223, s[8:11], s52 offen offset:3072 lds\n"
  "v_mfma_f32_16x16x32_bf16 v[176:179], v[208:211], a[196:199], v[176:179]\n"
  "v_mfma_f32_16x16x32_bf16 v[176:179], v[212:215], a[204:207], v[176:179]\n"
  "s_waitcnt lgkmcnt(4)\n"
  "v_mfma_f32_16x16x32_bf16 v[136:139], v[192:195], a[64:67], v[136:139]\n"
  "ds_read_b64_tr_b16 v[208:209], v229 offset:4096\n"
  "ds_read_b64_tr_b16 v[210:211], v229 offset:4608\n"
  "v_mfma_f32_16x16x32_bf16 v[136:139], v[196:199], a[72:75], v[136:139]\n"
  "v_mfma_f32_16x16x32_bf16 v[152:155], v[192:195], a[68:71], v[152:155]\n"
  "ds_read_b64_tr_b16 v[212:213], v229 offset:12288\n"
  "ds_read_b64_tr_b16 v[214:215], v229 offset:12800\n"
  "v_mfma_f32_16x16x32_bf16 v[152:155], v[196:199], a[76:79], v[152:155]\n"
  "s_add_u32 m0, s24, 49152\n"
  "v_mfma_f32_16x16x32_bf16 v[168:171], v[192:195], a[192:195], v[168:171]\n"
  "buffer_load_dwordx4 v220, s[8:11], s53 offen lds\n"
  "v_mfma_f32_16x16x32_bf16 v[168:171], v[196:199], a[200:203], v[168:171]\n"
  "v_mfma_f32_16x16x32_bf16 v[184:187], v[192:195], a[196:199], v[184:187]\n"
  "v_mfma_f32_16x16x32_bf16 v[184:187], v[196:199], a[204:207], v[184:187]\n"
  "s_waitcnt vmcnt(18) lgkmcnt(4)\n"
  "s_barrier\n"
  "v_mfma_f32_16x16x32_bf16 v[132:135], v[200:203], a[64:67], v[132:135]\n"
  "ds_read_b64_tr_b16 v[192:193], v228 offset:16384\n"
  "ds_read_b64_tr_b16 v[194:195], v228 offset:16896\n"
  "v_mfma_f32_16x16x32_bf16 v[132:135], v[204:207], a[72:75], v[132:135]\n"
  "v_mfma_f32_16x16x32_bf16 v[148:151], v[200:203], a[68:71], v[148:151]\n"
  "ds_read_b64_tr_b16 v[196:197], v228 offset:24576\n"
  "ds_read_b64_tr_b16 v[198:199], v228 offset:25088\n"
  "v_mfma_f32_16x16x32_bf16 v[148:151], v[204:207], a[76:79], v[148:151]\n"
  "buffer_load_dwordx4 v221, s[8:11], s53 offen offset:1024 lds\n"
  "v_mfma_f32_16x16x32_bf16 v[164:167], v[200:203], a[192:195], v[164:167]\n"
  "v_mfma_f32_16x16x32_bf16 v[164:167], v[204:207], a[200:203], v[164:167]\n"
  "v_mfma_f32_16x16x32_bf16 v[180:183], v[200:203], a[196:199], v[180:183]\n"
  "v_mfma_f32_16x16x32_bf16 v[180:183], v[204:207], a[204:207], v[180:183]\n"
  "s_waitcnt lgkmcnt(4)\n"
  "v_mfma_f32_16x16x32_bf16 v[140:143], v[208:211], a[64:67], v[140:143]\n"
  "ds_read_b64_tr_b16 v[200:201], v228 offset:20480\n"
  "ds_read_b64_tr_b16 v[202:203], v228 offset:20992\n"
  "v_mfma_f32_16x16x32_bf16 v[140:143], v[212:215], a[72:75], v[140:143]\n"
  "buffer_load_dwordx4 v222, s[8:11], s53 offen offset:2048 lds\n"
  "v_mfma_f32_16x16x32_bf16 v[156:159], v[208:211], a[68:71], v[156:159]\n"
  "ds_read_b64_tr_b16 v[204:205], v228 offset:28672\n"
  "ds_read_b64_tr_b16 v[206:207], v228 offset:29184\n"
  "v_mfma_f32_16x16x32_bf16 v[156:159], v[212:215], a[76:79], v[156:159]\n"
  "v_mfma_f32_16x16x32_bf16 v[172:175], v[208:211], a[192:195], v[172:175]\n"
  "v_mfma_f32_16x16x32_bf16 v[172:175], v[212:215], a[200:203], v[172:175]\n"
  "v_mfma_f32_16x16x32_bf16 v[188:191], v[208:211], a[196:199], v[188:191]\n"
  "v_mfma_f32_16x16x32_bf16 v[188:191], v[212:215], a[204:207], v[188:191]\n"
  "buffer_load_dwordx4 v223, s[8:11], s53 offen offset:3072 lds\n"
  "s_waitcnt lgkmcnt(4)\n"
  "v_mfma_f32_16x16x32_bf16 v[128:131], v[192:195], a[80:83], v[128:131]\n"
  "ds_read_b64_tr_b16 v[208:209], v229 offset:16384\n"
  "ds_read_b64_tr_b16 v[210:211], v229 offset:16896\n"
  "v_mfma_f32_16x16x32_bf16 v[128:131], v[196:199], a[88:91], v[128:131]\n"
  "v_mfma_f32_16x16x32_bf16 v[144:147], v[192:195], a[84:87], v[144:147]\n"
  "ds_read_b64_tr_b16 v[212:213], v229 offset:24576\n"
  "ds_read_b64_tr_b16 v[214:215], v229 offset:25088\n"
  "v_mfma_f32_16x16x32_bf16 v[144:147], v[196:199], a[92:95], v[144:147]\n"
  "v_mfma_f32_16x16x32_bf16 v[160:163], v[192:195], a[208:211], v[160:163]\n"
  "s_add_u32 m0, s24, 65536\n"
  "v_mfma_f32_16x16x32_bf16 v[160:163], v[196:199], a[216:219], v[160:163]\n"
  "buffer_load_dwordx4 v220, s[8:11], s54 offen lds\n"
  "v_mfma_f32_16x16x32_bf16 v[176:179], v[192:195], a[212:215], v[176:179]\n"
  "v_mfma_f32_16x16x32_bf16 v[176:179], v[196:199], a[220:223], v[176:179]\n"
  "s_waitcnt lgkmcnt(4)\n"
  "v_mfma_f32_16x16x32_bf16 v[136:139], v[200:203], a[80:83], v[136:139]\n"
  "ds_read_b64_tr_b16 v[192:193], v229 offset:20480\n"
  "ds_read_b64_tr_b16 v[194:195], v229 offset:20992\n"
  "v_mfma_f32_16x16x32_bf16 v[136:139], v[204:207], a[88:91], v[136:139]\n"
  "s_waitcnt vmcnt(9)\n"
  "buffer_store_dwordx2 v[250:251], v248, s[16:19], s30 offen sc1\n"
  "s_add_u32 s30, s30, 4096\n"
  "v_mfma_f32_16x16x32_bf16 v[152:155], v[200:203], a[84:87], v[152:155]\n"
  "ds_read_b64_tr_b16 v[196:197], v229 offset:28672\n"
  "ds_read_b64_tr_b16 v[198:199], v229 offset:29184\n"
  "v_mfma_f32_16x16x32_bf16 v[152:155], v[204:207], a[92:95], v[152:155]\n"
  "v_mfma_f32_16x16x32_bf16 v[168:171], v[200:203], a[208:211], v[168:171]\n"
  "v_mfma_f32_16x16x32_bf16 v[168:171], v[204:207], a[216:219], v[168:171]\n"
  "v_mfma_f32_16x16x32_bf16 v[184:187], v[200:203], a[212:215], v[184:187]\n"
  "v_mfma_f32_16x16x32_bf16 v[184:187], v[204:207], a[220:223], v[184:187]\n"
  "s_waitcnt lgkmcnt(4)\n"
  "s_barrier\n"
  "v_mfma_f32_16x16x32_bf16 v[132:135], v[208:211], a[80:83], v[132:135]\n"
  "v_add_u32 v228, 0xfffe0000, v228\n"
  "ds_read_b64_tr_b16 v[200:201], v228 offset:0\n"
  "v_mfma_f32_16x16x32_bf16 v[132:135], v[212:215], a[88:91], v[132:135]\n"
  "buffer_load_dwordx4 v221, s[8:11], s54 offen offset:1024 lds\n"
  "v_mfma_f32_16x16x32_bf16 v[148:151], v[208:211], a[84:87], v[148:151]\n"
  "ds_read_b64_tr_b16 v[202:203], v228 offset:512\n"
  "ds_read_b64_tr_b16 v[204:205], v228 offset:8192\n"
  "v_mfma_f32_16x16x32_bf16 v[148:151], v[212:215], a[92:95], v[148:151]\n"
  "v_mfma_f32_16x16x32_bf16 v[164:167], v[208:211], a[208:211], v[164:167]\n"
  "ds_read_b64_tr_b16 v[206:207], v228 offset:8704\n"
  "v_mfma_f32_16x16x32_bf16 v[164:167], v[212:215], a[216:219], v[164:167]\n"
  "v_mfma_f32_16x16x32_bf16 v[180:183], v[208:211], a[212:215], v[180:183]\n"
  "v_mfma_f32_16x16x32_bf16 v[180:183], v[212:215], a[220:223], v[180:183]\n"
  "buffer_load_dwordx4 v222, s[8:11], s54 offen offset:2048 lds\n"
  "s_waitcnt lgkmcnt(4)\n"
  "v_mfma_f32_16x16x32_bf16 v[140:143], v[192:195], a[80:83], v[140:143]\n"
  "ds_read_b64_tr_b16 v[208:209], v228 offset:4096\n"
  "ds_read_b64_tr_b16 v[210:211], v228 offset:4608\n"
  "v_mfma_f32_16x16x32_bf16 v[140:143], v[196:199], a[88:91], v[140:143]\n"
  "v_mfma_f32_16x16x32_bf16 v[156:159], v[192:195], a[84:87], v[156:159]\n"
  "ds_read_b64_tr_b16 v[212:213], v228 offset:12288\n"
  "ds_read_b64_tr_b16 v[214:215], v228 offset:12800\n"
  "v_mfma_f32_16x16x32_bf16 v[156:159], v[196:199], a[92:95], v[156:159]\n"
  "v_mfma_f32_16x16x32_bf16 v[172:175], v[192:195], a[208:211], v[172:175]\n"
  "v_mfma_f32_16x16x32_bf16 v[172:175], v[196:199], a[216:219], v[172:175]\n"
  "buffer_load_dwordx4 v223, s[8:11], s54 offen offset:3072 lds\n"
  "v_mfma_f32_16x16x32_bf16 v[188:191], v[192:195], a[212:215], v[188:191]\n"
  "v_mfma_f32_16x16x32_bf16 v[188:191], v[196:199], a[220:223], v[188:191]\n"
  "s_waitcnt lgkmcnt(4)\n"
  "v_mfma_f32_16x16x32_bf16 v[128:131], v[200:203], a[96:99], v[128:131]\n"
  "v_add_u32 v229, 0xfffe0000, v229\n"
  "ds_read_b64_tr_b16 v[192:193], v229 offset:0\n"
  "v_mfma_f32_16x16x32_bf16 v[128:131], v[204:207], a[104:107], v[128:131]\n"
  "v_mfma_f32_16x16x32_bf16 v[144:147], v[200:203], a[100:103], v[144:147]\n"
  "ds_read_b64_tr_b16 v[194:195], v229 offset:512\n"
  "ds_read_b64_tr_b16 v[196:197], v229 offset:8192\n"
  "v_mfma_f32_16x16x32_bf16 v[144:147], v[204:207], a[108:111], v[144:147]\n"
  "s_add_u32 m0, s24, 81920\n"
  "v_mfma_f32_16x16x32_bf16 v[160:163], v[200:203], a[224:227], v[160:163]\n"
  "ds_read_b64_tr_b16 v[198:199], v229 offset:8704\n"
  "v_mfma_f32_16x16x32_bf16 v[160:163], v[204:207], a[232:235], v[160:163]\n"
  "buffer_load_dwordx4 v220, s[8:11], s55 offen lds\n"
  "v_mfma_f32_16x16x32_bf16 v[176:179], v[200:203], a[228:231], v[176:179]\n"
  "v_mfma_f32_16x16x32_bf16 v[176:179], v[204:207], a[236:239], v[176:179]\n"
  "s_waitcnt lgkmcnt(4)\n"
  "v_mfma_f32_16x16x32_bf16 v[136:139], v[208:211], a[96:99], v[136:139]\n"
  "ds_read_b64_tr_b16 v[200:201], v229 offset:4096\n"
  "ds_read_b64_tr_b16 v[202:203], v229 offset:4608\n"
  "v_mfma_f32_16x16x32_bf16 v[136:139], v[212:215], a[104:107], v[136:139]\n"
  "v_mfma_f32_16x16x32_bf16 v[152:155], v[208:211], a[100:103], v[152:155]\n"
  "ds_read_b64_tr_b16 v[204:205], v229 offset:12288\n"
  "ds_read_b64_tr_b16 v[206:207], v229 offset:12800\n"
  "v_mfma_f32_16x16x32_bf16 v[152:155], v[212:215], a[108:111], v[152:155]\n"
  "buffer_load_dwordx4 v221, s[8:11], s55 offen offset:1024 lds\n"
  "v_mfma_f32_16x16x32_bf16 v[168:171], v[208:211], a[224:227], v[168:171]\n"
  "v_mfma_f32_16x16x32_bf16 v[168:171], v[212:215], a[232:235], v[168:171]\n"
  "v_mfma_f32_16x16x32_bf16 v[184:187], v[208:211], a[228:231], v[184:187]\n"
  "v_mfma_f32_16x16x32_bf16 v[184:187], v[212:215], a[236:239], v[184:187]\n"
  "s_waitcnt lgkmcnt(4)\n"
  "s_barrier\n"
  "v_mfma_f32_16x16x32_bf16 v[132:135], v[192:195], a[96:99], v[132:135]\n"
  "ds_read_b64_tr_b16 v[208:209], v228 offset:16384\n"
  "ds_read_b64_tr_b16 v[210:211], v228 offset:16896\n"
  "v_mfma_f32_16x16x32_bf16 v[132:135], v[196:199], a[104:107], v[132:135]\n"
  "buffer_load_dwordx4 v222, s[8:11], s55 offen offset:2048 lds\n"
  "v_mfma_f32_16x16x32_bf16 v[148:151], v[192:195], a[100:103], v[148:151]\n"
  "ds_read_b64_tr_b16 v[212:213], v228 offset:24576\n"
  "ds_read_b64_tr_b16 v[214:215], v228 offset:25088\n"
  "v_mfma_f32_16x16x32_bf16 v[148:151], v[196:199], a[108:111], v[148:151]\n"
  "v_mfma_f32_16x16x32_bf16 v[164:167], v[192:195], a[224:227], v[164:167]\n"
  "v_mfma_f32_16x16x32_bf16 v[164:167], v[196:199], a[232:235], v[164:167]\n"
  "v_mfma_f32_16x16x32_bf16 v[180:183], v[192:195], a[228:231], v[180:183]\n"
  "v_mfma_f32_16x16x32_bf16 v[180:183], v[196:199], a[236:239], v[180:183]\n"
  "buffer_load_dwordx4 v223, s[8:11], s55 offen offset:3072 lds\n"
  "s_waitcnt lgkmcnt(4)\n"
  "v_mfma_f32_16x16x32_bf16 v[140:143], v[200:203], a[96:99], v[140:143]\n"
  "ds_read_b64_tr_b16 v[192:193], v228 offset:20480\n"
  "ds_read_b64_tr_b16 v[194:195], v228 offset:20992\n"
  "v_mfma_f32_16x16x32_bf16 v[140:143], v[204:207], a[104:107], v[140:143]\n"
  "v_mfma_f32_16x16x32_bf16 v[156:159], v[200:203], a[100:103], v[156:159]\n"
  "ds_read_b64_tr_b16 v[196:197], v228 offset:28672\n"
  "ds_read_b64_tr_b16 v[198:199], v228 offset:29184\n"
  "v_mfma_f32_16x16x32_bf16 v[156:159], v[204:207], a[108:111], v[156:159]\n"
  "v_mfma_f32_16x16x32_bf16 v[172:175], v[200:203], a[224:227], v[172:175]\n"
  "s_add_u32 s26, s79, 0\n"
  "s_add_u32 m0, s24, 98304\n"
  "v_mfma_f32_16x16x32_bf16 v[172:175], v[204:207], a[232:235], v[172:175]\n"
  "buffer_load_dwordx4 v236, s[12:15], s26 offen lds\n"
  "v_mfma_f32_16x16x32_bf16 v[188:191], v[200:203], a[228:231], v[188:191]\n"
  "v_mfma_f32_16x16x32_bf16 v[188:191], v[204:207], a[236:239], v[188:191]\n"
  "s_waitcnt lgkmcnt(4)\n"
  "v_mfma_f32_16x16x32_bf16 v[128:131], v[208:211], a[112:115], v[128:131]\n"
  "ds_read_b64_tr_b16 v[200:201], v229 offset:16384\n"
  "ds_read_b64_tr_b16 v[202:203], v229 offset:16896\n"
  "v_mfma_f32_16x16x32_bf16 v[128:131], v[212:215], a[120:123], v[128:131]\n"
  "v_mfma_f32_16x16x32_bf16 v[144:147], v[208:211], a[116:119], v[144:147]\n"
  "ds_read_b64_tr_b16 v[204:205], v229 offset:24576\n"
  "ds_read_b64_tr_b16 v[206:207], v229 offset:25088\n"
  "v_mfma_f32_16x16x32_bf16 v[144:147], v[212:215], a[124:127], v[144:147]\n"
  "buffer_load_dwordx4 v237, s[12:15], s26 offen offset:1024 lds\n"
  "v_mfma_f32_16x16x32_bf16 v[160:163], v[208:211], a[240:243], v[160:163]\n"
  "v_mfma_f32_16x16x32_bf16 v[160:163], v[212:215], a[248:251], v[160:163]\n"
  "v_mfma_f32_16x16x32_bf16 v[176:179], v[208:211], a[244:247], v[176:179]\n"
  "v_mfma_f32_16x16x32_bf16 v[176:179], v[212:215], a[252:255], v[176:179]\n"
  "s_waitcnt lgkmcnt(4)\n"
  "v_mfma_f32_16x16x32_bf16 v[136:139], v[192:195], a[112:115], v[136:139]\n"
  "ds_read_b64_tr_b16 v[208:209], v229 offset:20480\n"
  "ds_read_b64_tr_b16 v[210:211], v229 offset:20992\n"
  "v_mfma_f32_16x16x32_bf16 v[136:139], v[196:199], a[120:123], v[136:139]\n"
  "buffer_load_dwordx4 v238, s[12:15], s26 offen offset:2048 lds\n"
  "v_mfma_f32_16x16x32_bf16 v[152:155], v[192:195], a[116:119], v[152:155]\n"
  "ds_read_b64_tr_b16 v[212:213], v229 offset:28672\n"
  "ds_read_b64_tr_b16 v[214:215], v229 offset:29184\n"
  "v_mfma_f32_16x16x32_bf16 v[152:155], v[196:199], a[124:127], v[152:155]\n"
  "v_mfma_f32_16x16x32_bf16 v[168:171], v[192:195], a[240:243], v[168:171]\n"
  "v_mfma_f32_16x16x32_bf16 v[168:171], v[196:199], a[248:251], v[168:171]\n"
  "v_mfma_f32_16x16x32_bf16 v[184:187], v[192:195], a[244:247], v[184:187]\n"
  "v_mfma_f32_16x16x32_bf16 v[184:187], v[196:199], a[252:255], v[184:187]\n"
  "s_waitcnt vmcnt(16) lgkmcnt(4)\n"
  "s_barrier\n"
  "v_mfma_f32_16x16x32_bf16 v[132:135], v[200:203], a[112:115], v[132:135]\n"
  "ds_read_b64_tr_b16 v[192:193], v230 offset:32768\n"
  "ds_read_b64_tr_b16 v[194:195], v230 offset:33024\n"
  "v_mfma_f32_16x16x32_bf16 v[132:135], v[204:207], a[120:123], v[132:135]\n"
  "buffer_load_dwordx4 v239, s[12:15], s26 offen offset:3072 lds\n"
  "v_mfma_f32_16x16x32_bf16 v[148:151], v[200:203], a[116:119], v[148:151]\n"
  "ds_read_b64_tr_b16 v[196:197], v230 offset:34816\n"
  "ds_read_b64_tr_b16 v[198:199], v230 offset:35072\n"
  "v_mfma_f32_16x16x32_bf16 v[148:151], v[204:207], a[124:127], v[148:151]\n"
  "v_mfma_f32_16x16x32_bf16 v[164:167], v[200:203], a[240:243], v[164:167]\n"
  "v_mfma_f32_16x16x32_bf16 v[164:167], v[204:207], a[248:251], v[164:167]\n"
  "v_mfma_f32_16x16x32_bf16 v[180:183], v[200:203], a[244:247], v[180:183]\n"
  "s_add_u32 s26, s80, 0\n"
  "s_add_u32 m0, s24, 114688\n"
  "v_mfma_f32_16x16x32_bf16 v[180:183], v[204:207], a[252:255], v[180:183]\n"
  "buffer_load_dwordx4 v236, s[12:15], s26 offen lds\n"
  "s_waitcnt lgkmcnt(4)\n"
  "v_mfma_f32_16x16x32_bf16 v[140:143], v[208:211], a[112:115], v[140:143]\n"
  "ds_read_b64_tr_b16 v[200:201], v231 offset:32768\n"
  "ds_read_b64_tr_b16 v[202:203], v231 offset:33024\n"
  "v_mfma_f32_16x16x32_bf16 v[140:143], v[212:215], a[120:123], v[140:143]\n"
  "v_mfma_f32_16x16x32_bf16 v[156:159], v[208:211], a[116:119], v[156:159]\n"
  "ds_read_b64_tr_b16 v[204:205], v231 offset:34816\n"
  "ds_read_b64_tr_b16 v[206:207], v231 offset:35072\n"
  "v_mfma_f32_16x16x32_bf16 v[156:159], v[212:215], a[124:127], v[156:159]\n"
  "v_mfma_f32_16x16x32_bf16 v[172:175], v[208:211], a[240:243], v[172:175]\n"
  "v_mfma_f32_16x16x32_bf16 v[172:175], v[212:215], a[248:251], v[172:175]\n"
  "buffer_load_dwordx4 v237, s[12:15], s26 offen offset:1024 lds\n"
  "v_pk_mul_f32 v[136:137], v[136:137], v[128:129]\n"
  "v_pk_mul_f32 v[138:139], v[138:139], v[130:131]\n"
  "v_pk_mul_f32 v[140:141], v[140:141], v[132:133]\n"
  "v_pk_mul_f32 v[142:143], v[142:143], v[134:135]\n"
  "v_pk_mul_f32 v[128:129], v[128:129], s[84:85]\n"
  "v_pk_mul_f32 v[130:131], v[130:131], s[84:85]\n"
  "v_pk_mul_f32 v[132:133], v[132:133], s[84:85]\n"
  "v_pk_mul_f32 v[134:135], v[134:135], s[84:85]\n"
  "v_mfma_f32_16x16x32_bf16 v[188:191], v[208:211], a[244:247], v[188:191]\n"
  "v_exp_f32 v128, v128\n"
  "v_mfma_f32_16x16x32_bf16 v[188:191], v[212:215], a[252:255], v[188:191]\n"
  "v_exp_f32 v129, v129\n"
  "s_waitcnt lgkmcnt(4)\n"
  "v_mfma_f32_16x16x32_bf16 v[0:3], v[192:195], a[0:3], 0\n"
  "ds_read_b64_tr_b16 v[208:209], v230 offset:36864\n"
  "ds_read_b64_tr_b16 v[210:211], v230 offset:37120\n"
  "v_mfma_f32_16x16x32_bf16 v[0:3], v[196:199], a[8:11], v[0:3]\n"
  "buffer_load_dwordx4 v238, s[12:15], s26 offen offset:2048 lds\n"
  "v_exp_f32 v130, v130\n"
  "v_mfma_f32_16x16x32_bf16 v[16:19], v[192:195], a[4:7], 0\n"
  "ds_read_b64_tr_b16 v[212:213], v230 offset:38912\n"
  "ds_read_b64_tr_b16 v[214:215], v230 offset:39168\n"
  "v_mfma_f32_16x16x32_bf16 v[16:19], v[196:199], a[12:15], v[16:19]\n"
  "v_exp_f32 v131, v131\n"
  "s_waitcnt lgkmcnt(4)\n"
  "v_mfma_f32_16x16x32_bf16 v[4:7], v[200:203], a[0:3], 0\n"
  "ds_read_b64_tr_b16 v[192:193], v231 offset:36864\n"
  "ds_read_b64_tr_b16 v[194:195], v231 offset:37120\n"
  "v_mfma_f32_16x16x32_bf16 v[4:7], v[204:207], a[8:11], v[4:7]\n"
  "buffer_load_dwordx4 v239, s[12:15], s26 offen offset:3072 lds\n"
  "v_exp_f32 v132, v132\n"
  "v_mfma_f32_16x16x32_bf16 v[20:23], v[200:203], a[4:7], 0\n"
  "ds_read_b64_tr_b16 v[196:197], v231 offset:38912\n"
  "ds_read_b64_tr_b16 v[198:199], v231 offset:39168\n"
  "v_mfma_f32_16x16x32_bf16 v[20:23], v[204:207], a[12:15], v[20:23]\n"
  "v_exp_f32 v133, v133\n"
  "s_waitcnt lgkmcnt(4)\n"
  "v_mfma_f32_16x16x32_bf16 v[8:11], v[208:211], a[0:3], 0\n"
  "ds_read_b64_tr_b16 v[200:201], v230 offset:40960\n"
  "ds_read_b64_tr_b16 v[202:203], v230 offset:41216\n"
  "v_mfma_f32_16x16x32_bf16 v[8:11], v[212:215], a[8:11], v[8:11]\n"
  "s_add_u32 s26, s79, 524288\n"
  "s_add_u32 m0, s24, 131072\n"
  "v_mfma_f32_16x16x32_bf16 v[24:27], v[208:211], a[4:7], 0\n"
  "ds_read_b64_tr_b16 v[204:205], v230 offset:43008\n"
  "ds_read_b64_tr_b16 v[206:207], v230 offset:43264\n"
  "v_mfma_f32_16x16x32_bf16 v[24:27], v[212:215], a[12:15], v[24:27]\n"
  "buffer_load_dwordx4 v236, s[12:15], s26 offen lds\n"
  "s_waitcnt lgkmcnt(4)\n"
  "v_mfma_f32_16x16x32_bf16 v[12:15], v[192:195], a[0:3], 0\n"
  "ds_read_b64_tr_b16 v[208:209], v231 offset:40960\n"
  "ds_read_b64_tr_b16 v[210:211], v231 offset:41216\n"
  "v_mfma_f32_16x16x32_bf16 v[12:15], v[196:199], a[8:11], v[12:15]\n"
  "v_exp_f32 v134, v134\n"
  "v_exp_f32 v135, v135\n"
  "v_pk_mul_f32 v[152:153], v[152:153], v[144:145]\n"
  "v_pk_mul_f32 v[154:155], v[154:155], v[146:147]\n"
  "v_pk_mul_f32 v[156:157], v[156:157], v[148:149]\n"
  "v_pk_mul_f32 v[158:159], v[158:159], v[150:151]\n"
  "v_pk_add_f32 v[128:129], v[128:129], 1.0 op_sel_hi:[1,0]\n"
  "v_pk_add_f32 v[130:131], v[130:131], 1.0 op_sel_hi:[1,0]\n"
  "v_pk_add_f32 v[132:133], v[132:133], 1.0 op_sel_hi:[1,0]\n"
  "v_pk_add_f32 v[134:135], v[134:135], 1.0 op_sel_hi:[1,0]\n"
  "v_pk_mul_f32 v[144:145], v[144:145], s[84:85]\n"
  "v_pk_mul_f32 v[146:147], v[146:147], s[84:85]\n"
  "v_pk_mul_f32 v[148:149], v[148:149], s[84:85]\n"
  "v_pk_mul_f32 v[150:151], v[150:151], s[84:85]\n"
  "v_mfma_f32_16x16x32_bf16 v[28:31], v[192:195], a[4:7], 0\n"
  "ds_read_b64_tr_b16 v[212:213], v231 offset:43008\n"
  "ds_read_b64_tr_b16 v[214:215], v231 offset:43264\n"
  "v_mfma_f32_16x16x32_bf16 v[28:31], v[196:199], a[12:15], v[28:31]\n"
  "buffer_load_dwordx4 v237, s[12:15], s26 offen offset:1024 lds\n"
  "s_waitcnt lgkmcnt(4)\n"
  "v_mfma_f32_16x16x32_bf16 v[0:3], v[200:203], a[16:19], v[0:3]\n"
  "ds_read_b64_tr_b16 v[192:193], v230 offset:45056\n"
  "ds_read_b64_tr_b16 v[194:195], v230 offset:45312\n"
  "v_mfma_f32_16x16x32_bf16 v[0:3], v[204:207], a[24:27], v[0:3]\n"
  "v_rcp_f32 v128, v128\n"
  "v_mfma_f32_16x16x32_bf16 v[16:19], v[200:203], a[20:23], v[16:19]\n"
  "ds_read_b64_tr_b16 v[196:197], v230 offset:47104\n"
  "ds_read_b64_tr_b16 v[198:199], v230 offset:47360\n"
  "v_mfma_f32_16x16x32_bf16 v[16:19], v[204:207], a[28:31], v[16:19]\n"
  "v_exp_f32 v144, v144\n"
  "s_waitcnt lgkmcnt(4)\n"
  "v_mfma_f32_16x16x32_bf16 v[4:7], v[208:211], a[16:19], v[4:7]\n"
  "ds_read_b64_tr_b16 v[200:201], v231 offset:45056\n"
  "ds_read_b64_tr_b16 v[202:203], v231 offset:45312\n"
  "FP4_%=:\n"
  "s_cbranch_execz FP8_%=\n"
  "v_mfma_f32_16x16x32_bf16 v[4:7], v[212:215], a[24:27], v[4:7]\n"
  "buffer_load_dwordx4 v238, s[12:15], s26 offen offset:2048 lds\n"
  "v_rcp_f32 v129, v129\n"
  "v_mfma_f32_16x16x32_bf16 v[20:23], v[208:211], a[20:23], v[20:23]\n"
  "ds_read_b64_tr_b16 v[204:205], v231 offset:47104\n"
  "ds_read_b64_tr_b16 v[206:207], v231 offset:47360\n"
  "v_mfma_f32_16x16x32_bf16 v[20:23], v[212:215], a[28:31], v[20:23]\n"
  "s_waitcnt vmcnt(20) lgkmcnt(4)\n"
  "s_barrier\n"
  "v_mfma_f32_16x16x32_bf16 v[8:11], v[192:195], a[16:19], v[8:11]\n"
  "ds_read_b64_tr_b16 v[208:209], v230 offset:49152\n"
  "ds_read_b64_tr_b16 v[210:211], v230 offset:49408\n"
  "v_mfma_f32_16x16x32_bf16 v[8:11], v[196:199], a[24:27], v[8:11]\n"
  "buffer_load_dwordx4 v239, s[12:15], s26 offen offset:3072 lds\n"
  "v_exp_f32 v145, v145\n"
  "v_mfma_f32_16x16x32_bf16 v[24:27], v[192:195], a[20:23], v[24:27]\n"
  "ds_read_b64_tr_b16 v[212:213], v230 offset:51200\n"
  "ds_read_b64_tr_b16 v[214:215], v230 offset:51456\n"
  "v_mfma_f32_16x16x32_bf16 v[24:27], v[196:199], a[28:31], v[24:27]\n"
  "v_rcp_f32 v130, v130\n"
  "s_waitcnt lgkmcnt(4)\n"
  "v_mfma_f32_16x16x32_bf16 v[12:15], v[200:203], a[16:19], v[12:15]\n"
  "ds_read_b64_tr_b16 v[192:193], v231 offset:49152\n"
  "ds_read_b64_tr_b16 v[194:195], v231 offset:49408\n"
  "v_mfma_f32_16x16x32_bf16 v[12:15], v[204:207], a[24:27], v[12:15]\n"
  "s_add_u32 s26, s80, 524288\n"
  "s_add_u32 m0, s24, 147456\n"
  "v_mfma_f32_16x16x32_bf16 v[28:31], v[200:203], a[20:23], v[28:31]\n"
  "ds_read_b64_tr_b16 v[196:197], v231 offset:51200\n"
  "ds_read_b64_tr_b16 v[198:199], v231 offset:51456\n"
  "v_mfma_f32_16x16x32_bf16 v[28:31], v[204:207], a[28:31], v[28:31]\n"
  "buffer_load_dwordx4 v236, s[12:15], s26 offen lds\n"
  "s_waitcnt lgkmcnt(4)\n"
  "v_mfma_f32_16x16x32_bf16 v[0:3], v[208:211], a[32:35], v[0:3]\n"
  "ds_read_b64_tr_b16 v[200:201], v230 offset:53248\n"
  "ds_read_b64_tr_b16 v[202:203], v230 offset:53504\n"
  "v_mfma_f32_16x16x32_bf16 v[0:3], v[212:215], a[40:43], v[0:3]\n"
  "v_exp_f32 v146, v146\n"
  "v_mfma_f32_16x16x32_bf16 v[16:19], v[208:211], a[36:39], v[16:19]\n"
  "ds_read_b64_tr_b16 v[204:205], v230 offset:55296\n"
  "ds_read_b64_tr_b16 v[206:207], v230 offset:55552\n"
  "v_mfma_f32_16x16x32_bf16 v[16:19], v[212:215], a[44:47], v[16:19]\n"
  "v_rcp_f32 v131, v131\n"
  "s_waitcnt lgkmcnt(4)\n"
  "v_mfma_f32_16x16x32_bf16 v[4:7], v[192:195], a[32:35], v[4:7]\n"
  "ds_read_b64_tr_b16 v[208:209], v231 offset:53248\n"
  "ds_read_b64_tr_b16 v[210:211], v231 offset:53504\n"
  "v_mfma_f32_16x16x32_bf16 v[4:7], v[196:199], a[40:43], v[4:7]\n"
  "buffer_load_dwordx4 v237, s[12:15], s26 offen offset:1024 lds\n"
  "v_exp_f32 v147, v147\n"
  "v_mfma_f32_16x16x32_bf16 v[20:23], v[192:195], a[36:39], v[20:23]\n"
  "ds_read_b64_tr_b16 v[212:213], v231 offset:55296\n"
  "ds_read_b64_tr_b16 v[214:215], v231 offset:55552\n"
  "v_mfma_f32_16x16x32_bf16 v[20:23], v[196:199], a[44:47], v[20:23]\n"
  "v_rcp_f32 v132, v132\n"
  "s_waitcnt lgkmcnt(4)\n"
  "v_mfma_f32_16x16x32_bf16 v[8:11], v[200:203], a[32:35], v[8:11]\n"
  "ds_read_b64_tr_b16 v[192:193], v230 offset:57344\n"
  "ds_read_b64_tr_b16 v[194:195], v230 offset:57600\n"
  "v_mfma_f32_16x16x32_bf16 v[8:11], v[204:207], a[40:43], v[8:11]\n"
  "buffer_load_dwordx4 v238, s[12:15], s26 offen offset:2048 lds\n"
  "v_exp_f32 v148, v148\n"
  "v_mfma_f32_16x16x32_bf16 v[24:27], v[200:203], a[36:39], v[24:27]\n"
  "ds_read_b64_tr_b16 v[196:197], v230 offset:59392\n"
  "ds_read_b64_tr_b16 v[198:199], v230 offset:59648\n"
  "v_mfma_f32_16x16x32_bf16 v[24:27], v[204:207], a[44:47], v[24:27]\n"
  "v_rcp_f32 v133, v133\n"
  "s_waitcnt lgkmcnt(4)\n"
  "v_mfma_f32_16x16x32_bf16 v[12:15], v[208:211], a[32:35], v[12:15]\n"
  "ds_read_b64_tr_b16 v[200:201], v231 offset:57344\n"
  "ds_read_b64_tr_b16 v[202:203], v231 offset:57600\n"
  "v_mfma_f32_16x16x32_bf16 v[12:15], v[212:215], a[40:43], v[12:15]\n"
  "buffer_load_dwordx4 v239, s[12:15], s26 offen offset:3072 lds\n"
  "v_exp_f32 v149, v149\n"
  "v_mfma_f32_16x16x32_bf16 v[28:31], v[208:211], a[36:39], v[28:31]\n"
  "ds_read_b64_tr_b16 v[204:205], v231 offset:59392\n"
  "ds_read_b64_tr_b16 v[206:207], v231 offset:59648\n"
  "v_mfma_f32_16x16x32_bf16 v[28:31], v[212:215], a[44:47], v[28:31]\n"
  "v_rcp_f32 v134, v134\n"
  "s_waitcnt lgkmcnt(4)\n"
  "v_mfma_f32_16x16x32_bf16 v[0:3], v[192:195], a[48:51], v[0:3]\n"
  "ds_read_b64_tr_b16 v[208:209], v230 offset:61440\n"
  "ds_read_b64_tr_b16 v[210:211], v230 offset:61696\n"
  "v_mfma_f32_16x16x32_bf16 v[0:3], v[196:199], a[56:59], v[0:3]\n"
  "buffer_load_dwordx2 v[252:253], v249, s[16:19], s31 offen sc1\n"
  "v_exp_f32 v150, v150\n"
  "v_mfma_f32_16x16x32_bf16 v[16:19], v[192:195], a[52:55], v[16:19]\n"
  "ds_read_b64_tr_b16 v[212:213], v230 offset:63488\n"
  "ds_read_b64_tr_b16 v[214:215], v230 offset:63744\n"
  "v_mfma_f32_16x16x32_bf16 v[16:19], v[196:199], a[60:63], v[16:19]\n"
  "v_rcp_f32 v135, v135\n"
  "v_exp_f32 v151, v151\n"
  "v_pk_mul_f32 v[136:137], v[136:137], v[128:129]\n"
  "v_pk_mul_f32 v[138:139], v[138:139], v[130:131]\n"
  "v_pk_mul_f32 v[140:141], v[140:141], v[132:133]\n"
  "v_pk_mul_f32 v[142:143], v[142:143], v[134:135]\n"
  "v_pk_mul_f32 v[168:169], v[168:169], v[160:161]\n"
  "v_pk_mul_f32 v[170:171], v[170:171], v[162:163]\n"
  "v_pk_mul_f32 v[172:173], v[172:173], v[164:165]\n"
  "v_pk_mul_f32 v[174:175], v[174:175], v[166:167]\n"
  "v_pk_add_f32 v[144:145], v[144:145], 1.0 op_sel_hi:[1,0]\n"
  "v_pk_add_f32 v[146:147], v[146:147], 1.0 op_sel_hi:[1,0]\n"
  "v_pk_add_f32 v[148:149], v[148:149], 1.0 op_sel_hi:[1,0]\n"
  "v_pk_add_f32 v[150:151], v[150:151], 1.0 op_sel_hi:[1,0]\n"
  "v_pk_mul_f32 v[160:161], v[160:161], s[84:85]\n"
  "v_pk_mul_f32 v[162:163], v[162:163], s[84:85]\n"
  "v_pk_mul_f32 v[164:165], v[164:165], s[84:85]\n"
  "v_pk_mul_f32 v[166:167], v[166:167], s[84:85]\n"
  "s_waitcnt lgkmcnt(4)\n"
  "v_mfma_f32_16x16x32_bf16 v[4:7], v[200:203], a[48:51], v[4:7]\n"
  "ds_read_b64_tr_b16 v[192:193], v231 offset:61440\n"
  "ds_read_b64_tr_b16 v[194:195], v231 offset:61696\n"
  "v_cvt_pk_bf16_f32 v128, v136, v137\n"
  "v_mfma_f32_16x16x32_bf16 v[4:7], v[204:207], a[56:59], v[4:7]\n"
  "s_add_u32 s26, s86, 0\n"
  "s_add_u32 m0, s24, 0\n"
  "v_mfma_f32_16x16x32_bf16 v[20:23], v[200:203], a[52:55], v[20:23]\n"
  "ds_read_b64_tr_b16 v[196:197], v231 offset:63488\n"
  "ds_read_b64_tr_b16 v[198:199], v231 offset:63744\n"
  "v_mfma_f32_16x16x32_bf16 v[20:23], v[204:207], a[60:63], v[20:23]\n"
  "s_waitcnt vmcnt(21) lgkmcnt(4)\n"
  "s_barrier\n"
  "v_mfma_f32_16x16x32_bf16 v[8:11], v[208:211], a[48:51], v[8:11]\n"
  "v_add_u32 v230, 0x10000, v230\n"
  "ds_read_b64_tr_b16 v[200:201], v230 offset:0\n"
  "v_mfma_f32_16x16x32_bf16 v[8:11], v[212:215], a[56:59], v[8:11]\n"
  "buffer_load_dwordx4 v236, s[12:15], s26 offen lds\n"
  "v_rcp_f32 v144, v144\n"
  "v_mfma_f32_16x16x32_bf16 v[24:27], v[208:211], a[52:55], v[24:27]\n"
  "ds_read_b64_tr_b16 v[202:203], v230 offset:256\n"
  "ds_read_b64_tr_b16 v[204:205], v230 offset:2048\n"
  "v_cvt_pk_bf16_f32 v129, v138, v139\n"
  "v_mfma_f32_16x16x32_bf16 v[24:27], v[212:215], a[60:63], v[24:27]\n"
  "v_exp_f32 v160, v160\n"
  "v_rcp_f32 v145, v145\n"
  "v_exp_f32 v161, v161\n"
  "buffer_load_dwordx4 v237, s[12:15], s26 offen offset:1024 lds\n"
  "v_rcp_f32 v146, v146\n"
  "v_cvt_pk_bf16_f32 v130, v140, v141\n"
  "v_exp_f32 v162, v162\n"
  "v_rcp_f32 v147, v147\n"
  "v_cvt_pk_bf16_f32 v131, v142, v143\n"
  "v_exp_f32 v163, v163\n"
  "v_rcp_f32 v148, v148\n"
  "s_add_u32 s29, s27, 32768\n"
  "buffer_store_dwordx4 v[128:131], v244, s[16:19], s29 offen sc1\n"
  "v_exp_f32 v164, v164\n"
  "v_rcp_f32 v149, v149\n"
  "v_exp_f32 v165, v165\n"
  "v_rcp_f32 v150, v150\n"
  "v_exp_f32 v166, v166\n"
  "v_rcp_f32 v151, v151\n"
  "v_exp_f32 v167, v167\n"
  "v_pk_mul_f32 v[152:153], v[152:153], v[144:145]\n"
  "v_pk_mul_f32 v[154:155], v[154:155], v[146:147]\n"
  "v_pk_mul_f32 v[156:157], v[156:157], v[148:149]\n"
  "v_pk_mul_f32 v[158:159], v[158:159], v[150:151]\n"
  "v_pk_mul_f32 v[184:185], v[184:185], v[176:177]\n"
  "v_pk_mul_f32 v[186:187], v[186:187], v[178:179]\n"
  "v_pk_mul_f32 v[188:189], v[188:189], v[180:181]\n"
  "v_pk_mul_f32 v[190:191], v[190:191], v[182:183]\n"
  "v_pk_add_f32 v[160:161], v[160:161], 1.0 op_sel_hi:[1,0]\n"
  "v_pk_add_f32 v[162:163], v[162:163], 1.0 op_sel_hi:[1,0]\n"
  "v_pk_add_f32 v[164:165], v[164:165], 1.0 op_sel_hi:[1,0]\n"
  "v_pk_add_f32 v[166:167], v[166:167], 1.0 op_sel_hi:[1,0]\n"
  "v_pk_mul_f32 v[176:177], v[176:177], s[84:85]\n"
  "v_pk_mul_f32 v[178:179], v[178:179], s[84:85]\n"
  "v_pk_mul_f32 v[180:181], v[180:181], s[84:85]\n"
  "v_pk_mul_f32 v[182:183], v[182:183], s[84:85]\n"
  "v_rcp_f32 v160, v160\n"
  "buffer_load_dwordx4 v238, s[12:15], s26 offen offset:2048 lds\n"
  "v_cvt_pk_bf16_f32 v144, v152, v153\n"
  "v_exp_f32 v176, v176\n"
  "v_rcp_f32 v161, v161\n"
  "v_cvt_pk_bf16_f32 v145, v154, v155\n"
  "v_exp_f32 v177, v177\n"
  "v_rcp_f32 v162, v162\n"
  "v_cvt_pk_bf16_f32 v146, v156, v157\n"
  "v_exp_f32 v178, v178\n"
  "v_rcp_f32 v163, v163\n"
  "v_cvt_pk_bf16_f32 v147, v158, v159\n"
  "buffer_load_dwordx4 v239, s[12:15], s26 offen offset:3072 lds\n"
  "v_exp_f32 v179, v179\n"
  "v_rcp_f32 v164, v164\n"
  "buffer_store_dwordx4 v[144:147], v245, s[16:19], s29 offen sc1\n"
  "v_exp_f32 v180, v180\n"
  "v_rcp_f32 v165, v165\n"
  "v_exp_f32 v181, v181\n"
  "v_rcp_f32 v166, v166\n"
  "v_exp_f32 v182, v182\n"
  "v_rcp_f32 v167, v167\n"
  "v_exp_f32 v183, v183\n"
  "s_waitcnt vmcnt(6)\n"
  "v_cmp_ne_u64 vcc, v[252:253], v[250:251]\n"
  "s_cbranch_vccnz SLP0_%=\n"
  "BKP0_%=:\n"
  "s_add_u32 s31, s31, 4096\n"
  "s_add_u32 s31, s31, s56\n"
  "v_and_b32 v249, 0x38, v249\n"
  "v_pk_mul_f32 v[168:169], v[168:169], v[160:161]\n"
  "v_pk_mul_f32 v[170:171], v[170:171], v[162:163]\n"
  "v_pk_mul_f32 v[172:173], v[172:173], v[164:165]\n"
  "v_pk_mul_f32 v[174:175], v[174:175], v[166:167]\n"
  "v_pk_add_f32 v[176:177], v[176:177], 1.0 op_sel_hi:[1,0]\n"
  "v_pk_add_f32 v[178:179], v[178:179], 1.0 op_sel_hi:[1,0]\n"
  "v_pk_add_f32 v[180:181], v[180:181], 1.0 op_sel_hi:[1,0]\n"
  "v_pk_add_f32 v[182:183], v[182:183], 1.0 op_sel_hi:[1,0]\n"
  "v_rcp_f32 v176, v176\n"
  "s_add_u32 s57, s76, 0\n"
  "v_cvt_pk_bf16_f32 v160, v168, v169\n"
  "v_rcp_f32 v177, v177\n"
  "buffer_load_dwordx4 a[160:163], v244, s[16:19], s57 offen offset:0 sc1\n"
  "v_cvt_pk_bf16_f32 v161, v170, v171\n"
  "v_rcp_f32 v178, v178\n"
  "v_cvt_pk_bf16_f32 v162, v172, v173\n"
  "v_rcp_f32 v179, v179\n"
  "v_cvt_pk_bf16_f32 v163, v174, v175\n"
  "v_rcp_f32 v180, v180\n"
  "s_add_u32 s29, s28, 32768\n"
  "buffer_store_dwordx4 v[160:163], v244, s[16:19], s29 offen sc1\n"
  "v_rcp_f32 v181, v181\n"
  "v_rcp_f32 v182, v182\n"
  "v_rcp_f32 v183, v183\n"
  "v_pk_mul_f32 v[184:185], v[184:185], v[176:177]\n"
  "v_pk_mul_f32 v[186:187], v[186:187], v[178:179]\n"
  "v_pk_mul_f32 v[188:189], v[188:189], v[180:181]\n"
  "v_pk_mul_f32 v[190:191], v[190:191], v[182:183]\n"
  "v_cvt_pk_bf16_f32 v176, v184, v185\n"
  "v_cvt_pk_bf16_f32 v177, v186, v187\n"
  "v_cvt_pk_bf16_f32 v178, v188, v189\n"
  "v_cvt_pk_bf16_f32 v179, v190, v191\n"
  "buffer_load_dwordx4 a[168:171], v245, s[16:19], s57 offen offset:0 sc1\n"
  "buffer_store_dwordx4 v[176:179], v245, s[16:19], s29 offen sc1\n"
  "s_waitcnt lgkmcnt(3)\n"
  "v_mfma_f32_16x16x32_bf16 v[12:15], v[192:195], a[48:51], v[12:15]\n"
  "ds_read_b64_tr_b16 v[206:207], v230 offset:2304\n"
  "v_add_u32 v231, 0x10000, v231\n"
  "ds_read_b64_tr_b16 v[208:209], v231 offset:0\n"
  "v_mfma_f32_16x16x32_bf16 v[12:15], v[196:199], a[56:59], v[12:15]\n"
  "v_mfma_f32_16x16x32_bf16 v[28:31], v[192:195], a[52:55], v[28:31]\n"
  "ds_read_b64_tr_b16 v[210:211], v231 offset:256\n"
  "ds_read_b64_tr_b16 v[212:213], v231 offset:2048\n"
  "v_mfma_f32_16x16x32_bf16 v[28:31], v[196:199], a[60:63], v[28:31]\n"
  "s_waitcnt lgkmcnt(3)\n"
  "v_mfma_f32_16x16x32_bf16 v[0:3], v[200:203], a[64:67], v[0:3]\n"
  "ds_read_b64_tr_b16 v[214:215], v231 offset:2304\n"
  "ds_read_b64_tr_b16 v[192:193], v230 offset:4096\n"
  "v_mfma_f32_16x16x32_bf16 v[0:3], v[204:207], a[72:75], v[0:3]\n"
  "buffer_load_dwordx4 a[164:167], v244, s[16:19], s57 offen offset:1024 sc1\n"
  "v_mfma_f32_16x16x32_bf16 v[16:19], v[200:203], a[68:71], v[16:19]\n"
  "ds_read_b64_tr_b16 v[194:195], v230 offset:4352\n"
  "ds_read_b64_tr_b16 v[196:197], v230 offset:6144\n"
  "v_mfma_f32_16x16x32_bf16 v[16:19], v[204:207], a[76:79], v[16:19]\n"
  "s_waitcnt lgkmcnt(3)\n"
  "v_mfma_f32_16x16x32_bf16 v[4:7], v[208:211], a[64:67], v[4:7]\n"
  "ds_read_b64_tr_b16 v[198:199], v230 offset:6400\n"
  "ds_read_b64_tr_b16 v[200:201], v231 offset:4096\n"
  "v_mfma_f32_16x16x32_bf16 v[4:7], v[212:215], a[72:75], v[4:7]\n"
  "v_mfma_f32_16x16x32_bf16 v[20:23], v[208:211], a[68:71], v[20:23]\n"
  "ds_read_b64_tr_b16 v[202:203], v231 offset:4352\n"
  "ds_read_b64_tr_b16 v[204:205], v231 offset:6144\n"
  "v_mfma_f32_16x16x32_bf16 v[20:23], v[212:215], a[76:79], v[20:23]\n"
  "buffer_load_dwordx4 a[172:175], v245, s[16:19], s57 offen offset:1024 sc1\n"
  "s_waitcnt lgkmcnt(3)\n"
  "v_mfma_f32_16x16x32_bf16 v[8:11], v[192:195], a[64:67], v[8:11]\n"
  "ds_read_b64_tr_b16 v[206:207], v231 offset:6400\n"
  "ds_read_b64_tr_b16 v[208:209], v230 offset:8192\n"
  "v_mfma_f32_16x16x32_bf16 v[8:11], v[196:199], a[72:75], v[8:11]\n"
  "v_mfma_f32_16x16x32_bf16 v[24:27], v[192:195], a[68:71], v[24:27]\n"
  "ds_read_b64_tr_b16 v[210:211], v230 offset:8448\n"
  "ds_read_b64_tr_b16 v[212:213], v230 offset:10240\n"
  "v_mfma_f32_16x16x32_bf16 v[24:27], v[196:199], a[76:79], v[24:27]\n"
  "s_waitcnt lgkmcnt(3)\n"
  "v_mfma_f32_16x16x32_bf16 v[12:15], v[200:203], a[64:67], v[12:15]\n"
  "ds_read_b64_tr_b16 v[214:215], v230 offset:10496\n"
  "ds_read_b64_tr_b16 v[192:193], v231 offset:8192\n"
  "v_mfma_f32_16x16x32_bf16 v[12:15], v[204:207], a[72:75], v[12:15]\n"
  "buffer_load_dwordx4 a[176:179], v244, s[16:19], s57 offen offset:2048 sc1\n"
  "v_mfma_f32_16x16x32_bf16 v[28:31], v[200:203], a[68:71], v[28:31]\n"
  "ds_read_b64_tr_b16 v[194:195], v231 offset:8448\n"
  "ds_read_b64_tr_b16 v[196:197], v231 offset:10240\n"
  "v_mfma_f32_16x16x32_bf16 v[28:31], v[204:207], a[76:79], v[28:31]\n"
  "s_waitcnt lgkmcnt(3)\n"
  "v_mfma_f32_16x16x32_bf16 v[0:3], v[208:211], a[80:83], v[0:3]\n"
  "ds_read_b64_tr_b16 v[198:199], v231 offset:10496\n"
  "ds_read_b64_tr_b16 v[200:201], v230 offset:12288\n"
  "v_mfma_f32_16x16x32_bf16 v[0:3], v[212:215], a[88:91], v[0:3]\n"
  "v_mfma_f32_16x16x32_bf16 v[16:19], v[208:211], a[84:87], v[16:19]\n"
  "ds_read_b64_tr_b16 v[202:203], v230 offset:12544\n"
  "ds_read_b64_tr_b16 v[204:205], v230 offset:14336\n"
  "v_mfma_f32_16x16x32_bf16 v[16:19], v[212:215], a[92:95], v[16:19]\n"
  "buffer_load_dwordx4 a[184:187], v245, s[16:19], s57 offen offset:2048 sc1\n"
  "s_waitcnt lgkmcnt(3)\n"
  "v_mfma_f32_16x16x32_bf16 v[4:7], v[192:195], a[80:83], v[4:7]\n"
  "ds_read_b64_tr_b16 v[206:207], v230 offset:14592\n"
  "ds_read_b64_tr_b16 v[208:209], v231 offset:12288\n"
  "v_mfma_f32_16x16x32_bf16 v[4:7], v[196:199], a[88:91], v[4:7]\n"
  "v_mfma_f32_16x16x32_bf16 v[20:23], v[192:195], a[84:87], v[20:23]\n"
  "ds_read_b64_tr_b16 v[210:211], v231 offset:12544\n"
  "ds_read_b64_tr_b16 v[212:213], v231 offset:14336\n"
  "v_mfma_f32_16x16x32_bf16 v[20:23], v[196:199], a[92:95], v[20:23]\n"
  "s_waitcnt lgkmcnt(3)\n"
  "s_barrier\n"
  "v_mfma_f32_16x16x32_bf16 v[8:11], v[200:203], a[80:83], v[8:11]\n"
  "ds_read_b64_tr_b16 v[214:215], v231 offset:14592\n"
  "ds_read_b64_tr_b16 v[192:193], v230 offset:16384\n"
  "v_mfma_f32_16x16x32_bf16 v[8:11], v[204:207], a[88:91], v[8:11]\n"
  "buffer_load_dwordx4 a[180:183], v244, s[16:19], s57 offen offset:3072 sc1\n"
  "v_mfma_f32_16x16x32_bf16 v[24:27], v[200:203], a[84:87], v[24:27]\n"
  "ds_read_b64_tr_b16 v[194:195], v230 offset:16640\n"
  "ds_read_b64_tr_b16 v[196:197], v230 offset:18432\n"
  "v_mfma_f32_16x16x32_bf16 v[24:27], v[204:207], a[92:95], v[24:27]\n"
  "s_waitcnt lgkmcnt(3)\n"
  "v_mfma_f32_16x16x32_bf16 v[12:15], v[208:211], a[80:83], v[12:15]\n"
  "ds_read_b64_tr_b16 v[198:199], v230 offset:18688\n"
  "ds_read_b64_tr_b16 v[200:201], v231 offset:16384\n"
  "v_mfma_f32_16x16x32_bf16 v[12:15], v[212:215], a[88:91], v[12:15]\n"
  "v_mfma_f32_16x16x32_bf16 v[28:31], v[208:211], a[84:87], v[28:31]\n"
  "ds_read_b64_tr_b16 v[202:203], v231 offset:16640\n"
  "ds_read_b64_tr_b16 v[204:205], v231 offset:18432\n"
  "v_mfma_f32_16x16x32_bf16 v[28:31], v[212:215], a[92:95], v[28:31]\n"
  "buffer_load_dwordx4 a[188:191], v245, s[16:19], s57 offen offset:3072 sc1\n"
  "s_waitcnt lgkmcnt(3)\n"
  "v_mfma_f32_16x16x32_bf16 v[0:3], v[192:195], a[96:99], v[0:3]\n"
  "ds_read_b64_tr_b16 v[206:207], v231 offset:18688\n"
  "ds_read_b64_tr_b16 v[208:209], v230 offset:20480\n"
  "v_mfma_f32_16x16x32_bf16 v[0:3], v[196:199], a[104:107], v[0:3]\n"
  "v_mfma_f32_16x16x32_bf16 v[16:19], v[192:195], a[100:103], v[16:19]\n"
  "ds_read_b64_tr_b16 v[210:211], v230 offset:20736\n"
  "ds_read_b64_tr_b16 v[212:213], v230 offset:22528\n"
  "v_mfma_f32_16x16x32_bf16 v[16:19], v[196:199], a[108:111], v[16:19]\n"
  "s_add_u32 s57, s77, 0\n"
  "s_waitcnt lgkmcnt(3)\n"
  "v_mfma_f32_16x16x32_bf16 v[4:7], v[200:203], a[96:99], v[4:7]\n"
  "ds_read_b64_tr_b16 v[214:215], v230 offset:22784\n"
  "ds_read_b64_tr_b16 v[192:193], v231 offset:20480\n"
  "v_mfma_f32_16x16x32_bf16 v[4:7], v[204:207], a[104:107], v[4:7]\n"
  "buffer_load_dwordx4 a[192:195], v244, s[16:19], s57 offen offset:0 sc1\n"
  "v_mfma_f32_16x16x32_bf16 v[20:23], v[200:203], a[100:103], v[20:23]\n"
  "ds_read_b64_tr_b16 v[194:195], v231 offset:20736\n"
  "ds_read_b64_tr_b16 v[196:197], v231 offset:22528\n"
  "v_mfma_f32_16x16x32_bf16 v[20:23], v[204:207], a[108:111], v[20:23]\n"
  "s_waitcnt lgkmcnt(3)\n"
  "v_mfma_f32_16x16x32_bf16 v[8:11], v[208:211], a[96:99], v[8:11]\n"
  "ds_read_b64_tr_b16 v[198:199], v231 offset:22784\n"
  "ds_read_b64_tr_b16 v[200:201], v230 offset:24576\n"
  "v_mfma_f32_16x16x32_bf16 v[8:11], v[212:215], a[104:107], v[8:11]\n"
  "v_mfma_f32_16x16x32_bf16 v[24:27], v[208:211], a[100:103], v[24:27]\n"
  "ds_read_b64_tr_b16 v[202:203], v230 offset:24832\n"
  "ds_read_b64_tr_b16 v[204:205], v230 offset:26624\n"
  "v_mfma_f32_16x16x32_bf16 v[24:27], v[212:215], a[108:111], v[24:27]\n"
  "buffer_load_dwordx4 a[200:203], v245, s[16:19], s57 offen offset:0 sc1\n"
  "s_waitcnt lgkmcnt(3)\n"
  "v_mfma_f32_16x16x32_bf16 v[12:15], v[192:195], a[96:99], v[12:15]\n"
  "ds_read_b64_tr_b16 v[206:207], v230 offset:26880\n"
  "ds_read_b64_tr_b16 v[208:209], v231 offset:24576\n"
  "v_mfma_f32_16x16x32_bf16 v[12:15], v[196:199], a[104:107], v[12:15]\n"
  "v_mfma_f32_16x16x32_bf16 v[28:31], v[192:195], a[100:103], v[28:31]\n"
  "ds_read_b64_tr_b16 v[210:211], v231 offset:24832\n"
  "ds_read_b64_tr_b16 v[212:213], v231 offset:26624\n"
  "v_mfma_f32_16x16x32_bf16 v[28:31], v[196:199], a[108:111], v[28:31]\n"
  "s_waitcnt lgkmcnt(3)\n"
  "v_mfma_f32_16x16x32_bf16 v[0:3], v[200:203], a[112:115], v[0:3]\n"
  "ds_read_b64_tr_b16 v[214:215], v231 offset:26880\n"
  "ds_read_b64_tr_b16 v[192:193], v230 offset:28672\n"
  "v_mfma_f32_16x16x32_bf16 v[0:3], v[204:207], a[120:123], v[0:3]\n"
  "buffer_load_dwordx2 v[252:253], v249, s[16:19], s31 offen sc1\n"
  "v_mfma_f32_16x16x32_bf16 v[16:19], v[200:203], a[116:119], v[16:19]\n"
  "ds_read_b64_tr_b16 v[194:195], v230 offset:28928\n"
  "ds_read_b64_tr_b16 v[196:197], v230 offset:30720\n"
  "v_mfma_f32_16x16x32_bf16 v[16:19], v[204:207], a[124:127], v[16:19]\n"
  "s_waitcnt lgkmcnt(3)\n"
  "s_barrier\n"
  "v_mfma_f32_16x16x32_bf16 v[4:7], v[208:211], a[112:115], v[4:7]\n"
  "ds_read_b64_tr_b16 v[198:199], v230 offset:30976\n"
  "ds_read_b64_tr_b16 v[200:201], v231 offset:28672\n"
  "v_mfma_f32_16x16x32_bf16 v[4:7], v[212:215], a[120:123], v[4:7]\n"
  "ds_read_b64_tr_b16 v[202:203], v231 offset:28928\n"
  "ds_read_b64_tr_b16 v[204:205], v231 offset:30720\n"
  "v_mfma_f32_16x16x32_bf16 v[20:23], v[208:211], a[116:119], v[20:23]\n"
  "ds_read_b64_tr_b16 v[206:207], v231 offset:30976\n"
  "v_add_u32 v224, 0x10000, v224\n"
  "ds_read_b64_tr_b16 a[128:129], v224 offset:32768\n"
  "v_mfma_f32_16x16x32_bf16 v[20:23], v[212:215], a[124:127], v[20:23]\n"
  "buffer_load_dwordx4 a[196:199], v244, s[16:19], s57 offen offset:1024 sc1\n"
  "s_waitcnt lgkmcnt(5)\n"
  "v_mfma_f32_16x16x32_bf16 v[8:11], v[192:195], a[112:115], v[8:11]\n"
  "ds_read_b64_tr_b16 a[130:131], v224 offset:34816\n"
  "ds_read_b64_tr_b16 a[132:133], v224 offset:36864\n"
  "v_mfma_f32_16x16x32_bf16 v[8:11], v[196:199], a[120:123], v[8:11]\n"
  "ds_read_b64_tr_b16 a[134:135], v224 offset:38912\n"
  "v_add_u32 v225, 0x10000, v225\n"
  "ds_read_b64_tr_b16 a[136:137], v225 offset:32768\n"
  "v_mfma_f32_16x16x32_bf16 v[24:27], v[192:195], a[116:119], v[24:27]\n"
  "s_waitcnt vmcnt(10)\n"
  "buffer_store_dwordx2 v[250:251], v248, s[16:19], s30 offen sc1\n"
  "s_add_u32 s30, s30, 4096\n"
  "v_mfma_f32_16x16x32_bf16 v[24:27], v[196:199], a[124:127], v[24:27]\n"
  "ds_read_b64_tr_b16 a[138:139], v225 offset:34816\n"
  "s_waitcnt lgkmcnt(6)\n"
  "v_mfma_f32_16x16x32_bf16 v[12:15], v[200:203], a[112:115], v[12:15]\n"
  "ds_read_b64_tr_b16 a[140:141], v225 offset:36864\n"
  "ds_read_b64_tr_b16 a[142:143], v225 offset:38912\n"
  "v_mfma_f32_16x16x32_bf16 v[12:15], v[204:207], a[120:123], v[12:15]\n"
  "v_mfma_f32_16x16x32_bf16 v[28:31], v[200:203], a[116:119], v[28:31]\n"
  "v_add_u32 v226, 0x10000, v226\n"
  "ds_read_b64_tr_b16 a[144:145], v226 offset:32768\n"
  "v_mfma_f32_16x16x32_bf16 v[28:31], v[204:207], a[124:127], v[28:31]\n"
  "buffer_load_dwordx4 a[204:207], v245, s[16:19], s57 offen offset:1024 sc1\n"
  "s_waitcnt vmcnt(10) lgkmcnt(5)\n"
  "v_mfma_f32_16x16x32_bf16 v[32:35], a[128:131], a[160:163], 0\n"
  "ds_read_b64_tr_b16 a[146:147], v226 offset:34816\n"
  "ds_read_b64_tr_b16 a[148:149], v226 offset:36864\n"
  "v_mfma_f32_16x16x32_bf16 v[32:35], a[132:135], a[164:167], v[32:35]\n"
  "ds_read_b64_tr_b16 a[150:151], v226 offset:38912\n"
  "v_add_u32 v227, 0x10000, v227\n"
  "ds_read_b64_tr_b16 a[152:153], v227 offset:32768\n"
  "v_pk_mul_f32 v[8:9], v[8:9], v[0:1]\n"
  "v_pk_mul_f32 v[10:11], v[10:11], v[2:3]\n"
  "v_pk_mul_f32 v[12:13], v[12:13], v[4:5]\n"
  "v_pk_mul_f32 v[14:15], v[14:15], v[6:7]\n"
  "v_pk_mul_f32 v[0:1], v[0:1], s[84:85]\n"
  "v_pk_mul_f32 v[2:3], v[2:3], s[84:85]\n"
  "v_pk_mul_f32 v[4:5], v[4:5], s[84:85]\n"
  "v_pk_mul_f32 v[6:7], v[6:7], s[84:85]\n"
  "v_mfma_f32_16x16x32_bf16 v[48:51], a[128:131], a[168:171], 0\n"
  "buffer_load_dwordx4 a[208:211], v244, s[16:19], s57 offen offset:2048 sc1\n"
  "v_exp_f32 v0, v0\n"
  "v_mfma_f32_16x16x32_bf16 v[48:51], a[132:135], a[172:175], v[48:51]\n"
  "ds_read_b64_tr_b16 a[154:155], v227 offset:34816\n"
  "s_waitcnt lgkmcnt(6)\n"
  "v_mfma_f32_16x16x32_bf16 v[36:39], a[136:139], a[160:163], 0\n"
  "ds_read_b64_tr_b16 a[156:157], v227 offset:36864\n"
  "ds_read_b64_tr_b16 a[158:159], v227 offset:38912\n"
  "v_mfma_f32_16x16x32_bf16 v[36:39], a[140:143], a[164:167], v[36:39]\n"
  "v_exp_f32 v1, v1\n"
  "v_mfma_f32_16x16x32_bf16 v[52:55], a[136:139], a[168:171], 0\n"
  "ds_read_b64_tr_b16 a[128:129], v224 offset:40960\n"
  "ds_read_b64_tr_b16 a[130:131], v224 offset:43008\n"
  "v_mfma_f32_16x16x32_bf16 v[52:55], a[140:143], a[172:175], v[52:55]\n"
  "buffer_load_dwordx4 a[216:219], v245, s[16:19], s57 offen offset:2048 sc1\n"
  "s_waitcnt lgkmcnt(6)\n"
  "v_mfma_f32_16x16x32_bf16 v[40:43], a[144:147], a[160:163], 0\n"
  "ds_read_b64_tr_b16 a[132:133], v224 offset:45056\n"
  "ds_read_b64_tr_b16 a[134:135], v224 offset:47104\n"
  "v_mfma_f32_16x16x32_bf16 v[40:43], a[148:151], a[164:167], v[40:43]\n"
  "v_exp_f32 v2, v2\n"
  "v_mfma_f32_16x16x32_bf16 v[56:59], a[144:147], a[168:171], 0\n"
  "ds_read_b64_tr_b16 a[136:137], v225 offset:40960\n"
  "ds_read_b64_tr_b16 a[138:139], v225 offset:43008\n"
  "v_mfma_f32_16x16x32_bf16 v[56:59], a[148:151], a[172:175], v[56:59]\n"
  "buffer_load_dwordx4 a[212:215], v244, s[16:19], s57 offen offset:3072 sc1\n"
  "s_waitcnt lgkmcnt(6)\n"
  "v_mfma_f32_16x16x32_bf16 v[44:47], a[152:155], a[160:163], 0\n"
  "ds_read_b64_tr_b16 a[140:141], v225 offset:45056\n"
  "ds_read_b64_tr_b16 a[142:143], v225 offset:47104\n"
  "v_mfma_f32_16x16x32_bf16 v[44:47], a[156:159], a[164:167], v[44:47]\n"
  "v_exp_f32 v3, v3\n"
  "v_mfma_f32_16x16x32_bf16 v[60:63], a[152:155], a[168:171], 0\n"
  "ds_read_b64_tr_b16 a[144:145], v226 offset:40960\n"
  "ds_read_b64_tr_b16 a[146:147], v226 offset:43008\n"
  "v_mfma_f32_16x16x32_bf16 v[60:63], a[156:159], a[172:175], v[60:63]\n"
  "buffer_load_dwordx4 a[220:223], v245, s[16:19], s57 offen offset:3072 sc1\n"
  "s_waitcnt vmcnt(10) lgkmcnt(6)\n"
  "v_mfma_f32_16x16x32_bf16 v[32:35], a[128:131], a[176:179], v[32:35]\n"
  "ds_read_b64_tr_b16 a[148:149], v226 offset:45056\n"
  "ds_read_b64_tr_b16 a[150:151], v226 offset:47104\n"
  "v_mfma_f32_16x16x32_bf16 v[32:35], a[132:135], a[180:183], v[32:35]\n"
  "v_exp_f32 v4, v4\n"
  "v_mfma_f32_16x16x32_bf16 v[48:51], a[128:131], a[184:187], v[48:51]\n"
  "ds_read_b64_tr_b16 a[152:153], v227 offset:40960\n"
  "ds_read_b64_tr_b16 a[154:155], v227 offset:43008\n"
  "v_mfma_f32_16x16x32_bf16 v[48:51], a[132:135], a[188:191], v[48:51]\n"
  "s_waitcnt lgkmcnt(6)\n"
  "s_barrier\n"
  "v_mfma_f32_16x16x32_bf16 v[36:39], a[136:139], a[176:179], v[36:39]\n"
  "ds_read_b64_tr_b16 a[156:157], v227 offset:45056\n"
  "ds_read_b64_tr_b16 a[158:159], v227 offset:47104\n"
  "v_mfma_f32_16x16x32_bf16 v[36:39], a[140:143], a[180:183], v[36:39]\n"
  "s_add_u32 s26, s87, 0\n"
  "s_add_u32 m0, s24, 16384\n"
  "v_mfma_f32_16x16x32_bf16 v[52:55], a[136:139], a[184:187], v[52:55]\n"
  "ds_read_b64_tr_b16 a[128:129], v224 offset:49152\n"
  "ds_read_b64_tr_b16 a[130:131], v224 offset:51200\n"
  "v_mfma_f32_16x16x32_bf16 v[52:55], a[140:143], a[188:191], v[52:55]\n"
  "buffer_load_dwordx4 v236, s[12:15], s26 offen lds\n"
  "s_waitcnt lgkmcnt(6)\n"
  "v_mfma_f32_16x16x32_bf16 v[40:43], a[144:147], a[176:179], v[40:43]\n"
  "ds_read_b64_tr_b16 a[132:133], v224 offset:53248\n"
  "ds_read_b64_tr_b16 a[134:135], v224 offset:55296\n"
  "v_mfma_f32_16x16x32_bf16 v[40:43], a[148:151], a[180:183], v[40:43]\n"
  "s_waitcnt vmcnt(8)\n"
  "v_cmp_ne_u64 vcc, v[252:253], v[250:251]\n"
  "s_cbranch_vccnz SLP1_%=\n"
  "BKP1_%=:\n"
  "s_add_u32 s31, s31, 4096\n"
  "v_mfma_f32_16x16x32_bf16 v[56:59], a[144:147], a[184:187], v[56:59]\n"
  "ds_read_b64_tr_b16 a[136:137], v225 offset:49152\n"
  "ds_read_b64_tr_b16 a[138:139], v225 offset:51200\n"
  "v_mfma_f32_16x16x32_bf16 v[56:59], a[148:151], a[188:191], v[56:59]\n"
  "buffer_load_dwordx4 v237, s[12:15], s26 offen offset:1024 lds\n"
  "s_waitcnt lgkmcnt(6)\n"
  "v_mfma_f32_16x16x32_bf16 v[44:47], a[152:155], a[176:179], v[44:47]\n"
  "ds_read_b64_tr_b16 a[140:141], v225 offset:53248\n"
  "ds_read_b64_tr_b16 a[142:143], v225 offset:55296\n"
  "v_mfma_f32_16x16x32_bf16 v[44:47], a[156:159], a[180:183], v[44:47]\n"
  "v_exp_f32 v5, v5\n"
  "v_mfma_f32_16x16x32_bf16 v[60:63], a[152:155], a[184:187], v[60:63]\n"
  "ds_read_b64_tr_b16 a[144:145], v226 offset:49152\n"
  "ds_read_b64_tr_b16 a[146:147], v226 offset:51200\n"
  "v_mfma_f32_16x16x32_bf16 v[60:63], a[156:159], a[188:191], v[60:63]\n"
  "buffer_load_dwordx4 v238, s[12:15], s26 offen offset:2048 lds\n"
  "v_exp_f32 v6, v6\n"
  "v_exp_f32 v7, v7\n"
  "v_pk_mul_f32 v[24:25], v[24:25], v[16:17]\n"
  "v_pk_mul_f32 v[26:27], v[26:27], v[18:19]\n"
  "v_pk_mul_f32 v[28:29], v[28:29], v[20:21]\n"
  "v_pk_mul_f32 v[30:31], v[30:31], v[22:23]\n"
  "v_pk_add_f32 v[0:1], v[0:1], 1.0 op_sel_hi:[1,0]\n"
  "v_pk_add_f32 v[2:3], v[2:3], 1.0 op_sel_hi:[1,0]\n"
  "v_pk_add_f32 v[4:5], v[4:5], 1.0 op_sel_hi:[1,0]\n"
  "v_pk_add_f32 v[6:7], v[6:7], 1.0 op_sel_hi:[1,0]\n"
  "v_pk_mul_f32 v[16:17], v[16:17], s[84:85]\n"
  "v_pk_mul_f32 v[18:19], v[18:19], s[84:85]\n"
  "v_pk_mul_f32 v[20:21], v[20:21], s[84:85]\n"
  "v_pk_mul_f32 v[22:23], v[22:23], s[84:85]\n"
  "v_rcp_f32 v0, v0\n"
  "buffer_load_dwordx4 v239, s[12:15], s26 offen offset:3072 lds\n"
  "v_exp_f32 v16, v16\n"
  "v_rcp_f32 v1, v1\n"
  "v_exp_f32 v17, v17\n"
  "v_rcp_f32 v2, v2\n"
  "v_exp_f32 v18, v18\n"
  "v_rcp_f32 v3, v3\n"
  "s_add_u32 s57, s76, 16384\n"
  "v_exp_f32 v19, v19\n"
  "v_rcp_f32 v4, v4\n"
  "buffer_load_dwordx4 a[224:227], v244, s[16:19], s57 offen offset:0 sc1\n"
  "v_exp_f32 v20, v20\n"
  "v_rcp_f32 v5, v5\n"
  "v_exp_f32 v21, v21\n"
  "v_rcp_f32 v6, v6\n"
  "v_exp_f32 v22, v22\n"
  "v_rcp_f32 v7, v7\n"
  "v_exp_f32 v23, v23\n"
  "v_pk_mul_f32 v[8:9], v[8:9], v[0:1]\n"
  "v_pk_mul_f32 v[10:11], v[10:11], v[2:3]\n"
  "v_pk_mul_f32 v[12:13], v[12:13], v[4:5]\n"
  "v_pk_mul_f32 v[14:15], v[14:15], v[6:7]\n"
  "v_pk_add_f32 v[16:17], v[16:17], 1.0 op_sel_hi:[1,0]\n"
  "v_pk_add_f32 v[18:19], v[18:19], 1.0 op_sel_hi:[1,0]\n"
  "v_pk_add_f32 v[20:21], v[20:21], 1.0 op_sel_hi:[1,0]\n"
  "v_pk_add_f32 v[22:23], v[22:23], 1.0 op_sel_hi:[1,0]\n"
  "buffer_load_dwordx4 a[232:235], v245, s[16:19], s57 offen offset:0 sc1\n"
  "v_rcp_f32 v16, v16\n"
  "v_cvt_pk_bf16_f32 v0, v8, v9\n"
  "v_rcp_f32 v17, v17\n"
  "v_cvt_pk_bf16_f32 v1, v10, v11\n"
  "s_waitcnt vmcnt(10) lgkmcnt(6)\n"
  "v_mfma_f32_16x16x32_bf16 v[32:35], a[128:131], a[192:195], v[32:35]\n"
  "ds_read_b64_tr_b16 a[148:149], v226 offset:53248\n"
  "ds_read_b64_tr_b16 a[150:151], v226 offset:55296\n"
  "v_cvt_pk_bf16_f32 v2, v12, v13\n"
  "v_mfma_f32_16x16x32_bf16 v[32:35], a[132:135], a[196:199], v[32:35]\n"
  "buffer_load_dwordx4 a[228:231], v244, s[16:19], s57 offen offset:1024 sc1\n"
  "v_rcp_f32 v18, v18\n"
  "v_mfma_f32_16x16x32_bf16 v[48:51], a[128:131], a[200:203], v[48:51]\n"
  "ds_read_b64_tr_b16 a[152:153], v227 offset:49152\n"
  "ds_read_b64_tr_b16 a[154:155], v227 offset:51200\n"
  "v_cvt_pk_bf16_f32 v3, v14, v15\n"
  "v_mfma_f32_16x16x32_bf16 v[48:51], a[132:135], a[204:207], v[48:51]\n"
  "buffer_load_dwordx4 a[236:239], v245, s[16:19], s57 offen offset:1024 sc1\n"
  "s_add_u32 s29, s73, 49152\n"
  "s_waitcnt lgkmcnt(6)\n"
  "v_mfma_f32_16x16x32_bf16 v[36:39], a[136:139], a[192:195], v[36:39]\n"
  "ds_read_b64_tr_b16 a[156:157], v227 offset:53248\n"
  "ds_read_b64_tr_b16 a[158:159], v227 offset:55296\n"
  "buffer_store_dwordx4 v[0:3], v244, s[16:19], s29 offen sc1\n"
  "v_mfma_f32_16x16x32_bf16 v[36:39], a[140:143], a[196:199], v[36:39]\n"
  "v_rcp_f32 v19, v19\n"
  "v_mfma_f32_16x16x32_bf16 v[52:55], a[136:139], a[200:203], v[52:55]\n"
  "ds_read_b64_tr_b16 a[128:129], v224 offset:57344\n"
  "FP5_%=:\n"
  "s_cbranch_execz FPE_%=\n"
  "ds_read_b64_tr_b16 a[130:131], v224 offset:59392\n"
  "v_mfma_f32_16x16x32_bf16 v[52:55], a[140:143], a[204:207], v[52:55]\n"
  "v_rcp_f32 v20, v20\n"
  "s_waitcnt lgkmcnt(6)\n"
  "v_mfma_f32_16x16x32_bf16 v[40:43], a[144:147], a[192:195], v[40:43]\n"
  "ds_read_b64_tr_b16 a[132:133], v224 offset:61440\n"
  "ds_read_b64_tr_b16 a[134:135], v224 offset:63488\n"
  "v_mfma_f32_16x16x32_bf16 v[40:43], a[148:151], a[196:199], v[40:43]\n"
  "buffer_load_dwordx4 a[240:243], v244, s[16:19], s57 offen offset:2048 sc1\n"
  "v_rcp_f32 v21, v21\n"
  "v_mfma_f32_16x16x32_bf16 v[56:59], a[144:147], a[200:203], v[56:59]\n"
  "ds_read_b64_tr_b16 a[136:137], v225 offset:57344\n"
  "ds_read_b64_tr_b16 a[138:139], v225 offset:59392\n"
  "v_mfma_f32_16x16x32_bf16 v[56:59], a[148:151], a[204:207], v[56:59]\n"
  "v_rcp_f32 v22, v22\n"
  "v_rcp_f32 v23, v23\n"
  "v_pk_mul_f32 v[24:25], v[24:25], v[16:17]\n"
  "v_pk_mul_f32 v[26:27], v[26:27], v[18:19]\n"
  "v_pk_mul_f32 v[28:29], v[28:29], v[20:21]\n"
  "v_pk_mul_f32 v[30:31], v[30:31], v[22:23]\n"
  "s_waitcnt lgkmcnt(6)\n"
  "v_mfma_f32_16x16x32_bf16 v[44:47], a[152:155], a[192:195], v[44:47]\n"
  "ds_read_b64_tr_b16 a[140:141], v225 offset:61440\n"
  "ds_read_b64_tr_b16 a[142:143], v225 offset:63488\n"
  "v_cvt_pk_bf16_f32 v16, v24, v25\n"
  "v_mfma_f32_16x16x32_bf16 v[44:47], a[156:159], a[196:199], v[44:47]\n"
  "buffer_load_dwordx4 a[248:251], v245, s[16:19], s57 offen offset:2048 sc1\n"
  "v_cvt_pk_bf16_f32 v17, v26, v27\n"
  "v_cvt_pk_bf16_f32 v18, v28, v29\n"
  "v_mfma_f32_16x16x32_bf16 v[60:63], a[152:155], a[200:203], v[60:63]\n"
  "ds_read_b64_tr_b16 a[144:145], v226 offset:57344\n"
  "ds_read_b64_tr_b16 a[146:147], v226 offset:59392\n"
  "v_cvt_pk_bf16_f32 v19, v30, v31\n"
  "v_mfma_f32_16x16x32_bf16 v[60:63], a[156:159], a[204:207], v[60:63]\n"
  "buffer_store_dwordx4 v[16:19], v245, s[16:19], s29 offen sc1\n"
  "s_waitcnt vmcnt(12) lgkmcnt(6)\n"
  "v_mfma_f32_16x16x32_bf16 v[32:35], a[128:131], a[208:211], v[32:35]\n"
  "ds_read_b64_tr_b16 a[148:149], v226 offset:61440\n"
  "ds_read_b64_tr_b16 a[150:151], v226 offset:63488\n"
  "v_mfma_f32_16x16x32_bf16 v[32:35], a[132:135], a[212:215], v[32:35]\n"
  "v_mfma_f32_16x16x32_bf16 v[48:51], a[128:131], a[216:219], v[48:51]\n"
  "ds_read_b64_tr_b16 a[152:153], v227 offset:57344\n"
  "ds_read_b64_tr_b16 a[154:155], v227 offset:59392\n"
  "v_mfma_f32_16x16x32_bf16 v[48:51], a[132:135], a[220:223], v[48:51]\n"
  "buffer_load_dwordx4 a[244:247], v244, s[16:19], s57 offen offset:3072 sc1\n"
  "s_waitcnt lgkmcnt(6)\n"
  "s_barrier\n"
  "v_mfma_f32_16x16x32_bf16 v[36:39], a[136:139], a[208:211], v[36:39]\n"
  "ds_read_b64_tr_b16 a[156:157], v227 offset:61440\n"
  "ds_read_b64_tr_b16 a[158:159], v227 offset:63488\n"
  "v_mfma_f32_16x16x32_bf16 v[36:39], a[140:143], a[212:215], v[36:39]\n"
  "buffer_load_dwordx4 a[252:255], v245, s[16:19], s57 offen offset:3072 sc1\n"
  "v_mfma_f32_16x16x32_bf16 v[52:55], a[136:139], a[216:219], v[52:55]\n"
  "v_add_u32 v224, 0x10000, v224\n"
  "ds_read_b64_tr_b16 a[128:129], v224 offset:0\n"
  "v_mfma_f32_16x16x32_bf16 v[52:55], a[140:143], a[220:223], v[52:55]\n"
  "s_waitcnt lgkmcnt(5)\n"
  "v_mfma_f32_16x16x32_bf16 v[40:43], a[144:147], a[208:211], v[40:43]\n"
  "ds_read_b64_tr_b16 a[130:131], v224 offset:2048\n"
  "ds_read_b64_tr_b16 a[132:133], v224 offset:4096\n"
  "v_mfma_f32_16x16x32_bf16 v[40:43], a[148:151], a[212:215], v[40:43]\n"
  "ds_read_b64_tr_b16 a[134:135], v224 offset:6144\n"
  "v_add_u32 v225, 0x10000, v225\n"
  "ds_read_b64_tr_b16 a[136:137], v225 offset:0\n"
  "v_mfma_f32_16x16x32_bf16 v[56:59], a[144:147], a[216:219], v[56:59]\n"
  "s_add_u32 s57, s77, 16384\n"
  "v_mfma_f32_16x16x32_bf16 v[56:59], a[148:151], a[220:223], v[56:59]\n"
  "ds_read_b64_tr_b16 a[138:139], v225 offset:2048\n"
  "s_waitcnt lgkmcnt(6)\n"
  "v_mfma_f32_16x16x32_bf16 v[44:47], a[152:155], a[208:211], v[44:47]\n"
  "ds_read_b64_tr_b16 a[140:141], v225 offset:4096\n"
  "ds_read_b64_tr_b16 a[142:143], v225 offset:6144\n"
  "v_mfma_f32_16x16x32_bf16 v[44:47], a[156:159], a[212:215], v[44:47]\n"
  "buffer_load_dwordx4 a[0:3], v244, s[16:19], s57 offen offset:0 sc1\n"
  "v_mfma_f32_16x16x32_bf16 v[60:63], a[152:155], a[216:219], v[60:63]\n"
  "v_add_u32 v226, 0x10000, v226\n"
  "ds_read_b64_tr_b16 a[144:145], v226 offset:0\n"
  "v_mfma_f32_16x16x32_bf16 v[60:63], a[156:159], a[220:223], v[60:63]\n"
  "s_waitcnt vmcnt(7) lgkmcnt(5)\n"
  "v_mfma_f32_16x16x32_bf16 v[32:35], a[128:131], a[224:227], v[32:35]\n"
  "ds_read_b64_tr_b16 a[146:147], v226 offset:2048\n"
  "ds_read_b64_tr_b16 a[148:149], v226 offset:4096\n"
  "v_mfma_f32_16x16x32_bf16 v[32:35], a[132:135], a[228:231], v[32:35]\n"
  "ds_read_b64_tr_b16 a[150:151], v226 offset:6144\n"
  "v_add_u32 v227, 0x10000, v227\n"
  "ds_read_b64_tr_b16 a[152:153], v227 offset:0\n"
  "v_mfma_f32_16x16x32_bf16 v[48:51], a[128:131], a[232:235], v[48:51]\n"
  "buffer_load_dwordx4 a[8:11], v245, s[16:19], s57 offen offset:0 sc1\n"
  "v_mfma_f32_16x16x32_bf16 v[48:51], a[132:135], a[236:239], v[48:51]\n"
  "ds_read_b64_tr_b16 a[154:155], v227 offset:2048\n"
  "s_waitcnt lgkmcnt(6)\n"
  "v_mfma_f32_16x16x32_bf16 v[36:39], a[136:139], a[224:227], v[36:39]\n"
  "ds_read_b64_tr_b16 a[156:157], v227 offset:4096\n"
  "ds_read_b64_tr_b16 a[158:159], v227 offset:6144\n"
  "v_mfma_f32_16x16x32_bf16 v[36:39], a[140:143], a[228:231], v[36:39]\n"
  "buffer_load_dwordx4 a[4:7], v244, s[16:19], s57 offen offset:1024 sc1\n"
  "v_mfma_f32_16x16x32_bf16 v[52:55], a[136:139], a[232:235], v[52:55]\n"
  "ds_read_b64_tr_b16 a[128:129], v224 offset:8192\n"
  "ds_read_b64_tr_b16 a[130:131], v224 offset:10240\n"
  "v_mfma_f32_16x16x32_bf16 v[52:55], a[140:143], a[236:239], v[52:55]\n"
  "buffer_load_dwordx4 a[12:15], v245, s[16:19], s57 offen offset:1024 sc1\n"
  "s_waitcnt lgkmcnt(6)\n"
  "v_mfma_f32_16x16x32_bf16 v[40:43], a[144:147], a[224:227], v[40:43]\n"
  "ds_read_b64_tr_b16 a[132:133], v224 offset:12288\n"
  "ds_read_b64_tr_b16 a[134:135], v224 offset:14336\n"
  "v_mfma_f32_16x16x32_bf16 v[40:43], a[148:151], a[228:231], v[40:43]\n"
  "v_mfma_f32_16x16x32_bf16 v[56:59], a[144:147], a[232:235], v[56:59]\n"
  "ds_read_b64_tr_b16 a[136:137], v225 offset:8192\n"
  "ds_read_b64_tr_b16 a[138:139], v225 offset:10240\n"
  "v_mfma_f32_16x16x32_bf16 v[56:59], a[148:151], a[236:239], v[56:59]\n"
  "buffer_load_dwordx4 a[16:19], v244, s[16:19], s57 offen offset:2048 sc1\n"
  "s_waitcnt lgkmcnt(6)\n"
  "v_mfma_f32_16x16x32_bf16 v[44:47], a[152:155], a[224:227], v[44:47]\n"
  "ds_read_b64_tr_b16 a[140:141], v225 offset:12288\n"
  "ds_read_b64_tr_b16 a[142:143], v225 offset:14336\n"
  "v_mfma_f32_16x16x32_bf16 v[44:47], a[156:159], a[228:231], v[44:47]\n"
  "v_mfma_f32_16x16x32_bf16 v[60:63], a[152:155], a[232:235], v[60:63]\n"
  "ds_read_b64_tr_b16 a[144:145], v226 offset:8192\n"
  "ds_read_b64_tr_b16 a[146:147], v226 offset:10240\n"
  "v_mfma_f32_16x16x32_bf16 v[60:63], a[156:159], a[236:239], v[60:63]\n"
  "buffer_load_dwordx4 a[24:27], v245, s[16:19], s57 offen offset:2048 sc1\n"
  "buffer_load_dwordx4 a[20:23], v244, s[16:19], s57 offen offset:3072 sc1\n"
  "buffer_load_dwordx4 a[28:31], v245, s[16:19], s57 offen offset:3072 sc1\n"
  "s_lshl_b32 s60, s74, 10\n"
  "s_add_u32 s60, s60, 22020096\n"
  "v_lshrrev_b32 v107, 2, v244\n"
  "buffer_load_dword v104, v107, s[16:19], s60 offen offset:0 sc1\n"
  "buffer_load_dword v105, v107, s[16:19], s60 offen offset:256 sc1\n"
  "s_waitcnt vmcnt(10) lgkmcnt(6)\n"
  "v_mfma_f32_16x16x32_bf16 v[32:35], a[128:131], a[240:243], v[32:35]\n"
  "ds_read_b64_tr_b16 a[148:149], v226 offset:12288\n"
  "ds_read_b64_tr_b16 a[150:151], v226 offset:14336\n"
  "v_mfma_f32_16x16x32_bf16 v[32:35], a[132:135], a[244:247], v[32:35]\n"
  "buffer_store_dwordx2 v[250:251], v248, s[16:19], s30 offen sc1\n"
  "s_add_u32 s30, s30, 4096\n"
  "v_mfma_f32_16x16x32_bf16 v[48:51], a[128:131], a[248:251], v[48:51]\n"
  "ds_read_b64_tr_b16 a[152:153], v227 offset:8192\n"
  "ds_read_b64_tr_b16 a[154:155], v227 offset:10240\n"
  "v_mfma_f32_16x16x32_bf16 v[48:51], a[132:135], a[252:255], v[48:51]\n"
  "s_waitcnt lgkmcnt(6)\n"
  "s_barrier\n"
  "v_mfma_f32_16x16x32_bf16 v[36:39], a[136:139], a[240:243], v[36:39]\n"
  "ds_read_b64_tr_b16 a[156:157], v227 offset:12288\n"
  "ds_read_b64_tr_b16 a[158:159], v227 offset:14336\n"
  "v_mfma_f32_16x16x32_bf16 v[36:39], a[140:143], a[244:247], v[36:39]\n"
  "s_add_u32 s26, s79, 1048576\n"
  "s_add_u32 m0, s24, 32768\n"
  "v_mfma_f32_16x16x32_bf16 v[52:55], a[136:139], a[248:251], v[52:55]\n"
  "ds_read_b64_tr_b16 a[128:129], v224 offset:16384\n"
  "ds_read_b64_tr_b16 a[130:131], v224 offset:18432\n"
  "v_mfma_f32_16x16x32_bf16 v[52:55], a[140:143], a[252:255], v[52:55]\n"
  "buffer_load_dwordx4 v236, s[12:15], s26 offen lds\n"
  "s_waitcnt lgkmcnt(6)\n"
  "v_mfma_f32_16x16x32_bf16 v[40:43], a[144:147], a[240:243], v[40:43]\n"
  "ds_read_b64_tr_b16 a[132:133], v224 offset:20480\n"
  "ds_read_b64_tr_b16 a[134:135], v224 offset:22528\n"
  "v_mfma_f32_16x16x32_bf16 v[40:43], a[148:151], a[244:247], v[40:43]\n"
  "v_mfma_f32_16x16x32_bf16 v[56:59], a[144:147], a[248:251], v[56:59]\n"
  "ds_read_b64_tr_b16 a[136:137], v225 offset:16384\n"
  "ds_read_b64_tr_b16 a[138:139], v225 offset:18432\n"
  "v_mfma_f32_16x16x32_bf16 v[56:59], a[148:151], a[252:255], v[56:59]\n"
  "buffer_load_dwordx4 v237, s[12:15], s26 offen offset:1024 lds\n"
  "s_waitcnt lgkmcnt(6)\n"
  "v_mfma_f32_16x16x32_bf16 v[44:47], a[152:155], a[240:243], v[44:47]\n"
  "ds_read_b64_tr_b16 a[140:141], v225 offset:20480\n"
  "ds_read_b64_tr_b16 a[142:143], v225 offset:22528\n"
  "v_mfma_f32_16x16x32_bf16 v[44:47], a[156:159], a[244:247], v[44:47]\n"
  "v_mfma_f32_16x16x32_bf16 v[60:63], a[152:155], a[248:251], v[60:63]\n"
  "ds_read_b64_tr_b16 a[144:145], v226 offset:16384\n"
  "ds_read_b64_tr_b16 a[146:147], v226 offset:18432\n"
  "v_mfma_f32_16x16x32_bf16 v[60:63], a[156:159], a[252:255], v[60:63]\n"
  "buffer_load_dwordx4 v238, s[12:15], s26 offen offset:2048 lds\n"
  "s_waitcnt vmcnt(10) lgkmcnt(6)\n"
  "v_mfma_f32_16x16x32_bf16 v[32:35], a[128:131], a[0:3], v[32:35]\n"
  "ds_read_b64_tr_b16 a[148:149], v226 offset:20480\n"
  "ds_read_b64_tr_b16 a[150:151], v226 offset:22528\n"
  "v_mfma_f32_16x16x32_bf16 v[32:35], a[132:135], a[4:7], v[32:35]\n"
  "v_mfma_f32_16x16x32_bf16 v[48:51], a[128:131], a[8:11], v[48:51]\n"
  "ds_read_b64_tr_b16 a[152:153], v227 offset:16384\n"
  "ds_read_b64_tr_b16 a[154:155], v227 offset:18432\n"
  "v_mfma_f32_16x16x32_bf16 v[48:51], a[132:135], a[12:15], v[48:51]\n"
  "buffer_load_dwordx4 v239, s[12:15], s26 offen offset:3072 lds\n"
  "s_waitcnt lgkmcnt(6)\n"
  "v_mfma_f32_16x16x32_bf16 v[36:39], a[136:139], a[0:3], v[36:39]\n"
  "ds_read_b64_tr_b16 a[156:157], v227 offset:20480\n"
  "ds_read_b64_tr_b16 a[158:159], v227 offset:22528\n"
  "v_mfma_f32_16x16x32_bf16 v[36:39], a[140:143], a[4:7], v[36:39]\n"
  "v_mfma_f32_16x16x32_bf16 v[52:55], a[136:139], a[8:11], v[52:55]\n"
  "ds_read_b64_tr_b16 a[128:129], v224 offset:24576\n"
  "ds_read_b64_tr_b16 a[130:131], v224 offset:26624\n"
  "v_mfma_f32_16x16x32_bf16 v[52:55], a[140:143], a[12:15], v[52:55]\n"
  "buffer_load_dword v108, v107, s[16:19], s60 offen offset:512 sc1\n"
  "s_waitcnt lgkmcnt(6)\n"
  "v_mfma_f32_16x16x32_bf16 v[40:43], a[144:147], a[0:3], v[40:43]\n"
  "ds_read_b64_tr_b16 a[132:133], v224 offset:28672\n"
  "ds_read_b64_tr_b16 a[134:135], v224 offset:30720\n"
  "v_mfma_f32_16x16x32_bf16 v[40:43], a[148:151], a[4:7], v[40:43]\n"
  "v_mfma_f32_16x16x32_bf16 v[56:59], a[144:147], a[8:11], v[56:59]\n"
  "ds_read_b64_tr_b16 a[136:137], v225 offset:24576\n"
  "ds_read_b64_tr_b16 a[138:139], v225 offset:26624\n"
  "v_mfma_f32_16x16x32_bf16 v[56:59], a[148:151], a[12:15], v[56:59]\n"
  "s_add_u32 s26, s80, 1048576\n"
  "s_add_u32 m0, s24, 49152\n"
  "s_waitcnt lgkmcnt(6)\n"
  "v_mfma_f32_16x16x32_bf16 v[44:47], a[152:155], a[0:3], v[44:47]\n"
  "ds_read_b64_tr_b16 a[140:141], v225 offset:28672\n"
  "ds_read_b64_tr_b16 a[142:143], v225 offset:30720\n"
  "v_mfma_f32_16x16x32_bf16 v[44:47], a[156:159], a[4:7], v[44:47]\n"
  "buffer_load_dwordx4 v236, s[12:15], s26 offen lds\n"
  "v_mfma_f32_16x16x32_bf16 v[60:63], a[152:155], a[8:11], v[60:63]\n"
  "ds_read_b64_tr_b16 a[144:145], v226 offset:24576\n"
  "ds_read_b64_tr_b16 a[146:147], v226 offset:26624\n"
  "v_mfma_f32_16x16x32_bf16 v[60:63], a[156:159], a[12:15], v[60:63]\n"
  "s_waitcnt vmcnt(9) lgkmcnt(6)\n"
  "v_mfma_f32_16x16x32_bf16 v[32:35], a[128:131], a[16:19], v[32:35]\n"
  "ds_read_b64_tr_b16 a[148:149], v226 offset:28672\n"
  "ds_read_b64_tr_b16 a[150:151], v226 offset:30720\n"
  "v_mfma_f32_16x16x32_bf16 v[32:35], a[132:135], a[20:23], v[32:35]\n"
  "buffer_load_dwordx2 v[252:253], v249, s[16:19], s31 offen sc1\n"
  "v_mfma_f32_16x16x32_bf16 v[48:51], a[128:131], a[24:27], v[48:51]\n"
  "ds_read_b64_tr_b16 a[152:153], v227 offset:24576\n"
  "ds_read_b64_tr_b16 a[154:155], v227 offset:26624\n"
  "v_mfma_f32_16x16x32_bf16 v[48:51], a[132:135], a[28:31], v[48:51]\n"
  "s_waitcnt lgkmcnt(6)\n"
  "s_barrier\n"
  "v_mfma_f32_16x16x32_bf16 v[36:39], a[136:139], a[16:19], v[36:39]\n"
  "ds_read_b64_tr_b16 a[156:157], v227 offset:28672\n"
  "ds_read_b64_tr_b16 a[158:159], v227 offset:30720\n"
  "v_mfma_f32_16x16x32_bf16 v[36:39], a[140:143], a[20:23], v[36:39]\n"
  "buffer_load_dwordx4 v237, s[12:15], s26 offen offset:1024 lds\n"
  "v_mfma_f32_16x16x32_bf16 v[52:55], a[136:139], a[24:27], v[52:55]\n"
  "v_add_u32 v224, 0xfffe0000, v224\n"
  "ds_read_b64_tr_b16 a[128:129], v224 offset:0\n"
  "v_mfma_f32_16x16x32_bf16 v[52:55], a[140:143], a[28:31], v[52:55]\n"
  "s_waitcnt lgkmcnt(5)\n"
  "v_mfma_f32_16x16x32_bf16 v[40:43], a[144:147], a[16:19], v[40:43]\n"
  "ds_read_b64_tr_b16 a[130:131], v224 offset:2048\n"
  "ds_read_b64_tr_b16 a[132:133], v224 offset:4096\n"
  "v_mfma_f32_16x16x32_bf16 v[40:43], a[148:151], a[20:23], v[40:43]\n"
  "ds_read_b64_tr_b16 a[134:135], v224 offset:6144\n"
  "v_add_u32 v225, 0xfffe0000, v225\n"
  "ds_read_b64_tr_b16 a[136:137], v225 offset:0\n"
  "v_mfma_f32_16x16x32_bf16 v[56:59], a[144:147], a[24:27], v[56:59]\n"
  "buffer_load_dwordx4 v238, s[12:15], s26 offen offset:2048 lds\n"
  "v_mfma_f32_16x16x32_bf16 v[56:59], a[148:151], a[28:31], v[56:59]\n"
  "ds_read_b64_tr_b16 a[138:139], v225 offset:2048\n"
  "s_waitcnt lgkmcnt(6)\n"
  "v_mfma_f32_16x16x32_bf16 v[44:47], a[152:155], a[16:19], v[44:47]\n"
  "ds_read_b64_tr_b16 a[140:141], v225 offset:4096\n"
  "ds_read_b64_tr_b16 a[142:143], v225 offset:6144\n"
  "v_mfma_f32_16x16x32_bf16 v[44:47], a[156:159], a[20:23], v[44:47]\n"
  "v_mfma_f32_16x16x32_bf16 v[60:63], a[152:155], a[24:27], v[60:63]\n"
  "v_add_u32 v226, 0xfffe0000, v226\n"
  "ds_read_b64_tr_b16 a[144:145], v226 offset:0\n"
  "v_mfma_f32_16x16x32_bf16 v[60:63], a[156:159], a[28:31], v[60:63]\n"
  "buffer_load_dwordx4 v239, s[12:15], s26 offen offset:3072 lds\n"
  "s_waitcnt lgkmcnt(5)\n"
  "v_mfma_f32_16x16x32_bf16 v[64:67], a[128:131], a[160:163], 0\n"
  "ds_read_b64_tr_b16 a[146:147], v226 offset:2048\n"
  "ds_read_b64_tr_b16 a[148:149], v226 offset:4096\n"
  "v_mfma_f32_16x16x32_bf16 v[64:67], a[132:135], a[164:167], v[64:67]\n"
  "ds_read_b64_tr_b16 a[150:151], v226 offset:6144\n"
  "v_add_u32 v227, 0xfffe0000, v227\n"
  "ds_read_b64_tr_b16 a[152:153], v227 offset:0\n"
  "v_mfma_f32_16x16x32_bf16 v[80:83], a[128:131], a[168:171], 0\n"
  "v_mfma_f32_16x16x32_bf16 v[80:83], a[132:135], a[172:175], v[80:83]\n"
  "ds_read_b64_tr_b16 a[154:155], v227 offset:2048\n"
  "s_waitcnt lgkmcnt(6)\n"
  "v_mfma_f32_16x16x32_bf16 v[68:71], a[136:139], a[160:163], 0\n"
  "ds_read_b64_tr_b16 a[156:157], v227 offset:4096\n"
  "ds_read_b64_tr_b16 a[158:159], v227 offset:6144\n"
  "v_mfma_f32_16x16x32_bf16 v[68:71], a[140:143], a[164:167], v[68:71]\n"
  "buffer_load_dword v109, v107, s[16:19], s60 offen offset:768 sc1\n"
  "v_mfma_f32_16x16x32_bf16 v[84:87], a[136:139], a[168:171], 0\n"
  "ds_read_b64_tr_b16 a[128:129], v224 offset:8192\n"
  "ds_read_b64_tr_b16 a[130:131], v224 offset:10240\n"
  "v_mfma_f32_16x16x32_bf16 v[84:87], a[140:143], a[172:175], v[84:87]\n"
  "s_waitcnt lgkmcnt(6)\n"
  "v_mfma_f32_16x16x32_bf16 v[72:75], a[144:147], a[160:163], 0\n"
  "ds_read_b64_tr_b16 a[132:133], v224 offset:12288\n"
  "ds_read_b64_tr_b16 a[134:135], v224 offset:14336\n"
  "v_mfma_f32_16x16x32_bf16 v[72:75], a[148:151], a[164:167], v[72:75]\n"
  "s_add_u32 s26, s81, 1572864\n"
  "s_add_u32 m0, s24, 65536\n"
  "v_mfma_f32_16x16x32_bf16 v[88:91], a[144:147], a[168:171], 0\n"
  "ds_read_b64_tr_b16 a[136:137], v225 offset:8192\n"
  "ds_read_b64_tr_b16 a[138:139], v225 offset:10240\n"
  "v_mfma_f32_16x16x32_bf16 v[88:91], a[148:151], a[172:175], v[88:91]\n"
  "buffer_load_dwordx4 v236, s[12:15], s26 offen lds\n"
  "s_waitcnt lgkmcnt(6)\n"
  "v_mfma_f32_16x16x32_bf16 v[76:79], a[152:155], a[160:163], 0\n"
  "ds_read_b64_tr_b16 a[140:141], v225 offset:12288\n"
  "ds_read_b64_tr_b16 a[142:143], v225 offset:14336\n"
  "v_mfma_f32_16x16x32_bf16 v[76:79], a[156:159], a[164:167], v[76:79]\n"
  "v_mfma_f32_16x16x32_bf16 v[92:95], a[152:155], a[168:171], 0\n"
  "ds_read_b64_tr_b16 a[144:145], v226 offset:8192\n"
  "ds_read_b64_tr_b16 a[146:147], v226 offset:10240\n"
  "v_mfma_f32_16x16x32_bf16 v[92:95], a[156:159], a[172:175], v[92:95]\n"
  "buffer_load_dwordx4 v237, s[12:15], s26 offen offset:1024 lds\n"
  "s_waitcnt lgkmcnt(6)\n"
  "v_mfma_f32_16x16x32_bf16 v[64:67], a[128:131], a[176:179], v[64:67]\n"
  "ds_read_b64_tr_b16 a[148:149], v226 offset:12288\n"
  "ds_read_b64_tr_b16 a[150:151], v226 offset:14336\n"
  "v_mfma_f32_16x16x32_bf16 v[64:67], a[132:135], a[180:183], v[64:67]\n"
  "v_mfma_f32_16x16x32_bf16 v[80:83], a[128:131], a[184:187], v[80:83]\n"
  "ds_read_b64_tr_b16 a[152:153], v227 offset:8192\n"
  "ds_read_b64_tr_b16 a[154:155], v227 offset:10240\n"
  "v_mfma_f32_16x16x32_bf16 v[80:83], a[132:135], a[188:191], v[80:83]\n"
  "s_waitcnt lgkmcnt(6)\n"
  "s_barrier\n"
  "v_mfma_f32_16x16x32_bf16 v[68:71], a[136:139], a[176:179], v[68:71]\n"
  "ds_read_b64_tr_b16 a[156:157], v227 offset:12288\n"
  "ds_read_b64_tr_b16 a[158:159], v227 offset:14336\n"
  "v_mfma_f32_16x16x32_bf16 v[68:71], a[140:143], a[180:183], v[68:71]\n"
  "buffer_load_dwordx4 v238, s[12:15], s26 offen offset:2048 lds\n"
  "v_mfma_f32_16x16x32_bf16 v[84:87], a[136:139], a[184:187], v[84:87]\n"
  "ds_read_b64_tr_b16 a[128:129], v224 offset:16384\n"
  "ds_read_b64_tr_b16 a[130:131], v224 offset:18432\n"
  "v_mfma_f32_16x16x32_bf16 v[84:87], a[140:143], a[188:191], v[84:87]\n"
  "s_waitcnt lgkmcnt(6)\n"
  "v_mfma_f32_16x16x32_bf16 v[72:75], a[144:147], a[176:179], v[72:75]\n"
  "ds_read_b64_tr_b16 a[132:133], v224 offset:20480\n"
  "ds_read_b64_tr_b16 a[134:135], v224 offset:22528\n"
  "v_mfma_f32_16x16x32_bf16 v[72:75], a[148:151], a[180:183], v[72:75]\n"
  "buffer_load_dwordx4 v239, s[12:15], s26 offen offset:3072 lds\n"
  "v_mfma_f32_16x16x32_bf16 v[88:91], a[144:147], a[184:187], v[88:91]\n"
  "ds_read_b64_tr_b16 a[136:137], v225 offset:16384\n"
  "ds_read_b64_tr_b16 a[138:139], v225 offset:18432\n"
  "v_mfma_f32_16x16x32_bf16 v[88:91], a[148:151], a[188:191], v[88:91]\n"
  "s_waitcnt lgkmcnt(6)\n"
  "v_mfma_f32_16x16x32_bf16 v[76:79], a[152:155], a[176:179], v[76:79]\n"
  "ds_read_b64_tr_b16 a[140:141], v225 offset:20480\n"
  "ds_read_b64_tr_b16 a[142:143], v225 offset:22528\n"
  "v_mfma_f32_16x16x32_bf16 v[76:79], a[156:159], a[180:183], v[76:79]\n"
  "s_waitcnt vmcnt(8)\n"
  "v_cmp_ne_u64 vcc, v[252:253], v[250:251]\n"
  "s_cbranch_vccnz SLP2_%=\n"
  "BKP2_%=:\n"
  "s_add_u32 s31, s31, 4096\n"
  "v_mfma_f32_16x16x32_bf16 v[92:95], a[152:155], a[184:187], v[92:95]\n"
  "ds_read_b64_tr_b16 a[144:145], v226 offset:16384\n"
  "ds_read_b64_tr_b16 a[146:147], v226 offset:18432\n"
  "v_mfma_f32_16x16x32_bf16 v[92:95], a[156:159], a[188:191], v[92:95]\n"
  "s_add_u32 s57, s76, 32768\n"
  "s_waitcnt lgkmcnt(6)\n"
  "v_mfma_f32_16x16x32_bf16 v[64:67], a[128:131], a[192:195], v[64:67]\n"
  "ds_read_b64_tr_b16 a[148:149], v226 offset:20480\n"
  "ds_read_b64_tr_b16 a[150:151], v226 offset:22528\n"
  "v_mfma_f32_16x16x32_bf16 v[64:67], a[132:135], a[196:199], v[64:67]\n"
  "buffer_load_dwordx4 a[32:35], v244, s[16:19], s57 offen offset:0 sc1\n"
  "v_mfma_f32_16x16x32_bf16 v[80:83], a[128:131], a[200:203], v[80:83]\n"
  "ds_read_b64_tr_b16 a[152:153], v227 offset:16384\n"
  "ds_read_b64_tr_b16 a[154:155], v227 offset:18432\n"
  "v_mfma_f32_16x16x32_bf16 v[80:83], a[132:135], a[204:207], v[80:83]\n"
  "buffer_load_dwordx4 a[40:43], v245, s[16:19], s57 offen offset:0 sc1\n"
  "s_waitcnt lgkmcnt(6)\n"
  "v_mfma_f32_16x16x32_bf16 v[68:71], a[136:139], a[192:195], v[68:71]\n"
  "ds_read_b64_tr_b16 a[156:157], v227 offset:20480\n"
  "ds_read_b64_tr_b16 a[158:159], v227 offset:22528\n"
  "v_mfma_f32_16x16x32_bf16 v[68:71], a[140:143], a[196:199], v[68:71]\n"
  "buffer_load_dwordx4 a[36:39], v244, s[16:19], s57 offen offset:1024 sc1\n"
  "v_mfma_f32_16x16x32_bf16 v[84:87], a[136:139], a[200:203], v[84:87]\n"
  "ds_read_b64_tr_b16 a[128:129], v224 offset:24576\n"
  "ds_read_b64_tr_b16 a[130:131], v224 offset:26624\n"
  "v_mfma_f32_16x16x32_bf16 v[84:87], a[140:143], a[204:207], v[84:87]\n"
  "buffer_load_dwordx4 a[44:47], v245, s[16:19], s57 offen offset:1024 sc1\n"
  "s_waitcnt lgkmcnt(6)\n"
  "v_mfma_f32_16x16x32_bf16 v[72:75], a[144:147], a[192:195], v[72:75]\n"
  "ds_read_b64_tr_b16 a[132:133], v224 offset:28672\n"
  "ds_read_b64_tr_b16 a[134:135], v224 offset:30720\n"
  "v_mfma_f32_16x16x32_bf16 v[72:75], a[148:151], a[196:199], v[72:75]\n"
  "v_mfma_f32_16x16x32_bf16 v[88:91], a[144:147], a[200:203], v[88:91]\n"
  "ds_read_b64_tr_b16 a[136:137], v225 offset:24576\n"
  "ds_read_b64_tr_b16 a[138:139], v225 offset:26624\n"
  "v_mfma_f32_16x16x32_bf16 v[88:91], a[148:151], a[204:207], v[88:91]\n"
  "buffer_load_dwordx4 a[48:51], v244, s[16:19], s57 offen offset:2048 sc1\n"
  "s_waitcnt lgkmcnt(6)\n"
  "v_mfma_f32_16x16x32_bf16 v[76:79], a[152:155], a[192:195], v[76:79]\n"
  "ds_read_b64_tr_b16 a[140:141], v225 offset:28672\n"
  "ds_read_b64_tr_b16 a[142:143], v225 offset:30720\n"
  "v_mfma_f32_16x16x32_bf16 v[76:79], a[156:159], a[196:199], v[76:79]\n"
  "v_mfma_f32_16x16x32_bf16 v[92:95], a[152:155], a[200:203], v[92:95]\n"
  "ds_read_b64_tr_b16 a[144:145], v226 offset:24576\n"
  "ds_read_b64_tr_b16 a[146:147], v226 offset:26624\n"
  "v_mfma_f32_16x16x32_bf16 v[92:95], a[156:159], a[204:207], v[92:95]\n"
  "buffer_load_dwordx4 a[56:59], v245, s[16:19], s57 offen offset:2048 sc1\n"
  "s_waitcnt lgkmcnt(6)\n"
  "v_mfma_f32_16x16x32_bf16 v[64:67], a[128:131], a[208:211], v[64:67]\n"
  "ds_read_b64_tr_b16 a[148:149], v226 offset:28672\n"
  "ds_read_b64_tr_b16 a[150:151], v226 offset:30720\n"
  "v_mfma_f32_16x16x32_bf16 v[64:67], a[132:135], a[212:215], v[64:67]\n"
  "buffer_load_dwordx2 v[252:253], v249, s[16:19], s31 offen sc1\n"
  "v_mfma_f32_16x16x32_bf16 v[80:83], a[128:131], a[216:219], v[80:83]\n"
  "ds_read_b64_tr_b16 a[152:153], v227 offset:24576\n"
  "ds_read_b64_tr_b16 a[154:155], v227 offset:26624\n"
  "v_mfma_f32_16x16x32_bf16 v[80:83], a[132:135], a[220:223], v[80:83]\n"
  "buffer_load_dwordx4 a[52:55], v244, s[16:19], s57 offen offset:3072 sc1\n"
  "s_waitcnt lgkmcnt(6)\n"
  "s_barrier\n"
  "v_mfma_f32_16x16x32_bf16 v[68:71], a[136:139], a[208:211], v[68:71]\n"
  "ds_read_b64_tr_b16 a[156:157], v227 offset:28672\n"
  "ds_read_b64_tr_b16 a[158:159], v227 offset:30720\n"
  "v_mfma_f32_16x16x32_bf16 v[68:71], a[140:143], a[212:215], v[68:71]\n"
  "buffer_load_dwordx4 a[60:63], v245, s[16:19], s57 offen offset:3072 sc1\n"
  "v_mfma_f32_16x16x32_bf16 v[84:87], a[136:139], a[216:219], v[84:87]\n"
  "ds_read_b64_tr_b16 a[128:129], v224 offset:32768\n"
  "ds_read_b64_tr_b16 a[130:131], v224 offset:34816\n"
  "v_mfma_f32_16x16x32_bf16 v[84:87], a[140:143], a[220:223], v[84:87]\n"
  "s_waitcnt lgkmcnt(6)\n"
  "v_mfma_f32_16x16x32_bf16 v[72:75], a[144:147], a[208:211], v[72:75]\n"
  "ds_read_b64_tr_b16 a[132:133], v224 offset:36864\n"
  "ds_read_b64_tr_b16 a[134:135], v224 offset:38912\n"
  "v_mfma_f32_16x16x32_bf16 v[72:75], a[148:151], a[212:215], v[72:75]\n"
  "s_add_u32 s57, s77, 32768\n"
  "v_mfma_f32_16x16x32_bf16 v[88:91], a[144:147], a[216:219], v[88:91]\n"
  "ds_read_b64_tr_b16 a[136:137], v225 offset:32768\n"
  "ds_read_b64_tr_b16 a[138:139], v225 offset:34816\n"
  "v_mfma_f32_16x16x32_bf16 v[88:91], a[148:151], a[220:223], v[88:91]\n"
  "buffer_load_dwordx4 a[64:67], v244, s[16:19], s57 offen offset:0 sc1\n"
  "s_waitcnt lgkmcnt(6)\n"
  "v_mfma_f32_16x16x32_bf16 v[76:79], a[152:155], a[208:211], v[76:79]\n"
  "ds_read_b64_tr_b16 a[140:141], v225 offset:36864\n"
  "ds_read_b64_tr_b16 a[142:143], v225 offset:38912\n"
  "v_mfma_f32_16x16x32_bf16 v[76:79], a[156:159], a[212:215], v[76:79]\n"
  "v_mfma_f32_16x16x32_bf16 v[92:95], a[152:155], a[216:219], v[92:95]\n"
  "ds_read_b64_tr_b16 a[144:145], v226 offset:32768\n"
  "ds_read_b64_tr_b16 a[146:147], v226 offset:34816\n"
  "v_mfma_f32_16x16x32_bf16 v[92:95], a[156:159], a[220:223], v[92:95]\n"
  "buffer_load_dwordx4 a[72:75], v245, s[16:19], s57 offen offset:0 sc1\n"
  "buffer_load_dwordx4 a[68:71], v244, s[16:19], s57 offen offset:1024 sc1\n"
  "buffer_load_dwordx4 a[76:79], v245, s[16:19], s57 offen offset:1024 sc1\n"
  "s_waitcnt vmcnt(9) lgkmcnt(6)\n"
  "v_mfma_f32_16x16x32_bf16 v[32:35], a[128:131], a[32:35], v[32:35]\n"
  "ds_read_b64_tr_b16 a[148:149], v226 offset:36864\n"
  "ds_read_b64_tr_b16 a[150:151], v226 offset:38912\n"
  "v_mfma_f32_16x16x32_bf16 v[32:35], a[132:135], a[36:39], v[32:35]\n"
  "buffer_load_dwordx4 a[80:83], v244, s[16:19], s57 offen offset:2048 sc1\n"
  "v_mfma_f32_16x16x32_bf16 v[48:51], a[128:131], a[40:43], v[48:51]\n"
  "ds_read_b64_tr_b16 a[152:153], v227 offset:32768\n"
  "ds_read_b64_tr_b16 a[154:155], v227 offset:34816\n"
  "v_mfma_f32_16x16x32_bf16 v[48:51], a[132:135], a[44:47], v[48:51]\n"
  "s_waitcnt lgkmcnt(6)\n"
  "v_mfma_f32_16x16x32_bf16 v[36:39], a[136:139], a[32:35], v[36:39]\n"
  "ds_read_b64_tr_b16 a[156:157], v227 offset:36864\n"
  "ds_read_b64_tr_b16 a[158:159], v227 offset:38912\n"
  "v_mfma_f32_16x16x32_bf16 v[36:39], a[140:143], a[36:39], v[36:39]\n"
  "buffer_load_dwordx4 a[88:91], v245, s[16:19], s57 offen offset:2048 sc1\n"
  "v_mfma_f32_16x16x32_bf16 v[52:55], a[136:139], a[40:43], v[52:55]\n"
  "ds_read_b64_tr_b16 a[128:129], v224 offset:40960\n"
  "ds_read_b64_tr_b16 a[130:131], v224 offset:43008\n"
  "v_mfma_f32_16x16x32_bf16 v[52:55], a[140:143], a[44:47], v[52:55]\n"
  "s_waitcnt lgkmcnt(6)\n"
  "v_mfma_f32_16x16x32_bf16 v[40:43], a[144:147], a[32:35], v[40:43]\n"
  "ds_read_b64_tr_b16 a[132:133], v224 offset:45056\n"
  "ds_read_b64_tr_b16 a[134:135], v224 offset:47104\n"
  "v_mfma_f32_16x16x32_bf16 v[40:43], a[148:151], a[36:39], v[40:43]\n"
  "s_waitcnt vmcnt(8)\n"
  "v_cmp_ne_u64 vcc, v[252:253], v[250:251]\n"
  "s_cbranch_vccnz SLS_%=\n"
  "BKS_%=:\n"
  "s_add_u32 s31, s31, 4096\n"
  "v_mfma_f32_16x16x32_bf16 v[56:59], a[144:147], a[40:43], v[56:59]\n"
  "ds_read_b64_tr_b16 a[136:137], v225 offset:40960\n"
  "ds_read_b64_tr_b16 a[138:139], v225 offset:43008\n"
  "v_mfma_f32_16x16x32_bf16 v[56:59], a[148:151], a[44:47], v[56:59]\n"
  "buffer_load_dwordx4 a[84:87], v244, s[16:19], s57 offen offset:3072 sc1\n"
  "s_waitcnt lgkmcnt(6)\n"
  "v_mfma_f32_16x16x32_bf16 v[44:47], a[152:155], a[32:35], v[44:47]\n"
  "ds_read_b64_tr_b16 a[140:141], v225 offset:45056\n"
  "ds_read_b64_tr_b16 a[142:143], v225 offset:47104\n"
  "v_mfma_f32_16x16x32_bf16 v[44:47], a[156:159], a[36:39], v[44:47]\n"
  "v_mfma_f32_16x16x32_bf16 v[60:63], a[152:155], a[40:43], v[60:63]\n"
  "ds_read_b64_tr_b16 a[144:145], v226 offset:40960\n"
  "ds_read_b64_tr_b16 a[146:147], v226 offset:43008\n"
  "v_mfma_f32_16x16x32_bf16 v[60:63], a[156:159], a[44:47], v[60:63]\n"
  "buffer_load_dwordx4 a[92:95], v245, s[16:19], s57 offen offset:3072 sc1\n"
  "s_add_u32 s57, s78, 49152\n"
  "buffer_load_dwordx4 a[96:99], v244, s[16:19], s57 offen offset:0 sc1\n"
  "s_waitcnt vmcnt(9) lgkmcnt(6)\n"
  "v_mfma_f32_16x16x32_bf16 v[32:35], a[128:131], a[48:51], v[32:35]\n"
  "ds_read_b64_tr_b16 a[148:149], v226 offset:45056\n"
  "ds_read_b64_tr_b16 a[150:151], v226 offset:47104\n"
  "v_mfma_f32_16x16x32_bf16 v[32:35], a[132:135], a[52:55], v[32:35]\n"
  "buffer_load_dwordx4 a[104:107], v245, s[16:19], s57 offen offset:0 sc1\n"
  "v_mfma_f32_16x16x32_bf16 v[48:51], a[128:131], a[56:59], v[48:51]\n"
  "ds_read_b64_tr_b16 a[152:153], v227 offset:40960\n"
  "ds_read_b64_tr_b16 a[154:155], v227 offset:43008\n"
  "v_mfma_f32_16x16x32_bf16 v[48:51], a[132:135], a[60:63], v[48:51]\n"
  "s_waitcnt lgkmcnt(6)\n"
  "s_barrier\n"
  "v_mfma_f32_16x16x32_bf16 v[36:39], a[136:139], a[48:51], v[36:39]\n"
  "ds_read_b64_tr_b16 a[156:157], v227 offset:45056\n"
  "ds_read_b64_tr_b16 a[158:159], v227 offset:47104\n"
  "v_mfma_f32_16x16x32_bf16 v[36:39], a[140:143], a[52:55], v[36:39]\n"
  "buffer_load_dwordx4 a[100:103], v244, s[16:19], s57 offen offset:1024 sc1\n"
  "v_mfma_f32_16x16x32_bf16 v[52:55], a[136:139], a[56:59], v[52:55]\n"
  "ds_read_b64_tr_b16 a[128:129], v224 offset:49152\n"
  "ds_read_b64_tr_b16 a[130:131], v224 offset:51200\n"
  "v_mfma_f32_16x16x32_bf16 v[52:55], a[140:143], a[60:63], v[52:55]\n"
  "s_waitcnt lgkmcnt(6)\n"
  "v_mfma_f32_16x16x32_bf16 v[40:43], a[144:147], a[48:51], v[40:43]\n"
  "ds_read_b64_tr_b16 a[132:133], v224 offset:53248\n"
  "ds_read_b64_tr_b16 a[134:135], v224 offset:55296\n"
  "v_mfma_f32_16x16x32_bf16 v[40:43], a[148:151], a[52:55], v[40:43]\n"
  "buffer_load_dwordx4 a[108:111], v245, s[16:19], s57 offen offset:1024 sc1\n"
  "v_lshrrev_b32 v120, 4, v244\n"
  "v_mfma_f32_16x16x32_bf16 v[56:59], a[144:147], a[56:59], v[56:59]\n"
  "ds_read_b64_tr_b16 a[136:137], v225 offset:49152\n"
  "ds_read_b64_tr_b16 a[138:139], v225 offset:51200\n"
  "v_mfma_f32_16x16x32_bf16 v[56:59], a[148:151], a[60:63], v[56:59]\n"
  "v_bfe_u32 v121, v120, 2, 2\n"
  "s_waitcnt lgkmcnt(6)\n"
  "v_mfma_f32_16x16x32_bf16 v[44:47], a[152:155], a[48:51], v[44:47]\n"
  "ds_read_b64_tr_b16 a[140:141], v225 offset:53248\n"
  "ds_read_b64_tr_b16 a[142:143], v225 offset:55296\n"
  "v_mfma_f32_16x16x32_bf16 v[44:47], a[156:159], a[52:55], v[44:47]\n"
  "buffer_load_dwordx4 a[112:115], v244, s[16:19], s57 offen offset:2048 sc1\n"
  "v_lshrrev_b32 v122, 4, v120\n"
  "v_mfma_f32_16x16x32_bf16 v[60:63], a[152:155], a[56:59], v[60:63]\n"
  "ds_read_b64_tr_b16 a[144:145], v226 offset:49152\n"
  "ds_read_b64_tr_b16 a[146:147], v226 offset:51200\n"
  "v_mfma_f32_16x16x32_bf16 v[60:63], a[156:159], a[60:63], v[60:63]\n"
  "v_cmp_eq_u32 s[62:63], v121, v122\n"
  "s_waitcnt vmcnt(9) lgkmcnt(6)\n"
  "v_mfma_f32_16x16x32_bf16 v[32:35], a[128:131], a[64:67], v[32:35]\n"
  "ds_read_b64_tr_b16 a[148:149], v226 offset:53248\n"
  "ds_read_b64_tr_b16 a[150:151], v226 offset:55296\n"
  "v_mfma_f32_16x16x32_bf16 v[32:35], a[132:135], a[68:71], v[32:35]\n"
  "buffer_load_dwordx4 a[120:123], v245, s[16:19], s57 offen offset:2048 sc1\n"
  "v_and_b32 v121, 1, v120\n"
  "v_mfma_f32_16x16x32_bf16 v[48:51], a[128:131], a[72:75], v[48:51]\n"
  "ds_read_b64_tr_b16 a[152:153], v227 offset:49152\n"
  "ds_read_b64_tr_b16 a[154:155], v227 offset:51200\n"
  "v_mfma_f32_16x16x32_bf16 v[48:51], a[132:135], a[76:79], v[48:51]\n"
  "v_lshlrev_b32 v121, 4, v121\n"
  "s_waitcnt lgkmcnt(6)\n"
  "v_mfma_f32_16x16x32_bf16 v[36:39], a[136:139], a[64:67], v[36:39]\n"
  "ds_read_b64_tr_b16 a[156:157], v227 offset:53248\n"
  "ds_read_b64_tr_b16 a[158:159], v227 offset:55296\n"
  "v_mfma_f32_16x16x32_bf16 v[36:39], a[140:143], a[68:71], v[36:39]\n"
  "buffer_load_dwordx4 a[116:119], v244, s[16:19], s57 offen offset:3072 sc1\n"
  "v_mov_b32 v123, 0x3f80\n"
  "v_mfma_f32_16x16x32_bf16 v[52:55], a[136:139], a[72:75], v[52:55]\n"
  "ds_read_b64_tr_b16 a[128:129], v224 offset:57344\n"
  "ds_read_b64_tr_b16 a[130:131], v224 offset:59392\n"
  "v_mfma_f32_16x16x32_bf16 v[52:55], a[140:143], a[76:79], v[52:55]\n"
  "v_lshlrev_b32 v121, v121, v123\n"
  "s_waitcnt lgkmcnt(6)\n"
  "v_mfma_f32_16x16x32_bf16 v[40:43], a[144:147], a[64:67], v[40:43]\n"
  "FP6_%=:\n"
  "s_cbranch_execz FPE_%=\n"
  "ds_read_b64_tr_b16 a[132:133], v224 offset:61440\n"
  "ds_read_b64_tr_b16 a[134:135], v224 offset:63488\n"
  "v_mfma_f32_16x16x32_bf16 v[40:43], a[148:151], a[68:71], v[40:43]\n"
  "buffer_load_dwordx4 a[124:127], v245, s[16:19], s57 offen offset:3072 sc1\n"
  "v_cndmask_b32 v121, 0, v121, s[62:63]\n"
  "v_mfma_f32_16x16x32_bf16 v[56:59], a[144:147], a[72:75], v[56:59]\n"
  "ds_read_b64_tr_b16 a[136:137], v225 offset:57344\n"
  "ds_read_b64_tr_b16 a[138:139], v225 offset:59392\n"
  "v_mfma_f32_16x16x32_bf16 v[56:59], a[148:151], a[76:79], v[56:59]\n"
  "v_and_b32 v123, 2, v120\n"
  "s_waitcnt lgkmcnt(6)\n"
  "v_mfma_f32_16x16x32_bf16 v[44:47], a[152:155], a[64:67], v[44:47]\n"
  "ds_read_b64_tr_b16 a[140:141], v225 offset:61440\n"
  "ds_read_b64_tr_b16 a[142:143], v225 offset:63488\n"
  "v_mfma_f32_16x16x32_bf16 v[44:47], a[156:159], a[68:71], v[44:47]\n"
  "s_add_u32 s26, s86, 524288\n"
  "s_add_u32 m0, s24, 81920\n"
  "v_mfma_f32_16x16x32_bf16 v[60:63], a[152:155], a[72:75], v[60:63]\n"
  "ds_read_b64_tr_b16 a[144:145], v226 offset:57344\n"
  "ds_read_b64_tr_b16 a[146:147], v226 offset:59392\n"
  "v_mfma_f32_16x16x32_bf16 v[60:63], a[156:159], a[76:79], v[60:63]\n"
  "buffer_load_dwordx4 v236, s[12:15], s26 offen lds\n"
  "s_waitcnt vmcnt(9) lgkmcnt(6)\n"
  "v_mfma_f32_16x16x32_bf16 v[32:35], a[128:131], a[80:83], v[32:35]\n"
  "ds_read_b64_tr_b16 a[148:149], v226 offset:61440\n"
  "ds_read_b64_tr_b16 a[150:151], v226 offset:63488\n"
  "v_mfma_f32_16x16x32_bf16 v[32:35], a[132:135], a[84:87], v[32:35]\n"
  "v_cmp_eq_u32 s[62:63], 0, v123\n"
  "v_mov_b32 v96, 0\n"
  "v_mfma_f32_16x16x32_bf16 v[48:51], a[128:131], a[88:91], v[48:51]\n"
  "ds_read_b64_tr_b16 a[152:153], v227 offset:57344\n"
  "ds_read_b64_tr_b16 a[154:155], v227 offset:59392\n"
  "v_mfma_f32_16x16x32_bf16 v[48:51], a[132:135], a[92:95], v[48:51]\n"
  "s_waitcnt lgkmcnt(6)\n"
  "s_barrier\n"
  "v_mfma_f32_16x16x32_bf16 v[36:39], a[136:139], a[80:83], v[36:39]\n"
  "ds_read_b64_tr_b16 a[156:157], v227 offset:61440\n"
  "ds_read_b64_tr_b16 a[158:159], v227 offset:63488\n"
  "v_mfma_f32_16x16x32_bf16 v[36:39], a[140:143], a[84:87], v[36:39]\n"
  "buffer_load_dwordx4 v237, s[12:15], s26 offen offset:1024 lds\n"
  "v_mov_b32 v97, 0\n"
  "v_mfma_f32_16x16x32_bf16 v[52:55], a[136:139], a[88:91], v[52:55]\n"
  "v_add_u32 v224, 0x10000, v224\n"
  "ds_read_b64_tr_b16 a[128:129], v224 offset:0\n"
  "v_mfma_f32_16x16x32_bf16 v[52:55], a[140:143], a[92:95], v[52:55]\n"
  "buffer_load_dwordx4 v238, s[12:15], s26 offen offset:2048 lds\n"
  "s_waitcnt lgkmcnt(5)\n"
  "v_mfma_f32_16x16x32_bf16 v[40:43], a[144:147], a[80:83], v[40:43]\n"
  "ds_read_b64_tr_b16 a[130:131], v224 offset:2048\n"
  "ds_read_b64_tr_b16 a[132:133], v224 offset:4096\n"
  "v_mfma_f32_16x16x32_bf16 v[40:43], a[148:151], a[84:87], v[40:43]\n"
  "ds_read_b64_tr_b16 a[134:135], v224 offset:6144\n"
  "v_add_u32 v225, 0x10000, v225\n"
  "ds_read_b64_tr_b16 a[136:137], v225 offset:0\n"
  "v_mfma_f32_16x16x32_bf16 v[56:59], a[144:147], a[88:91], v[56:59]\n"
  "buffer_load_dwordx4 v239, s[12:15], s26 offen offset:3072 lds\n"
  "v_mov_b32 v100, 0\n"
  "v_mfma_f32_16x16x32_bf16 v[56:59], a[148:151], a[92:95], v[56:59]\n"
  "ds_read_b64_tr_b16 a[138:139], v225 offset:2048\n"
  "s_waitcnt lgkmcnt(6)\n"
  "v_mfma_f32_16x16x32_bf16 v[44:47], a[152:155], a[80:83], v[44:47]\n"
  "ds_read_b64_tr_b16 a[140:141], v225 offset:4096\n"
  "ds_read_b64_tr_b16 a[142:143], v225 offset:6144\n"
  "v_mfma_f32_16x16x32_bf16 v[44:47], a[156:159], a[84:87], v[44:47]\n"
  "s_add_u32 s26, s87, 524288\n"
  "s_add_u32 m0, s24, 98304\n"
  "v_mfma_f32_16x16x32_bf16 v[60:63], a[152:155], a[88:91], v[60:63]\n"
  "v_add_u32 v226, 0x10000, v226\n"
  "ds_read_b64_tr_b16 a[144:145], v226 offset:0\n"
  "v_mfma_f32_16x16x32_bf16 v[60:63], a[156:159], a[92:95], v[60:63]\n"
  "buffer_load_dwordx4 v236, s[12:15], s26 offen lds\n"
  "v_mov_b32 v101, 0\n"
  "v_cndmask_b32 v98, 0, v121, s[62:63]\n"
  "s_waitcnt vmcnt(9) lgkmcnt(5)\n"
  "v_mfma_f32_16x16x32_bf16 v[32:35], a[128:131], a[96:99], v[32:35]\n"
  "ds_read_b64_tr_b16 a[146:147], v226 offset:2048\n"
  "ds_read_b64_tr_b16 a[148:149], v226 offset:4096\n"
  "v_mfma_f32_16x16x32_bf16 v[32:35], a[132:135], a[100:103], v[32:35]\n"
  "ds_read_b64_tr_b16 a[150:151], v226 offset:6144\n"
  "v_add_u32 v227, 0x10000, v227\n"
  "ds_read_b64_tr_b16 a[152:153], v227 offset:0\n"
  "v_mfma_f32_16x16x32_bf16 v[48:51], a[128:131], a[104:107], v[48:51]\n"
  "buffer_load_dwordx4 v237, s[12:15], s26 offen offset:1024 lds\n"
  "v_sub_u32 v99, v121, v98\n"
  "v_mfma_f32_16x16x32_bf16 v[48:51], a[132:135], a[108:111], v[48:51]\n"
  "ds_read_b64_tr_b16 a[154:155], v227 offset:2048\n"
  "s_waitcnt lgkmcnt(6)\n"
  "v_mfma_f32_16x16x32_bf16 v[36:39], a[136:139], a[96:99], v[36:39]\n"
  "ds_read_b64_tr_b16 a[156:157], v227 offset:4096\n"
  "ds_read_b64_tr_b16 a[158:159], v227 offset:6144\n"
  "v_mfma_f32_16x16x32_bf16 v[36:39], a[140:143], a[100:103], v[36:39]\n"
  "v_mov_b32 v112, 0\n"
  "v_mov_b32 v113, 0\n"
  "v_mfma_f32_16x16x32_bf16 v[52:55], a[136:139], a[104:107], v[52:55]\n"
  "ds_read_b64_tr_b16 a[128:129], v224 offset:8192\n"
  "ds_read_b64_tr_b16 a[130:131], v224 offset:10240\n"
  "v_mfma_f32_16x16x32_bf16 v[52:55], a[140:143], a[108:111], v[52:55]\n"
  "buffer_load_dwordx4 v238, s[12:15], s26 offen offset:2048 lds\n"
  "s_waitcnt lgkmcnt(6)\n"
  "v_mfma_f32_16x16x32_bf16 v[40:43], a[144:147], a[96:99], v[40:43]\n"
  "ds_read_b64_tr_b16 a[132:133], v224 offset:12288\n"
  "ds_read_b64_tr_b16 a[134:135], v224 offset:14336\n"
  "v_mfma_f32_16x16x32_bf16 v[40:43], a[148:151], a[100:103], v[40:43]\n"
  "v_mov_b32 v116, 0\n"
  "v_mov_b32 v117, 0\n"
  "v_mfma_f32_16x16x32_bf16 v[56:59], a[144:147], a[104:107], v[56:59]\n"
  "ds_read_b64_tr_b16 a[136:137], v225 offset:8192\n"
  "ds_read_b64_tr_b16 a[138:139], v225 offset:10240\n"
  "v_mfma_f32_16x16x32_bf16 v[56:59], a[148:151], a[108:111], v[56:59]\n"
  "buffer_load_dwordx4 v239, s[12:15], s26 offen offset:3072 lds\n"
  "s_waitcnt lgkmcnt(6)\n"
  "v_mfma_f32_16x16x32_bf16 v[44:47], a[152:155], a[96:99], v[44:47]\n"
  "ds_read_b64_tr_b16 a[140:141], v225 offset:12288\n"
  "ds_read_b64_tr_b16 a[142:143], v225 offset:14336\n"
  "v_mfma_f32_16x16x32_bf16 v[44:47], a[156:159], a[100:103], v[44:47]\n"
  "v_xor_b32 v114, 0x80008000, v98\n"
  "v_xor_b32 v115, 0x80008000, v99\n"
  "v_mfma_f32_16x16x32_bf16 v[60:63], a[152:155], a[104:107], v[60:63]\n"
  "ds_read_b64_tr_b16 a[144:145], v226 offset:8192\n"
  "ds_read_b64_tr_b16 a[146:147], v226 offset:10240\n"
  "v_mfma_f32_16x16x32_bf16 v[60:63], a[156:159], a[108:111], v[60:63]\n"
  "v_and_b32 v121, 15, v120\n"
  "v_lshlrev_b32 v121, 10, v121\n"
  "v_and_b32 v123, 1, v122\n"
  "v_lshl_add_u32 v121, v123, 5, v121\n"
  "v_lshrrev_b32 v122, 1, v122\n"
  "v_lshl_add_u32 v102, v122, 4, v121\n"
  "v_add_f32 v104, v104, v105\n"
  "v_add_f32 v108, v108, v109\n"
  "v_mov_b32 v105, v104\n"
  "v_mov_b32 v109, v108\n"
  "s_waitcnt vmcnt(8) lgkmcnt(6)\n"
  "v_mfma_f32_16x16x32_bf16 v[32:35], a[128:131], a[112:115], v[32:35]\n"
  "ds_read_b64_tr_b16 a[148:149], v226 offset:12288\n"
  "ds_read_b64_tr_b16 a[150:151], v226 offset:14336\n"
  "v_mfma_f32_16x16x32_bf16 v[32:35], a[132:135], a[116:119], v[32:35]\n"
  "s_add_u32 s26, s86, 1048576\n"
  "s_add_u32 m0, s24, 114688\n"
  "v_mfma_f32_16x16x32_bf16 v[48:51], a[128:131], a[120:123], v[48:51]\n"
  "ds_read_b64_tr_b16 a[152:153], v227 offset:8192\n"
  "ds_read_b64_tr_b16 a[154:155], v227 offset:10240\n"
  "v_mfma_f32_16x16x32_bf16 v[48:51], a[132:135], a[124:127], v[48:51]\n"
  "buffer_load_dwordx4 v236, s[12:15], s26 offen lds\n"
  "s_nop 0\n"
  "v_permlane16_swap_b32 v104, v105\n"
  "v_permlane16_swap_b32 v108, v109\n"
  "v_add_f32 v104, v104, v105\n"
  "v_add_f32 v108, v108, v109\n"
  "v_mov_b32 v105, v104\n"
  "v_mov_b32 v109, v108\n"
  "s_nop 0\n"
  "v_permlane32_swap_b32 v104, v105\n"
  "v_permlane32_swap_b32 v108, v109\n"
  "v_add_f32 v104, v104, v105\n"
  "v_add_f32 v108, v108, v109\n"
  "v_mul_f32 v104, s25, v104\n"
  "v_mul_f32 v108, s25, v108\n"
  "buffer_load_dwordx4 v237, s[12:15], s26 offen offset:1024 lds\n"
  "v_exp_f32 v104, v104\n"
  "v_exp_f32 v108, v108\n"
  "v_add_f32 v104, 1.0, v104\n"
  "v_add_f32 v108, 1.0, v108\n"
  "v_rcp_f32 v104, v104\n"
  "v_rcp_f32 v108, v108\n"
  "v_sub_f32 v105, 1.0, v104\n"
  "v_sub_f32 v109, 1.0, v108\n"
  "s_waitcnt vmcnt(6) lgkmcnt(6)\n"
  "s_barrier\n"
  "v_mfma_f32_16x16x32_bf16 v[36:39], a[136:139], a[112:115], v[36:39]\n"
  "ds_read_b64_tr_b16 a[156:157], v227 offset:12288\n"
  "ds_read_b64_tr_b16 a[158:159], v227 offset:14336\n"
  "v_mfma_f32_16x16x32_bf16 v[36:39], a[140:143], a[116:119], v[36:39]\n"
  "buffer_load_dwordx4 v238, s[12:15], s26 offen offset:2048 lds\n"
  "v_mfma_f32_16x16x32_bf16 v[52:55], a[136:139], a[120:123], v[52:55]\n"
  "ds_read_b64_tr_b16 a[128:129], v224 offset:16384\n"
  "ds_read_b64_tr_b16 a[130:131], v224 offset:18432\n"
  "v_mfma_f32_16x16x32_bf16 v[52:55], a[140:143], a[124:127], v[52:55]\n"
  "s_waitcnt lgkmcnt(6)\n"
  "v_mfma_f32_16x16x32_bf16 v[40:43], a[144:147], a[112:115], v[40:43]\n"
  "ds_read_b64_tr_b16 a[132:133], v224 offset:20480\n"
  "ds_read_b64_tr_b16 a[134:135], v224 offset:22528\n"
  "v_mfma_f32_16x16x32_bf16 v[40:43], a[148:151], a[116:119], v[40:43]\n"
  "buffer_load_dwordx4 v239, s[12:15], s26 offen offset:3072 lds\n"
  "v_cvt_pk_bf16_f32 v32, v32, v33\n"
  "v_cvt_pk_bf16_f32 v33, v34, v35\n"
  "v_cvt_pk_bf16_f32 v34, v36, v37\n"
  "v_mfma_f32_16x16x32_bf16 v[56:59], a[144:147], a[120:123], v[56:59]\n"
  "ds_read_b64_tr_b16 a[136:137], v225 offset:16384\n"
  "ds_read_b64_tr_b16 a[138:139], v225 offset:18432\n"
  "v_cvt_pk_bf16_f32 v35, v38, v39\n"
  "buffer_store_dwordx4 v[32:35], v244, s[16:19], s89 offen offset:0 sc1\n"
  "v_mfma_f32_16x16x32_bf16 v[56:59], a[148:151], a[124:127], v[56:59]\n"
  "v_cvt_pk_bf16_f32 v48, v48, v49\n"
  "v_cvt_pk_bf16_f32 v49, v50, v51\n"
  "v_cvt_pk_bf16_f32 v50, v52, v53\n"
  "s_waitcnt lgkmcnt(6)\n"
  "v_mfma_f32_16x16x32_bf16 v[44:47], a[152:155], a[112:115], v[44:47]\n"
  "ds_read_b64_tr_b16 a[140:141], v225 offset:20480\n"
  "ds_read_b64_tr_b16 a[142:143], v225 offset:22528\n"
  "v_cvt_pk_bf16_f32 v51, v54, v55\n"
  "buffer_store_dwordx4 v[48:51], v244, s[16:19], s89 offen offset:2048 sc1\n"
  "v_mfma_f32_16x16x32_bf16 v[44:47], a[156:159], a[116:119], v[44:47]\n"
  "v_mfma_f32_16x16x32_bf16 v[60:63], a[152:155], a[120:123], v[60:63]\n"
  "ds_read_b64_tr_b16 a[144:145], v226 offset:16384\n"
  "ds_read_b64_tr_b16 a[146:147], v226 offset:18432\n"
  "v_mfma_f32_16x16x32_bf16 v[60:63], a[156:159], a[124:127], v[60:63]\n"
  "s_add_u32 s26, s87, 1048576\n"
  "s_add_u32 m0, s24, 131072\n"
  "s_waitcnt lgkmcnt(6)\n"
  "v_mfma_f32_16x16x32_bf16 v[64:67], a[128:131], a[224:227], v[64:67]\n"
  "ds_read_b64_tr_b16 a[148:149], v226 offset:20480\n"
  "ds_read_b64_tr_b16 a[150:151], v226 offset:22528\n"
  "v_mfma_f32_16x16x32_bf16 v[64:67], a[132:135], a[228:231], v[64:67]\n"
  "buffer_load_dwordx4 v236, s[12:15], s26 offen lds\n"
  "v_cvt_pk_bf16_f32 v40, v40, v41\n"
  "v_cvt_pk_bf16_f32 v41, v42, v43\n"
  "v_cvt_pk_bf16_f32 v42, v44, v45\n"
  "v_mfma_f32_16x16x32_bf16 v[80:83], a[128:131], a[232:235], v[80:83]\n"
  "ds_read_b64_tr_b16 a[152:153], v227 offset:16384\n"
  "ds_read_b64_tr_b16 a[154:155], v227 offset:18432\n"
  "v_cvt_pk_bf16_f32 v43, v46, v47\n"
  "buffer_store_dwordx4 v[40:43], v244, s[16:19], s89 offen offset:1024 sc1\n"
  "v_mfma_f32_16x16x32_bf16 v[80:83], a[132:135], a[236:239], v[80:83]\n"
  "v_cvt_pk_bf16_f32 v56, v56, v57\n"
  "v_cvt_pk_bf16_f32 v57, v58, v59\n"
  "v_cvt_pk_bf16_f32 v58, v60, v61\n"
  "s_waitcnt lgkmcnt(6)\n"
  "v_mfma_f32_16x16x32_bf16 v[68:71], a[136:139], a[224:227], v[68:71]\n"
  "ds_read_b64_tr_b16 a[156:157], v227 offset:20480\n"
  "ds_read_b64_tr_b16 a[158:159], v227 offset:22528\n"
  "v_cvt_pk_bf16_f32 v59, v62, v63\n"
  "buffer_store_dwordx4 v[56:59], v244, s[16:19], s89 offen offset:3072 sc1\n"
  "v_mfma_f32_16x16x32_bf16 v[68:71], a[140:143], a[228:231], v[68:71]\n"
  "v_mfma_f32_16x16x32_bf16 v[84:87], a[136:139], a[232:235], v[84:87]\n"
  "ds_read_b64_tr_b16 a[128:129], v224 offset:24576\n"
  "ds_read_b64_tr_b16 a[130:131], v224 offset:26624\n"
  "v_mfma_f32_16x16x32_bf16 v[84:87], a[140:143], a[236:239], v[84:87]\n"
  "s_waitcnt lgkmcnt(6)\n"
  "v_mfma_f32_16x16x32_bf16 v[72:75], a[144:147], a[224:227], v[72:75]\n"
  "ds_read_b64_tr_b16 a[132:133], v224 offset:28672\n"
  "ds_read_b64_tr_b16 a[134:135], v224 offset:30720\n"
  "v_mfma_f32_16x16x32_bf16 v[72:75], a[148:151], a[228:231], v[72:75]\n"
  "buffer_load_dwordx4 v237, s[12:15], s26 offen offset:1024 lds\n"
  "v_mfma_f32_16x16x32_bf16 v[88:91], a[144:147], a[232:235], v[88:91]\n"
  "ds_read_b64_tr_b16 a[136:137], v225 offset:24576\n"
  "ds_read_b64_tr_b16 a[138:139], v225 offset:26624\n"
  "v_mfma_f32_16x16x32_bf16 v[88:91], a[148:151], a[236:239], v[88:91]\n"
  "s_waitcnt lgkmcnt(6)\n"
  "v_mfma_f32_16x16x32_bf16 v[76:79], a[152:155], a[224:227], v[76:79]\n"
  "ds_read_b64_tr_b16 a[140:141], v225 offset:28672\n"
  "ds_read_b64_tr_b16 a[142:143], v225 offset:30720\n"
  "v_mfma_f32_16x16x32_bf16 v[76:79], a[156:159], a[228:231], v[76:79]\n"
  "buffer_load_dwordx4 v238, s[12:15], s26 offen offset:2048 lds\n"
  "v_mfma_f32_16x16x32_bf16 v[92:95], a[152:155], a[232:235], v[92:95]\n"
  "ds_read_b64_tr_b16 a[144:145], v226 offset:24576\n"
  "ds_read_b64_tr_b16 a[146:147], v226 offset:26624\n"
  "v_mfma_f32_16x16x32_bf16 v[92:95], a[156:159], a[236:239], v[92:95]\n"
  "s_waitcnt lgkmcnt(6)\n"
  "v_mfma_f32_16x16x32_bf16 v[64:67], a[128:131], a[240:243], v[64:67]\n"
  "ds_read_b64_tr_b16 a[148:149], v226 offset:28672\n"
  "ds_read_b64_tr_b16 a[150:151], v226 offset:30720\n"
  "v_mfma_f32_16x16x32_bf16 v[64:67], a[132:135], a[244:247], v[64:67]\n"
  "buffer_load_dwordx4 v239, s[12:15], s26 offen offset:3072 lds\n"
  "v_mfma_f32_16x16x32_bf16 v[80:83], a[128:131], a[248:251], v[80:83]\n"
  "ds_read_b64_tr_b16 a[152:153], v227 offset:24576\n"
  "ds_read_b64_tr_b16 a[154:155], v227 offset:26624\n"
  "v_mfma_f32_16x16x32_bf16 v[80:83], a[132:135], a[252:255], v[80:83]\n"
  "s_waitcnt vmcnt(12) lgkmcnt(6)\n"
  "s_barrier\n"
  "v_mfma_f32_16x16x32_bf16 v[68:71], a[136:139], a[240:243], v[68:71]\n"
  "ds_read_b64_tr_b16 a[156:157], v227 offset:28672\n"
  "ds_read_b64_tr_b16 a[158:159], v227 offset:30720\n"
  "v_mfma_f32_16x16x32_bf16 v[68:71], a[140:143], a[244:247], v[68:71]\n"
  "s_add_u32 s26, s88, 1572864\n"
  "s_add_u32 m0, s24, 147456\n"
  "v_mfma_f32_16x16x32_bf16 v[84:87], a[136:139], a[248:251], v[84:87]\n"
  "ds_read_b64_tr_b16 a[128:129], v224 offset:32768\n"
  "ds_read_b64_tr_b16 a[130:131], v224 offset:34816\n"
  "v_mfma_f32_16x16x32_bf16 v[84:87], a[140:143], a[252:255], v[84:87]\n"
  "buffer_load_dwordx4 v236, s[12:15], s26 offen lds\n"
  "s_waitcnt lgkmcnt(6)\n"
  "v_mfma_f32_16x16x32_bf16 v[72:75], a[144:147], a[240:243], v[72:75]\n"
  "ds_read_b64_tr_b16 a[132:133], v224 offset:36864\n"
  "ds_read_b64_tr_b16 a[134:135], v224 offset:38912\n"
  "v_mfma_f32_16x16x32_bf16 v[72:75], a[148:151], a[244:247], v[72:75]\n"
  "v_mfma_f32_16x16x32_bf16 v[88:91], a[144:147], a[248:251], v[88:91]\n"
  "ds_read_b64_tr_b16 a[136:137], v225 offset:32768\n"
  "ds_read_b64_tr_b16 a[138:139], v225 offset:34816\n"
  "v_mfma_f32_16x16x32_bf16 v[88:91], a[148:151], a[252:255], v[88:91]\n"
  "buffer_load_dwordx4 v237, s[12:15], s26 offen offset:1024 lds\n"
  "s_waitcnt lgkmcnt(6)\n"
  "v_mfma_f32_16x16x32_bf16 v[76:79], a[152:155], a[240:243], v[76:79]\n"
  "ds_read_b64_tr_b16 a[140:141], v225 offset:36864\n"
  "ds_read_b64_tr_b16 a[142:143], v225 offset:38912\n"
  "v_mfma_f32_16x16x32_bf16 v[76:79], a[156:159], a[244:247], v[76:79]\n"
  "v_mfma_f32_16x16x32_bf16 v[92:95], a[152:155], a[248:251], v[92:95]\n"
  "ds_read_b64_tr_b16 a[144:145], v226 offset:32768\n"
  "ds_read_b64_tr_b16 a[146:147], v226 offset:34816\n"
  "v_mfma_f32_16x16x32_bf16 v[92:95], a[156:159], a[252:255], v[92:95]\n"
  "buffer_load_dwordx4 v238, s[12:15], s26 offen offset:2048 lds\n"
  "s_waitcnt lgkmcnt(6)\n"
  "v_mfma_f32_16x16x32_bf16 v[64:67], a[128:131], a[0:3], v[64:67]\n"
  "ds_read_b64_tr_b16 a[148:149], v226 offset:36864\n"
  "ds_read_b64_tr_b16 a[150:151], v226 offset:38912\n"
  "v_mfma_f32_16x16x32_bf16 v[64:67], a[132:135], a[4:7], v[64:67]\n"
  "v_mfma_f32_16x16x32_bf16 v[80:83], a[128:131], a[8:11], v[80:83]\n"
  "ds_read_b64_tr_b16 a[152:153], v227 offset:32768\n"
  "ds_read_b64_tr_b16 a[154:155], v227 offset:34816\n"
  "v_mfma_f32_16x16x32_bf16 v[80:83], a[132:135], a[12:15], v[80:83]\n"
  "buffer_load_dwordx4 v239, s[12:15], s26 offen offset:3072 lds\n"
  "s_waitcnt lgkmcnt(6)\n"
  "v_mfma_f32_16x16x32_bf16 v[68:71], a[136:139], a[0:3], v[68:71]\n"
  "ds_read_b64_tr_b16 a[156:157], v227 offset:36864\n"
  "ds_read_b64_tr_b16 a[158:159], v227 offset:38912\n"
  "v_mfma_f32_16x16x32_bf16 v[68:71], a[140:143], a[4:7], v[68:71]\n"
  "v_mfma_f32_16x16x32_bf16 v[84:87], a[136:139], a[8:11], v[84:87]\n"
  "ds_read_b64_tr_b16 a[128:129], v224 offset:40960\n"
  "ds_read_b64_tr_b16 a[130:131], v224 offset:43008\n"
  "v_mfma_f32_16x16x32_bf16 v[84:87], a[140:143], a[12:15], v[84:87]\n"
  "s_waitcnt lgkmcnt(6)\n"
  "v_mfma_f32_16x16x32_bf16 v[72:75], a[144:147], a[0:3], v[72:75]\n"
  "ds_read_b64_tr_b16 a[132:133], v224 offset:45056\n"
  "ds_read_b64_tr_b16 a[134:135], v224 offset:47104\n"
  "v_mfma_f32_16x16x32_bf16 v[72:75], a[148:151], a[4:7], v[72:75]\n"
  "v_mfma_f32_16x16x32_bf16 v[88:91], a[144:147], a[8:11], v[88:91]\n"
  "ds_read_b64_tr_b16 a[136:137], v225 offset:40960\n"
  "ds_read_b64_tr_b16 a[138:139], v225 offset:43008\n"
  "v_mfma_f32_16x16x32_bf16 v[88:91], a[148:151], a[12:15], v[88:91]\n"
  "s_waitcnt lgkmcnt(6)\n"
  "v_mfma_f32_16x16x32_bf16 v[76:79], a[152:155], a[0:3], v[76:79]\n"
  "ds_read_b64_tr_b16 a[140:141], v225 offset:45056\n"
  "ds_read_b64_tr_b16 a[142:143], v225 offset:47104\n"
  "v_mfma_f32_16x16x32_bf16 v[76:79], a[156:159], a[4:7], v[76:79]\n"
  "v_mfma_f32_16x16x32_bf16 v[92:95], a[152:155], a[8:11], v[92:95]\n"
  "ds_read_b64_tr_b16 a[144:145], v226 offset:40960\n"
  "ds_read_b64_tr_b16 a[146:147], v226 offset:43008\n"
  "v_mfma_f32_16x16x32_bf16 v[92:95], a[156:159], a[12:15], v[92:95]\n"
  "s_waitcnt lgkmcnt(6)\n"
  "v_mfma_f32_16x16x32_bf16 v[64:67], a[128:131], a[16:19], v[64:67]\n"
  "ds_read_b64_tr_b16 a[148:149], v226 offset:45056\n"
  "ds_read_b64_tr_b16 a[150:151], v226 offset:47104\n"
  "v_mfma_f32_16x16x32_bf16 v[64:67], a[132:135], a[20:23], v[64:67]\n"
  "v_mfma_f32_16x16x32_bf16 v[80:83], a[128:131], a[24:27], v[80:83]\n"
  "ds_read_b64_tr_b16 a[152:153], v227 offset:40960\n"
  "ds_read_b64_tr_b16 a[154:155], v227 offset:43008\n"
  "v_mfma_f32_16x16x32_bf16 v[80:83], a[132:135], a[28:31], v[80:83]\n"
  "s_waitcnt vmcnt(12) lgkmcnt(6)\n"
  "s_barrier\n"
  "v_mfma_f32_16x16x32_bf16 v[68:71], a[136:139], a[16:19], v[68:71]\n"
  "ds_read_b64_tr_b16 a[156:157], v227 offset:45056\n"
  "ds_read_b64_tr_b16 a[158:159], v227 offset:47104\n"
  "v_mfma_f32_16x16x32_bf16 v[68:71], a[140:143], a[20:23], v[68:71]\n"
  "v_mfma_f32_16x16x32_bf16 v[84:87], a[136:139], a[24:27], v[84:87]\n"
  "ds_read_b64_tr_b16 a[128:129], v224 offset:49152\n"
  "ds_read_b64_tr_b16 a[130:131], v224 offset:51200\n"
  "v_mfma_f32_16x16x32_bf16 v[84:87], a[140:143], a[28:31], v[84:87]\n"
  "s_waitcnt lgkmcnt(6)\n"
  "v_mfma_f32_16x16x32_bf16 v[72:75], a[144:147], a[16:19], v[72:75]\n"
  "ds_read_b64_tr_b16 a[132:133], v224 offset:53248\n"
  "ds_read_b64_tr_b16 a[134:135], v224 offset:55296\n"
  "v_mfma_f32_16x16x32_bf16 v[72:75], a[148:151], a[20:23], v[72:75]\n"
  "v_mfma_f32_16x16x32_bf16 v[88:91], a[144:147], a[24:27], v[88:91]\n"
  "ds_read_b64_tr_b16 a[136:137], v225 offset:49152\n"
  "ds_read_b64_tr_b16 a[138:139], v225 offset:51200\n"
  "v_mfma_f32_16x16x32_bf16 v[88:91], a[148:151], a[28:31], v[88:91]\n"
  "s_waitcnt lgkmcnt(6)\n"
  "v_mfma_f32_16x16x32_bf16 v[76:79], a[152:155], a[16:19], v[76:79]\n"
  "ds_read_b64_tr_b16 a[140:141], v225 offset:53248\n"
  "ds_read_b64_tr_b16 a[142:143], v225 offset:55296\n"
  "v_mfma_f32_16x16x32_bf16 v[76:79], a[156:159], a[20:23], v[76:79]\n"
  "s_waitcnt vmcnt(7)\n"
  "buffer_store_dwordx2 v[250:251], v248, s[16:19], s93 offen sc1\n"
  "v_mfma_f32_16x16x32_bf16 v[92:95], a[152:155], a[24:27], v[92:95]\n"
  "ds_read_b64_tr_b16 a[144:145], v226 offset:49152\n"
  "ds_read_b64_tr_b16 a[146:147], v226 offset:51200\n"
  "v_mfma_f32_16x16x32_bf16 v[92:95], a[156:159], a[28:31], v[92:95]\n"
  "s_waitcnt lgkmcnt(6)\n"
  "v_mfma_f32_16x16x32_bf16 v[64:67], a[128:131], a[32:35], v[64:67]\n"
  "ds_read_b64_tr_b16 a[148:149], v226 offset:53248\n"
  "ds_read_b64_tr_b16 a[150:151], v226 offset:55296\n"
  "v_mfma_f32_16x16x32_bf16 v[64:67], a[132:135], a[36:39], v[64:67]\n"
  "v_mfma_f32_16x16x32_bf16 v[80:83], a[128:131], a[40:43], v[80:83]\n"
  "ds_read_b64_tr_b16 a[152:153], v227 offset:49152\n"
  "ds_read_b64_tr_b16 a[154:155], v227 offset:51200\n"
  "v_mfma_f32_16x16x32_bf16 v[80:83], a[132:135], a[44:47], v[80:83]\n"
  "s_waitcnt lgkmcnt(6)\n"
  "v_mfma_f32_16x16x32_bf16 v[68:71], a[136:139], a[32:35], v[68:71]\n"
  "ds_read_b64_tr_b16 a[156:157], v227 offset:53248\n"
  "ds_read_b64_tr_b16 a[158:159], v227 offset:55296\n"
  "v_mfma_f32_16x16x32_bf16 v[68:71], a[140:143], a[36:39], v[68:71]\n"
  "v_mfma_f32_16x16x32_bf16 v[84:87], a[136:139], a[40:43], v[84:87]\n"
  "ds_read_b64_tr_b16 a[128:129], v224 offset:57344\n"
  "ds_read_b64_tr_b16 a[130:131], v224 offset:59392\n"
  "v_mfma_f32_16x16x32_bf16 v[84:87], a[140:143], a[44:47], v[84:87]\n"
  "s_waitcnt lgkmcnt(6)\n"
  "v_mfma_f32_16x16x32_bf16 v[72:75], a[144:147], a[32:35], v[72:75]\n"
  "ds_read_b64_tr_b16 a[132:133], v224 offset:61440\n"
  "ds_read_b64_tr_b16 a[134:135], v224 offset:63488\n"
  "v_mfma_f32_16x16x32_bf16 v[72:75], a[148:151], a[36:39], v[72:75]\n"
  "v_mfma_f32_16x16x32_bf16 v[88:91], a[144:147], a[40:43], v[88:91]\n"
  "ds_read_b64_tr_b16 a[136:137], v225 offset:57344\n"
  "ds_read_b64_tr_b16 a[138:139], v225 offset:59392\n"
  "v_mfma_f32_16x16x32_bf16 v[88:91], a[148:151], a[44:47], v[88:91]\n"
  "s_waitcnt lgkmcnt(6)\n"
  "v_mfma_f32_16x16x32_bf16 v[76:79], a[152:155], a[32:35], v[76:79]\n"
  "ds_read_b64_tr_b16 a[140:141], v225 offset:61440\n"
  "ds_read_b64_tr_b16 a[142:143], v225 offset:63488\n"
  "v_mfma_f32_16x16x32_bf16 v[76:79], a[156:159], a[36:39], v[76:79]\n"
  "v_mfma_f32_16x16x32_bf16 v[92:95], a[152:155], a[40:43], v[92:95]\n"
  "ds_read_b64_tr_b16 a[144:145], v226 offset:57344\n"
  "ds_read_b64_tr_b16 a[146:147], v226 offset:59392\n"
  "v_mfma_f32_16x16x32_bf16 v[92:95], a[156:159], a[44:47], v[92:95]\n"
  "s_waitcnt lgkmcnt(6)\n"
  "v_mfma_f32_16x16x32_bf16 v[64:67], a[128:131], a[48:51], v[64:67]\n"
  "ds_read_b64_tr_b16 a[148:149], v226 offset:61440\n"
  "ds_read_b64_tr_b16 a[150:151], v226 offset:63488\n"
  "v_mfma_f32_16x16x32_bf16 v[64:67], a[132:135], a[52:55], v[64:67]\n"
  "v_mfma_f32_16x16x32_bf16 v[80:83], a[128:131], a[56:59], v[80:83]\n"
  "ds_read_b64_tr_b16 a[152:153], v227 offset:57344\n"
  "ds_read_b64_tr_b16 a[154:155], v227 offset:59392\n"
  "v_mfma_f32_16x16x32_bf16 v[80:83], a[132:135], a[60:63], v[80:83]\n"
  "s_waitcnt vmcnt(5) lgkmcnt(6)\n"
  "s_barrier\n"
  "v_mfma_f32_16x16x32_bf16 v[68:71], a[136:139], a[48:51], v[68:71]\n"
  "ds_read_b64_tr_b16 a[156:157], v227 offset:61440\n"
  "ds_read_b64_tr_b16 a[158:159], v227 offset:63488\n"
  "v_mfma_f32_16x16x32_bf16 v[68:71], a[140:143], a[52:55], v[68:71]\n"
  "v_mfma_f32_16x16x32_bf16 v[84:87], a[136:139], a[56:59], v[84:87]\n"
  "v_add_u32 v224, 0x10000, v224\n"
  "ds_read_b64_tr_b16 a[128:129], v224 offset:0\n"
  "v_mfma_f32_16x16x32_bf16 v[84:87], a[140:143], a[60:63], v[84:87]\n"
  "s_waitcnt lgkmcnt(5)\n"
  "v_mfma_f32_16x16x32_bf16 v[72:75], a[144:147], a[48:51], v[72:75]\n"
  "ds_read_b64_tr_b16 a[130:131], v224 offset:2048\n"
  "ds_read_b64_tr_b16 a[132:133], v224 offset:4096\n"
  "v_mfma_f32_16x16x32_bf16 v[72:75], a[148:151], a[52:55], v[72:75]\n"
  "ds_read_b64_tr_b16 a[134:135], v224 offset:6144\n"
  "v_add_u32 v225, 0x10000, v225\n"
  "ds_read_b64_tr_b16 a[136:137], v225 offset:0\n"
  "v_mfma_f32_16x16x32_bf16 v[88:91], a[144:147], a[56:59], v[88:91]\n"
  "v_mfma_f32_16x16x32_bf16 v[88:91], a[148:151], a[60:63], v[88:91]\n"
  "ds_read_b64_tr_b16 a[138:139], v225 offset:2048\n"
  "s_waitcnt lgkmcnt(6)\n"
  "v_mfma_f32_16x16x32_bf16 v[76:79], a[152:155], a[48:51], v[76:79]\n"
  "ds_read_b64_tr_b16 a[140:141], v225 offset:4096\n"
  "ds_read_b64_tr_b16 a[142:143], v225 offset:6144\n"
  "v_mfma_f32_16x16x32_bf16 v[76:79], a[156:159], a[52:55], v[76:79]\n"
  "v_mfma_f32_16x16x32_bf16 v[92:95], a[152:155], a[56:59], v[92:95]\n"
  "v_add_u32 v226, 0x10000, v226\n"
  "ds_read_b64_tr_b16 a[144:145], v226 offset:0\n"
  "v_mfma_f32_16x16x32_bf16 v[92:95], a[156:159], a[60:63], v[92:95]\n"
  "s_waitcnt lgkmcnt(5)\n"
  "v_mfma_f32_16x16x32_bf16 v[64:67], a[128:131], a[64:67], v[64:67]\n"
  "ds_read_b64_tr_b16 a[146:147], v226 offset:2048\n"
  "ds_read_b64_tr_b16 a[148:149], v226 offset:4096\n"
  "v_mfma_f32_16x16x32_bf16 v[64:67], a[132:135], a[68:71], v[64:67]\n"
  "ds_read_b64_tr_b16 a[150:151], v226 offset:6144\n"
  "v_add_u32 v227, 0x10000, v227\n"
  "ds_read_b64_tr_b16 a[152:153], v227 offset:0\n"
  "v_mfma_f32_16x16x32_bf16 v[80:83], a[128:131], a[72:75], v[80:83]\n"
  "v_mfma_f32_16x16x32_bf16 v[80:83], a[132:135], a[76:79], v[80:83]\n"
  "ds_read_b64_tr_b16 a[154:155], v227 offset:2048\n"
  "s_waitcnt lgkmcnt(6)\n"
  "v_mfma_f32_16x16x32_bf16 v[68:71], a[136:139], a[64:67], v[68:71]\n"
  "ds_read_b64_tr_b16 a[156:157], v227 offset:4096\n"
  "ds_read_b64_tr_b16 a[158:159], v227 offset:6144\n"
  "v_mfma_f32_16x16x32_bf16 v[68:71], a[140:143], a[68:71], v[68:71]\n"
  "v_mfma_f32_16x16x32_bf16 v[84:87], a[136:139], a[72:75], v[84:87]\n"
  "ds_read_b64_tr_b16 a[128:129], v224 offset:8192\n"
  "ds_read_b64_tr_b16 a[130:131], v224 offset:10240\n"
  "v_mfma_f32_16x16x32_bf16 v[84:87], a[140:143], a[76:79], v[84:87]\n"
  "s_waitcnt lgkmcnt(6)\n"
  "v_mfma_f32_16x16x32_bf16 v[72:75], a[144:147], a[64:67], v[72:75]\n"
  "ds_read_b64_tr_b16 a[132:133], v224 offset:12288\n"
  "ds_read_b64_tr_b16 a[134:135], v224 offset:14336\n"
  "v_mfma_f32_16x16x32_bf16 v[72:75], a[148:151], a[68:71], v[72:75]\n"
  "v_mfma_f32_16x16x32_bf16 v[88:91], a[144:147], a[72:75], v[88:91]\n"
  "ds_read_b64_tr_b16 a[136:137], v225 offset:8192\n"
  "ds_read_b64_tr_b16 a[138:139], v225 offset:10240\n"
  "v_mfma_f32_16x16x32_bf16 v[88:91], a[148:151], a[76:79], v[88:91]\n"
  "s_waitcnt lgkmcnt(6)\n"
  "v_mfma_f32_16x16x32_bf16 v[76:79], a[152:155], a[64:67], v[76:79]\n"
  "ds_read_b64_tr_b16 a[140:141], v225 offset:12288\n"
  "ds_read_b64_tr_b16 a[142:143], v225 offset:14336\n"
  "v_mfma_f32_16x16x32_bf16 v[76:79], a[156:159], a[68:71], v[76:79]\n"
  "v_mfma_f32_16x16x32_bf16 v[92:95], a[152:155], a[72:75], v[92:95]\n"
  "ds_read_b64_tr_b16 a[144:145], v226 offset:8192\n"
  "ds_read_b64_tr_b16 a[146:147], v226 offset:10240\n"
  "v_mfma_f32_16x16x32_bf16 v[92:95], a[156:159], a[76:79], v[92:95]\n"
  "s_waitcnt lgkmcnt(6)\n"
  "v_mfma_f32_16x16x32_bf16 v[64:67], a[128:131], a[80:83], v[64:67]\n"
  "ds_read_b64_tr_b16 a[148:149], v226 offset:12288\n"
  "ds_read_b64_tr_b16 a[150:151], v226 offset:14336\n"
  "v_mfma_f32_16x16x32_bf16 v[64:67], a[132:135], a[84:87], v[64:67]\n"
  "v_mfma_f32_16x16x32_bf16 v[80:83], a[128:131], a[88:91], v[80:83]\n"
  "ds_read_b64_tr_b16 a[152:153], v227 offset:8192\n"
  "ds_read_b64_tr_b16 a[154:155], v227 offset:10240\n"
  "v_mfma_f32_16x16x32_bf16 v[80:83], a[132:135], a[92:95], v[80:83]\n"
  "s_waitcnt vmcnt(1) lgkmcnt(6)\n"
  "s_barrier\n"
  "v_mfma_f32_16x16x32_bf16 v[68:71], a[136:139], a[80:83], v[68:71]\n"
  "ds_read_b64_tr_b16 a[156:157], v227 offset:12288\n"
  "ds_read_b64_tr_b16 a[158:159], v227 offset:14336\n"
  "v_mfma_f32_16x16x32_bf16 v[68:71], a[140:143], a[84:87], v[68:71]\n"
  "v_mfma_f32_16x16x32_bf16 v[84:87], a[136:139], a[88:91], v[84:87]\n"
  "ds_read_b64_tr_b16 a[128:129], v224 offset:16384\n"
  "ds_read_b64_tr_b16 a[130:131], v224 offset:18432\n"
  "v_mfma_f32_16x16x32_bf16 v[84:87], a[140:143], a[92:95], v[84:87]\n"
  "s_waitcnt lgkmcnt(6)\n"
  "v_mfma_f32_16x16x32_bf16 v[72:75], a[144:147], a[80:83], v[72:75]\n"
  "ds_read_b64_tr_b16 a[132:133], v224 offset:20480\n"
  "ds_read_b64_tr_b16 a[134:135], v224 offset:22528\n"
  "v_mfma_f32_16x16x32_bf16 v[72:75], a[148:151], a[84:87], v[72:75]\n"
  "v_mfma_f32_16x16x32_bf16 v[88:91], a[144:147], a[88:91], v[88:91]\n"
  "ds_read_b64_tr_b16 a[136:137], v225 offset:16384\n"
  "ds_read_b64_tr_b16 a[138:139], v225 offset:18432\n"
  "v_mfma_f32_16x16x32_bf16 v[88:91], a[148:151], a[92:95], v[88:91]\n"
  "s_waitcnt lgkmcnt(6)\n"
  "v_mfma_f32_16x16x32_bf16 v[76:79], a[152:155], a[80:83], v[76:79]\n"
  "ds_read_b64_tr_b16 a[140:141], v225 offset:20480\n"
  "ds_read_b64_tr_b16 a[142:143], v225 offset:22528\n"
  "v_mfma_f32_16x16x32_bf16 v[76:79], a[156:159], a[84:87], v[76:79]\n"
  "s_xor_b32 s93, s93, 256\n"
  "buffer_load_dwordx2 v[246:247], off, s[16:19], s93 sc1\n"
  "v_mfma_f32_16x16x32_bf16 v[92:95], a[152:155], a[88:91], v[92:95]\n"
  "ds_read_b64_tr_b16 a[144:145], v226 offset:16384\n"
  "ds_read_b64_tr_b16 a[146:147], v226 offset:18432\n"
  "v_mfma_f32_16x16x32_bf16 v[92:95], a[156:159], a[92:95], v[92:95]\n"
  "s_waitcnt lgkmcnt(6)\n"
  "v_mfma_f32_16x16x32_bf16 v[64:67], a[128:131], a[96:99], v[64:67]\n"
  "ds_read_b64_tr_b16 a[148:149], v226 offset:20480\n"
  "ds_read_b64_tr_b16 a[150:151], v226 offset:22528\n"
  "v_mfma_f32_16x16x32_bf16 v[64:67], a[132:135], a[100:103], v[64:67]\n"
  "v_mfma_f32_16x16x32_bf16 v[80:83], a[128:131], a[104:107], v[80:83]\n"
  "ds_read_b64_tr_b16 a[152:153], v227 offset:16384\n"
  "ds_read_b64_tr_b16 a[154:155], v227 offset:18432\n"
  "v_mfma_f32_16x16x32_bf16 v[80:83], a[132:135], a[108:111], v[80:83]\n"
  "s_waitcnt lgkmcnt(6)\n"
  "v_mfma_f32_16x16x32_bf16 v[68:71], a[136:139], a[96:99], v[68:71]\n"
  "ds_read_b64_tr_b16 a[156:157], v227 offset:20480\n"
  "ds_read_b64_tr_b16 a[158:159], v227 offset:22528\n"
  "v_mfma_f32_16x16x32_bf16 v[68:71], a[140:143], a[100:103], v[68:71]\n"
  "v_and_b32 v249, 0x18, v249\n"
  "buffer_load_dwordx2 v[252:253], v249, s[16:19], s72 offen sc1\n"
  "v_mfma_f32_16x16x32_bf16 v[84:87], a[136:139], a[104:107], v[84:87]\n"
  "ds_read_b64_tr_b16 a[128:129], v224 offset:24576\n"
  "ds_read_b64_tr_b16 a[130:131], v224 offset:26624\n"
  "v_mfma_f32_16x16x32_bf16 v[84:87], a[140:143], a[108:111], v[84:87]\n"
  "s_waitcnt lgkmcnt(6)\n"
  "v_mfma_f32_16x16x32_bf16 v[72:75], a[144:147], a[96:99], v[72:75]\n"
  "ds_read_b64_tr_b16 a[132:133], v224 offset:28672\n"
  "ds_read_b64_tr_b16 a[134:135], v224 offset:30720\n"
  "v_mfma_f32_16x16x32_bf16 v[72:75], a[148:151], a[100:103], v[72:75]\n"
  "v_mfma_f32_16x16x32_bf16 v[88:91], a[144:147], a[104:107], v[88:91]\n"
  "ds_read_b64_tr_b16 a[136:137], v225 offset:24576\n"
  "ds_read_b64_tr_b16 a[138:139], v225 offset:26624\n"
  "v_mfma_f32_16x16x32_bf16 v[88:91], a[148:151], a[108:111], v[88:91]\n"
  "s_waitcnt lgkmcnt(6)\n"
  "v_mfma_f32_16x16x32_bf16 v[76:79], a[152:155], a[96:99], v[76:79]\n"
  "ds_read_b64_tr_b16 a[140:141], v225 offset:28672\n"
  "FP7_%=:\n"
  "s_cbranch_execz FPE_%=\n"
  "ds_read_b64_tr_b16 a[142:143], v225 offset:30720\n"
  "v_mfma_f32_16x16x32_bf16 v[76:79], a[156:159], a[100:103], v[76:79]\n"
  "v_mfma_f32_16x16x32_bf16 v[92:95], a[152:155], a[104:107], v[92:95]\n"
  "ds_read_b64_tr_b16 a[144:145], v226 offset:24576\n"
  "ds_read_b64_tr_b16 a[146:147], v226 offset:26624\n"
  "v_mfma_f32_16x16x32_bf16 v[92:95], a[156:159], a[108:111], v[92:95]\n"
  "s_waitcnt lgkmcnt(6)\n"
  "v_mfma_f32_16x16x32_bf16 v[64:67], a[128:131], a[112:115], v[64:67]\n"
  "ds_read_b64_tr_b16 a[148:149], v226 offset:28672\n"
  "ds_read_b64_tr_b16 a[150:151], v226 offset:30720\n"
  "v_mfma_f32_16x16x32_bf16 v[64:67], a[132:135], a[116:119], v[64:67]\n"
  "v_mfma_f32_16x16x32_bf16 v[80:83], a[128:131], a[120:123], v[80:83]\n"
  "ds_read_b64_tr_b16 a[152:153], v227 offset:24576\n"
  "ds_read_b64_tr_b16 a[154:155], v227 offset:26624\n"
  "v_mfma_f32_16x16x32_bf16 v[80:83], a[132:135], a[124:127], v[80:83]\n"
  "s_waitcnt lgkmcnt(6)\n"
  "v_mfma_f32_16x16x32_bf16 v[68:71], a[136:139], a[112:115], v[68:71]\n"
  "ds_read_b64_tr_b16 a[156:157], v227 offset:28672\n"
  "ds_read_b64_tr_b16 a[158:159], v227 offset:30720\n"
  "v_mfma_f32_16x16x32_bf16 v[68:71], a[140:143], a[116:119], v[68:71]\n"
  "v_mfma_f32_16x16x32_bf16 v[84:87], a[136:139], a[120:123], v[84:87]\n"
  "v_mfma_f32_16x16x32_bf16 v[84:87], a[140:143], a[124:127], v[84:87]\n"
  "s_waitcnt lgkmcnt(4)\n"
  "v_mfma_f32_16x16x32_bf16 v[72:75], a[144:147], a[112:115], v[72:75]\n"
  "v_mfma_f32_16x16x32_bf16 v[72:75], a[148:151], a[116:119], v[72:75]\n"
  "v_mfma_f32_16x16x32_bf16 v[88:91], a[144:147], a[120:123], v[88:91]\n"
  "v_mfma_f32_16x16x32_bf16 v[88:91], a[148:151], a[124:127], v[88:91]\n"
  "s_waitcnt lgkmcnt(0)\n"
  "v_mfma_f32_16x16x32_bf16 v[76:79], a[152:155], a[112:115], v[76:79]\n"
  "v_mfma_f32_16x16x32_bf16 v[76:79], a[156:159], a[116:119], v[76:79]\n"
  "v_mfma_f32_16x16x32_bf16 v[92:95], a[152:155], a[120:123], v[92:95]\n"
  "v_mfma_f32_16x16x32_bf16 v[92:95], a[156:159], a[124:127], v[92:95]\n"
  "s_lshl_b32 s62, s74, 15\n"
  "s_lshl_b32 s63, s75, 7\n"
  "s_add_u32 s62, s62, s63\n"
  "s_add_u32 s63, s62, 16384\n"
  "s_waitcnt vmcnt(1)\n"
  "v_cmp_ne_u64 vcc, v[246:247], v[250:251]\n"
  "s_cbranch_vccnz SLE_%=\n"
  "BKE_%=:\n"
  "buffer_load_dwordx4 a[160:163], v244, s[16:19], s92 offen offset:0 sc1\n"
  "buffer_load_dwordx4 a[164:167], v244, s[16:19], s92 offen offset:1024 sc1\n"
  "buffer_load_dwordx4 a[168:171], v244, s[16:19], s92 offen offset:2048 sc1\n"
  "buffer_load_dwordx4 a[172:175], v244, s[16:19], s92 offen offset:3072 sc1\n"
  "s_waitcnt vmcnt(4)\n"
  "v_cmp_ne_u64 vcc, v[252:253], v[250:251]\n"
  "s_cbranch_vccnz SLY_%=\n"
  "BKY_%=:\n"
  "s_add_u32 s36, s59, 0\n"
  "buffer_load_dwordx4 a[176:179], v244, s[16:19], s36 offen offset:0 sc1\n"
  "s_add_u32 s37, s59, 4096\n"
  "buffer_load_dwordx4 a[180:183], v244, s[16:19], s37 offen offset:0 sc1\n"
  "s_add_u32 s38, s59, 8192\n"
  "buffer_load_dwordx4 a[184:187], v244, s[16:19], s38 offen offset:0 sc1\n"
  "s_add_u32 s39, s59, 12288\n"
  "buffer_load_dwordx4 a[188:191], v244, s[16:19], s39 offen offset:0 sc1\n"
  "buffer_load_dwordx4 a[192:195], v244, s[16:19], s36 offen offset:1024 sc1\n"
  "buffer_load_dwordx4 a[196:199], v244, s[16:19], s37 offen offset:1024 sc1\n"
  "buffer_load_dwordx4 a[200:203], v244, s[16:19], s38 offen offset:1024 sc1\n"
  "buffer_load_dwordx4 a[204:207], v244, s[16:19], s39 offen offset:1024 sc1\n"
  "buffer_load_dwordx4 a[208:211], v244, s[16:19], s36 offen offset:2048 sc1\n"
  "buffer_load_dwordx4 a[212:215], v244, s[16:19], s37 offen offset:2048 sc1\n"
  "buffer_load_dwordx4 a[216:219], v244, s[16:19], s38 offen offset:2048 sc1\n"
  "buffer_load_dwordx4 a[220:223], v244, s[16:19], s39 offen offset:2048 sc1\n"
  "buffer_load_dwordx4 a[224:227], v244, s[16:19], s36 offen offset:3072 sc1\n"
  "buffer_load_dwordx4 a[228:231], v244, s[16:19], s37 offen offset:3072 sc1\n"
  "buffer_load_dwordx4 a[232:235], v244, s[16:19], s38 offen offset:3072 sc1\n"
  "buffer_load_dwordx4 a[236:239], v244, s[16:19], s39 offen offset:3072 sc1\n"
  "s_waitcnt vmcnt(19)\n"
  "v_mfma_f32_16x16x32_bf16 v[64:67], v[98:101], a[160:163], v[64:67]\n"
  "v_mfma_f32_16x16x32_bf16 v[68:71], v[96:99], a[160:163], v[68:71]\n"
  "s_waitcnt vmcnt(18)\n"
  "v_mfma_f32_16x16x32_bf16 v[72:75], v[98:101], a[164:167], v[72:75]\n"
  "v_mfma_f32_16x16x32_bf16 v[76:79], v[96:99], a[164:167], v[76:79]\n"
  "s_waitcnt vmcnt(17)\n"
  "v_mfma_f32_16x16x32_bf16 v[80:83], v[98:101], a[168:171], v[80:83]\n"
  "v_mfma_f32_16x16x32_bf16 v[84:87], v[96:99], a[168:171], v[84:87]\n"
  "s_waitcnt vmcnt(16)\n"
  "v_mfma_f32_16x16x32_bf16 v[88:91], v[98:101], a[172:175], v[88:91]\n"
  "v_mfma_f32_16x16x32_bf16 v[92:95], v[96:99], a[172:175], v[92:95]\n"
  "v_mul_f32 v64, v105, v64\n"
  "v_mul_f32 v65, v105, v65\n"
  "v_mul_f32 v66, v105, v66\n"
  "v_mul_f32 v67, v105, v67\n"
  "v_mul_f32 v68, v105, v68\n"
  "v_mul_f32 v69, v105, v69\n"
  "v_mul_f32 v70, v105, v70\n"
  "v_mul_f32 v71, v105, v71\n"
  "v_mul_f32 v72, v105, v72\n"
  "v_mul_f32 v73, v105, v73\n"
  "v_mul_f32 v74, v105, v74\n"
  "v_mul_f32 v75, v105, v75\n"
  "v_mul_f32 v76, v105, v76\n"
  "v_mul_f32 v77, v105, v77\n"
  "v_mul_f32 v78, v105, v78\n"
  "v_mul_f32 v79, v105, v79\n"
  "v_mul_f32 v80, v109, v80\n"
  "v_mul_f32 v81, v109, v81\n"
  "v_mul_f32 v82, v109, v82\n"
  "v_mul_f32 v83, v109, v83\n"
  "v_mul_f32 v84, v109, v84\n"
  "v_mul_f32 v85, v109, v85\n"
  "v_mul_f32 v86, v109, v86\n"
  "v_mul_f32 v87, v109, v87\n"
  "v_mul_f32 v88, v109, v88\n"
  "v_mul_f32 v89, v109, v89\n"
  "v_mul_f32 v90, v109, v90\n"
  "v_mul_f32 v91, v109, v91\n"
  "v_mul_f32 v92, v109, v92\n"
  "v_mul_f32 v93, v109, v93\n"
  "v_mul_f32 v94, v109, v94\n"
  "v_mul_f32 v95, v109, v95\n"
  "s_waitcnt vmcnt(15)\n"
  "v_mfma_f32_16x16x32_bf16 v[128:131], v[98:101], a[176:179], 0\n"
  "v_mfma_f32_16x16x32_bf16 v[132:135], v[96:99], a[176:179], 0\n"
  "s_waitcnt vmcnt(14)\n"
  "v_mfma_f32_16x16x32_bf16 v[128:131], v[98:101], a[180:183], v[128:131]\n"
  "v_mfma_f32_16x16x32_bf16 v[132:135], v[96:99], a[180:183], v[132:135]\n"
  "s_waitcnt vmcnt(13)\n"
  "v_mfma_f32_16x16x32_bf16 v[128:131], v[114:117], a[184:187], v[128:131]\n"
  "v_mfma_f32_16x16x32_bf16 v[132:135], v[112:115], a[184:187], v[132:135]\n"
  "s_waitcnt vmcnt(12)\n"
  "v_mfma_f32_16x16x32_bf16 v[128:131], v[114:117], a[188:191], v[128:131]\n"
  "v_mfma_f32_16x16x32_bf16 v[132:135], v[112:115], a[188:191], v[132:135]\n"
  "s_waitcnt vmcnt(11)\n"
  "v_mfma_f32_16x16x32_bf16 v[136:139], v[98:101], a[192:195], 0\n"
  "v_mfma_f32_16x16x32_bf16 v[140:143], v[96:99], a[192:195], 0\n"
  "s_waitcnt vmcnt(10)\n"
  "v_mfma_f32_16x16x32_bf16 v[136:139], v[98:101], a[196:199], v[136:139]\n"
  "s_nop 1\n"
  "v_fmac_f32 v64, v104, v128\n"
  "v_fmac_f32 v65, v104, v129\n"
  "v_fmac_f32 v66, v104, v130\n"
  "v_mfma_f32_16x16x32_bf16 v[140:143], v[96:99], a[196:199], v[140:143]\n"
  "v_fmac_f32 v67, v104, v131\n"
  "v_fmac_f32 v68, v104, v132\n"
  "v_fmac_f32 v69, v104, v133\n"
  "s_waitcnt vmcnt(9)\n"
  "v_mfma_f32_16x16x32_bf16 v[136:139], v[114:117], a[200:203], v[136:139]\n"
  "v_fmac_f32 v70, v104, v134\n"
  "v_fmac_f32 v71, v104, v135\n"
  "v_cvt_pk_bf16_f32 v64, v64, v65\n"
  "v_mfma_f32_16x16x32_bf16 v[140:143], v[112:115], a[200:203], v[140:143]\n"
  "v_cvt_pk_bf16_f32 v65, v66, v67\n"
  "v_cvt_pk_bf16_f32 v66, v68, v69\n"
  "v_cvt_pk_bf16_f32 v67, v70, v71\n"
  "s_waitcnt vmcnt(8)\n"
  "v_mfma_f32_16x16x32_bf16 v[136:139], v[114:117], a[204:207], v[136:139]\n"
  "v_permlane16_swap_b32 v64, v66\n"
  "v_permlane16_swap_b32 v65, v67\n"
  "buffer_store_dwordx4 v[64:67], v102, s[20:23], s62 offen offset:0\n"
  "v_mfma_f32_16x16x32_bf16 v[140:143], v[112:115], a[204:207], v[140:143]\n"
  "s_waitcnt vmcnt(8)\n"
  "v_mfma_f32_16x16x32_bf16 v[144:147], v[98:101], a[208:211], 0\n"
  "v_mfma_f32_16x16x32_bf16 v[148:151], v[96:99], a[208:211], 0\n"
  "s_waitcnt vmcnt(7)\n"
  "v_mfma_f32_16x16x32_bf16 v[144:147], v[98:101], a[212:215], v[144:147]\n"
  "v_fmac_f32 v72, v104, v136\n"
  "v_fmac_f32 v73, v104, v137\n"
  "v_fmac_f32 v74, v104, v138\n"
  "v_mfma_f32_16x16x32_bf16 v[148:151], v[96:99], a[212:215], v[148:151]\n"
  "v_fmac_f32 v75, v104, v139\n"
  "v_fmac_f32 v76, v104, v140\n"
  "v_fmac_f32 v77, v104, v141\n"
  "s_waitcnt vmcnt(6)\n"
  "v_mfma_f32_16x16x32_bf16 v[144:147], v[114:117], a[216:219], v[144:147]\n"
  "v_fmac_f32 v78, v104, v142\n"
  "v_fmac_f32 v79, v104, v143\n"
  "v_cvt_pk_bf16_f32 v72, v72, v73\n"
  "v_mfma_f32_16x16x32_bf16 v[148:151], v[112:115], a[216:219], v[148:151]\n"
  "v_cvt_pk_bf16_f32 v73, v74, v75\n"
  "v_cvt_pk_bf16_f32 v74, v76, v77\n"
  "v_cvt_pk_bf16_f32 v75, v78, v79\n"
  "s_waitcnt vmcnt(5)\n"
  "v_mfma_f32_16x16x32_bf16 v[144:147], v[114:117], a[220:223], v[144:147]\n"
  "v_permlane16_swap_b32 v72, v74\n"
  "v_permlane16_swap_b32 v73, v75\n"
  "buffer_store_dwordx4 v[72:75], v102, s[20:23], s62 offen offset:64\n"
  "v_mfma_f32_16x16x32_bf16 v[148:151], v[112:115], a[220:223], v[148:151]\n"
  "s_waitcnt vmcnt(5)\n"
  "v_mfma_f32_16x16x32_bf16 v[152:155], v[98:101], a[224:227], 0\n"
  "v_mfma_f32_16x16x32_bf16 v[156:159], v[96:99], a[224:227], 0\n"
  "s_waitcnt vmcnt(4)\n"
  "v_mfma_f32_16x16x32_bf16 v[152:155], v[98:101], a[228:231], v[152:155]\n"
  "v_fmac_f32 v80, v108, v144\n"
  "v_fmac_f32 v81, v108, v145\n"
  "v_fmac_f32 v82, v108, v146\n"
  "v_mfma_f32_16x16x32_bf16 v[156:159], v[96:99], a[228:231], v[156:159]\n"
  "v_fmac_f32 v83, v108, v147\n"
  "v_fmac_f32 v84, v108, v148\n"
  "v_fmac_f32 v85, v108, v149\n"
  "s_waitcnt vmcnt(3)\n"
  "v_mfma_f32_16x16x32_bf16 v[152:155], v[114:117], a[232:235], v[152:155]\n"
  "v_fmac_f32 v86, v108, v150\n"
  "v_fmac_f32 v87, v108, v151\n"
  "v_cvt_pk_bf16_f32 v80, v80, v81\n"
  "v_mfma_f32_16x16x32_bf16 v[156:159], v[112:115], a[232:235], v[156:159]\n"
  "v_cvt_pk_bf16_f32 v81, v82, v83\n"
  "v_cvt_pk_bf16_f32 v82, v84, v85\n"
  "v_cvt_pk_bf16_f32 v83, v86, v87\n"
  "s_waitcnt vmcnt(2)\n"
  "v_mfma_f32_16x16x32_bf16 v[152:155], v[114:117], a[236:239], v[152:155]\n"
  "v_permlane16_swap_b32 v80, v82\n"
  "v_permlane16_swap_b32 v81, v83\n"
  "buffer_store_dwordx4 v[80:83], v102, s[20:23], s63 offen offset:0\n"
  "v_mfma_f32_16x16x32_bf16 v[156:159], v[112:115], a[236:239], v[156:159]\n"
  "s_nop 3\n"
  "v_fmac_f32 v88, v108, v152\n"
  "v_fmac_f32 v89, v108, v153\n"
  "v_fmac_f32 v90, v108, v154\n"
  "v_fmac_f32 v91, v108, v155\n"
  "v_fmac_f32 v92, v108, v156\n"
  "v_fmac_f32 v93, v108, v157\n"
  "v_fmac_f32 v94, v108, v158\n"
  "v_fmac_f32 v95, v108, v159\n"
  "v_cvt_pk_bf16_f32 v88, v88, v89\n"
  "v_cvt_pk_bf16_f32 v89, v90, v91\n"
  "v_cvt_pk_bf16_f32 v90, v92, v93\n"
  "v_cvt_pk_bf16_f32 v91, v94, v95\n"
  "s_nop 0\n"
  "v_permlane16_swap_b32 v88, v90\n"
  "v_permlane16_swap_b32 v89, v91\n"
  "buffer_store_dwordx4 v[88:91], v102, s[20:23], s63 offen offset:64\n"
  "s_endpgm\n"
  "FP8_%=:\n"
  "s_cbranch_execz FPE_%=\n"
  "SLP0_%=:\n"
  "s_add_u32 s65, s65, 1\n"
  "s_cmp_gt_u32 s65, 0x40000\n"
  "s_cbranch_scc1 BKP0_%=\n"
  "buffer_load_dwordx2 v[252:253], v249, s[16:19], s31 offen sc1\n"
  "s_waitcnt vmcnt(0)\n"
  "v_cmp_ne_u64 vcc, v[252:253], v[250:251]\n"
  "s_cbranch_vccz BKP0_%=\n"
  "s_branch SLP0_%=\n"
  "SLP1_%=:\n"
  "s_add_u32 s65, s65, 1\n"
  "s_cmp_gt_u32 s65, 0x40000\n"
  "s_cbranch_scc1 BKP1_%=\n"
  "buffer_load_dwordx2 v[252:253], v249, s[16:19], s31 offen sc1\n"
  "s_waitcnt vmcnt(0)\n"
  "v_cmp_ne_u64 vcc, v[252:253], v[250:251]\n"
  "s_cbranch_vccz BKP1_%=\n"
  "s_branch SLP1_%=\n"
  "SLP2_%=:\n"
  "s_add_u32 s65, s65, 1\n"
  "s_cmp_gt_u32 s65, 0x40000\n"
  "s_cbranch_scc1 BKP2_%=\n"
  "buffer_load_dwordx2 v[252:253], v249, s[16:19], s31 offen sc1\n"
  "s_waitcnt vmcnt(0)\n"
  "v_cmp_ne_u64 vcc, v[252:253], v[250:251]\n"
  "s_cbranch_vccz BKP2_%=\n"
  "s_branch SLP2_%=\n"
  "SLS_%=:\n"
  "s_add_u32 s65, s65, 1\n"
  "s_cmp_gt_u32 s65, 0x40000\n"
  "s_cbranch_scc1 BKS_%=\n"
  "buffer_load_dwordx2 v[252:253], v249, s[16:19], s31 offen sc1\n"
  "s_waitcnt vmcnt(0)\n"
  "v_cmp_ne_u64 vcc, v[252:253], v[250:251]\n"
  "s_cbranch_vccz BKS_%=\n"
  "s_branch SLS_%=\n"
  "SLE_%=:\n"
  "s_add_u32 s65, s65, 1\n"
  "s_cmp_gt_u32 s65, 0x40000\n"
  "s_cbranch_scc1 BKE_%=\n"
  "buffer_load_dwordx2 v[246:247], off, s[16:19], s93 sc1\n"
  "s_waitcnt vmcnt(0)\n"
  "v_cmp_ne_u64 vcc, v[246:247], v[250:251]\n"
  "s_cbranch_vccz BKE_%=\n"
  "s_branch SLE_%=\n"
  "SLY_%=:\n"
  "s_add_u32 s65, s65, 1\n"
  "s_cmp_gt_u32 s65, 0x40000\n"
  "s_cbranch_scc1 BKY_%=\n"
  "buffer_load_dwordx2 v[252:253], v249, s[16:19], s72 offen sc1\n"
  "s_waitcnt vmcnt(0)\n"
  "v_cmp_ne_u64 vcc, v[252:253], v[250:251]\n"
  "s_cbranch_vccz BKY_%=\n"
  "s_branch SLY_%=\n"
  "FFNEND_%=:\n"
  :: "s"(__builtin_amdgcn_kernarg_segment_ptr()), "s"(blockIdx.x), "s"(w), "s"((unsigned)(size_t)lds)
  : "v0", "v1", "v2", "v3", "v4", "v5", "v6", "v7", "v8", "v9", "v10", "v11", "v12", "v13", "v14", "v15", "v16", "v17", "v18", "v19", "v20", "v21", "v22", "v23", "v24", "v25", "v26", "v27", "v28", "v29", "v30", "v31", "v32", "v33", "v34", "v35", "v36", "v37", "v38", "v39", "v40", "v41", "v42", "v43", "v44", "v45", "v46", "v47", "v48", "v49", "v50", "v51", "v52", "v53", "v54", "v55", "v56", "v57", "v58", "v59", "v60", "v61", "v62", "v63", "v64", "v65", "v66", "v67", "v68", "v69", "v70", "v71", "v72", "v73", "v74", "v75", "v76", "v77", "v78", "v79", "v80", "v81", "v82", "v83", "v84", "v85", "v86", "v87", "v88", "v89", "v90", "v91", "v92", "v93", "v94", "v95", "v96", "v97", "v98", "v99", "v100", "v101", "v102", "v103", "v104", "v105", "v106", "v107", "v108", "v109", "v110", "v111", "v112", "v113", "v114", "v115", "v116", "v117", "v118", "v119", "v120", "v121", "v122", "v123", "v124", "v125", "v126", "v127", "v128", "v129", "v130", "v131", "v132", "v133", "v134", "v135", "v136", "v137", "v138", "v139", "v140", "v141", "v142", "v143", "v144", "v145", "v146", "v147", "v148", "v149", "v150", "v151", "v152", "v153", "v154", "v155", "v156", "v157", "v158", "v159", "v160", "v161", "v162", "v163", "v164", "v165", "v166", "v167", "v168", "v169", "v170", "v171", "v172", "v173", "v174", "v175", "v176", "v177", "v178", "v179", "v180", "v181", "v182", "v183", "v184", "v185", "v186", "v187", "v188", "v189", "v190", "v191", "v192", "v193", "v194", "v195", "v196", "v197", "v198", "v199", "v200", "v201", "v202", "v203", "v204", "v205", "v206", "v207", "v208", "v209", "v210", "v211", "v212", "v213", "v214", "v215", "v216", "v217", "v218", "v219", "v220", "v221", "v222", "v223", "v224", "v225", "v226", "v227", "v228", "v229", "v230", "v231", "v232", "v233", "v234", "v235", "v236", "v237", "v238", "v239", "v240", "v241", "v242", "v243", "v244", "v245", "v246", "v247", "v248", "v249", "v250", "v251", "v252", "v253", "v254", "v255", "a0", "a1", "a2", "a3", "a4", "a5", "a6", "a7", "a8", "a9", "a10", "a11", "a12", "a13", "a14", "a15", "a16", "a17", "a18", "a19", "a20", "a21", "a22", "a23", "a24", "a25", "a26", "a27", "a28", "a29", "a30", "a31", "a32", "a33", "a34", "a35", "a36", "a37", "a38", "a39", "a40", "a41", "a42", "a43", "a44", "a45", "a46", "a47", "a48", "a49", "a50", "a51", "a52", "a53", "a54", "a55", "a56", "a57", "a58", "a59", "a60", "a61", "a62", "a63", "a64", "a65", "a66", "a67", "a68", "a69", "a70", "a71", "a72", "a73", "a74", "a75", "a76", "a77", "a78", "a79", "a80", "a81", "a82", "a83", "a84", "a85", "a86", "a87", "a88", "a89", "a90", "a91", "a92", "a93", "a94", "a95", "a96", "a97", "a98", "a99", "a100", "a101", "a102", "a103", "a104", "a105", "a106", "a107", "a108", "a109", "a110", "a111", "a112", "a113", "a114", "a115", "a116", "a117", "a118", "a119", "a120", "a121", "a122", "a123", "a124", "a125", "a126", "a127", "a128", "a129", "a130", "a131", "a132", "a133", "a134", "a135", "a136", "a137", "a138", "a139", "a140", "a141", "a142", "a143", "a144", "a145", "a146", "a147", "a148", "a149", "a150", "a151", "a152", "a153", "a154", "a155", "a156", "a157", "a158", "a159", "a160", "a161", "a162", "a163", "a164", "a165", "a166", "a167", "a168", "a169", "a170", "a171", "a172", "a173", "a174", "a175", "a176", "a177", "a178", "a179", "a180", "a181", "a182", "a183", "a184", "a185", "a186", "a187", "a188", "a189", "a190", "a191", "a192", "a193", "a194", "a195", "a196", "a197", "a198", "a199", "a200", "a201", "a202", "a203", "a204", "a205", "a206", "a207", "a208", "a209", "a210", "a211", "a212", "a213", "a214", "a215", "a216", "a217", "a218", "a219", "a220", "a221", "a222", "a223", "a224", "a225", "a226", "a227", "a228", "a229", "a230", "a231", "a232", "a233", "a234", "a235", "a236", "a237", "a238", "a239", "a240", "a241", "a242", "a243", "a244", "a245", "a246", "a247", "a248", "a249", "a250", "a251", "a252", "a253", "a254", "a255", "s2", "s3", "s4", "s5", "s6", "s7", "s8", "s9", "s10", "s11", "s12", "s13", "s14", "s15", "s16", "s17", "s18", "s19", "s20", "s21", "s22", "s23", "s24", "s25", "s26", "s27", "s28", "s29", "s30", "s31", "s34", "s35", "s36", "s37", "s38", "s39", "s40", "s41", "s42", "s43", "s44", "s45", "s46", "s47", "s48", "s49", "s50", "s51", "s52", "s53", "s54", "s55", "s56", "s57", "s58", "s59", "s60", "s61", "s62", "s63", "s64", "s65", "s66", "s67", "s68", "s69", "s70", "s71", "s72", "s73", "s74", "s75", "s76", "s77", "s78", "s79", "s80", "s81", "s82", "s83", "s84", "s85", "s86", "s87", "s88", "s89", "s90", "s91", "s92", "s93", "s94", "s95", "s96", "s97", "vcc", "scc", "memory");
}

extern "C" void kernel_launch(void* const* d_in, const int* in_sizes, int n_in, void* d_out, int out_size, void* d_ws, size_t ws_size, hipStream_t stream) {
  Args a;
  a.p[0] = d_in[0]; a.p[1] = d_in[1]; a.p[2] = d_in[2]; a.p[3] = d_in[3]; a.p[4] = d_in[4];
  a.p[5] = d_in[6]; a.p[6] = d_in[7]; a.p[7] = d_in[8]; a.p[8] = d_in[9];
  a.ws = d_ws; a.out = d_out;
  main_k<<<256, 256, 0, stream>>>(a);
}
```
